```python
import torch

def _softmax(x, axis=-1):
    return torch.softmax(x, dim=axis)
def _gelu(x):
    return torch.nn.functional.gelu(x, approximate="tanh")
def _silu(x):
    return torch.nn.functional.silu(x)
def _sigmoid(x):
    return torch.sigmoid(x)
def _layernorm(x, axis=-1, eps=1e-5):
    m = x.mean(dim=axis, keepdim=True)
    v = x.var(dim=axis, keepdim=True, unbiased=False)
    return (x - m) / torch.sqrt(v + eps)
def _scatter_add(tgt, idx, src, axis=0):
    return tgt.clone().index_add_(axis, idx, src)
def _segment_sum(src, seg, n):
    out = torch.zeros((n,) + src.shape[1:], dtype=src.dtype)
    return out.index_add_(0, seg, src)
def _pad(x, pw):
    flat = tuple(v for pair in reversed(pw) for v in pair)
    return torch.nn.functional.pad(x, flat)

kernel_name = 'recurrent_scan__mha_block_cached__glu_gated_geglu__decode'

def setup_inputs():

    _w = (lambda a: (lambda t: t.to(torch.bfloat16) if t.is_floating_point() else t)(torch.as_tensor(a)))
    def _A_setup():
        B, Sq, D = 2, 1, 640
        H, Sk, Dh = 32, 16384, 128
        return {
            "x": _w((lambda *s: torch.randn(*s, dtype=torch.bfloat16) * 0.4)(B, Sq, D)),
            "k_cache": _w((lambda *s: torch.randn(*s, dtype=torch.bfloat16) * 1.6)(B, H, Sk, Dh)),
            "v_cache": _w((lambda *s: torch.randn(*s, dtype=torch.bfloat16) * 1.6)(B, H, Sk, Dh)),
            "wq": _w((lambda *s: torch.randn(*s, dtype=torch.bfloat16) * 1.6)(D, H * Dh) * (D ** -0.5)),
            "wo": _w((lambda *s: torch.randn(*s, dtype=torch.bfloat16) * 1.6)(H * Dh, D) * ((H * Dh) ** -0.5)),
        }
    _A_in = _A_setup()

    def _B_setup():
        B, S, Dm, Df = 2, 1, 640, 1728
        return {
            "x": _w((lambda *s: torch.randn(*s, dtype=torch.bfloat16) * 0.4)(B, S, Dm)),
            "w_gate": _w((lambda *s: torch.randn(*s, dtype=torch.bfloat16) * 1.6)(Dm, Df) * (Dm ** -0.5)),
            "w_up": _w((lambda *s: torch.randn(*s, dtype=torch.bfloat16) * 1.6)(Dm, Df) * (Dm ** -0.5)),
            "w_down": _w((lambda *s: torch.randn(*s, dtype=torch.bfloat16) * 1.6)(Df, Dm) * (Df ** -0.5)),
        }
    _B_in = _B_setup()

    return {
        "A_x": _A_in["x"],
        "A_k_cache": _A_in["k_cache"],
        "A_v_cache": _A_in["v_cache"],
        "A_wq": _A_in["wq"],
        "A_wo": _A_in["wo"],
        "B_x": _B_in["x"],
        "B_w_gate": _B_in["w_gate"],
        "B_w_up": _B_in["w_up"],
        "B_w_down": _B_in["w_down"],
    }

def reference(A_x, A_k_cache, A_v_cache, A_wq, A_wo, B_x, B_w_gate, B_w_up, B_w_down):
    def _A_ref(x, k_cache, v_cache, wq, wo):
        B, Sq, D = x.shape
        H, Dh = 32, 128
        q = (lambda x, *d: x.permute(*d))((x @ wq).reshape(B, Sq, H, Dh), 0, 2, 1, 3)
        scores = torch.einsum("bhqd,bhkd->bhqk", q, k_cache) * (Dh ** -0.5)
        p = _softmax(scores, axis=-1)
        o = torch.einsum("bhqk,bhkd->bhqd", p, v_cache)
        return (lambda x, *d: x.permute(*d))(o, 0, 2, 1, 3).reshape(B, Sq, H * Dh) @ wo

    def _B_ref(x, w_gate, w_up, w_down):
        g = torch.einsum("bsd,df->bsf", x, w_gate)
        u = torch.einsum("bsd,df->bsf", x, w_up)
        h = _gelu(g) * u
        return torch.einsum("bsf,fd->bsd", h, w_down)

    x = A_x
    xn = _layernorm(x, axis=-1)
    s = _A_ref(xn, A_k_cache, A_v_cache, A_wq, A_wo)
    g = _silu(_B_ref(xn, B_w_gate, B_w_up, B_w_down))
    return x + s * g
```

```cpp
#include <hip/hip_runtime.h>
#include <hip/hip_bf16.h>
#include <stdint.h>

constexpr int B = 2, D = 640, H = 32, SK = 16384, DH = 128, DF = 1728;
constexpr int NSPLIT = 8;
typedef unsigned short u16;
using u32x4 = __attribute__((ext_vector_type(4))) unsigned;
using bf16x2 = __attribute__((ext_vector_type(2))) __bf16;

__device__ __forceinline__ float bf2f(u16 v) { return __uint_as_float((unsigned)v << 16); }
__device__ __forceinline__ float rbf(float f) { return bf2f(__builtin_bit_cast(u16, (__bf16)f)); }
__device__ __forceinline__ float lo(unsigned v) { return __uint_as_float(v << 16); }
__device__ __forceinline__ float hi(unsigned v) { return __uint_as_float(v & 0xffff0000u); }

__device__ void ln_rows(const u16* x, float* xn, float* red) {
  const int tid = threadIdx.x, lane = tid & 63, w = tid >> 6;
  if (w < 2) {
    float v[10]; float s = 0.f;
    for (int i = 0; i < 10; ++i) { v[i] = bf2f(x[w * D + lane + 64 * i]); s += v[i]; }
    for (int m = 32; m >= 1; m >>= 1) s += __shfl_xor(s, m);
    const float mean = s * (1.f / D);
    float q = 0.f;
    for (int i = 0; i < 10; ++i) { float d = v[i] - mean; q += d * d; }
    for (int m = 32; m >= 1; m >>= 1) q += __shfl_xor(q, m);
    const float var = rbf(q * (1.f / D));
    const float mb = rbf(mean);
    const float den = rbf(sqrtf(rbf(var + 1e-5f)));
    for (int i = 0; i < 10; ++i) xn[w * D + lane + 64 * i] = rbf(rbf(v[i] - mb) / den);
  }
  __syncthreads();
}

__device__ void gemv64(const float* xin, int K, const u16* W, int ldw, int col0, float* red, float* res) {
  const int tid = threadIdx.x, cg = tid & 7, rg = tid >> 3;
  float a0[8] = {}, a1[8] = {};
  for (int k = rg; k < K; k += 32) {
    u32x4 w = *reinterpret_cast<const u32x4*>(W + (size_t)k * ldw + col0 + cg * 8);
    const float x0 = xin[k], x1 = xin[K + k];
    for (int j = 0; j < 4; ++j) {
      a0[2 * j] += x0 * lo(w[j]); a0[2 * j + 1] += x0 * hi(w[j]);
      a1[2 * j] += x1 * lo(w[j]); a1[2 * j + 1] += x1 * hi(w[j]);
    }
  }
  for (int j = 0; j < 8; ++j) { red[(rg * 2 + 0) * 64 + cg * 8 + j] = a0[j]; red[(rg * 2 + 1) * 64 + cg * 8 + j] = a1[j]; }
  __syncthreads();
  if (tid < 128) { const int b = tid >> 6, c = tid & 63; float s = 0.f; for (int r = 0; r < 32; ++r) s += red[(r * 2 + b) * 64 + c]; res[b * 64 + c] = s; }
  __syncthreads();
}

__global__ __launch_bounds__(256) void k1(const u16* x, const u16* wq, const u16* wg, const u16* wu, float* q, float* h) {
  __shared__ float xn[2 * D], red[32 * 2 * 64], r0[128], r1[128];
  ln_rows(x, xn, red);
  const int j = blockIdx.x, tid = threadIdx.x;
  if (j < 64) {
    gemv64(xn, D, wq, H * DH, j * 64, red, r0);
    if (tid < 128) q[(tid >> 6) * (H * DH) + j * 64 + (tid & 63)] = rbf(r0[tid]);
  } else {
    const int c = j - 64;
    gemv64(xn, D, wg, DF, c * 64, red, r0);
    gemv64(xn, D, wu, DF, c * 64, red, r1);
    if (tid < 128) {
      const float g = rbf(r0[tid]), u = rbf(r1[tid]);
      const float t = tanhf(0.7978845608028654f * (g + 0.044715f * g * g * g));
      const float ge = rbf(0.5f * g * (1.f + t));
      h[(tid >> 6) * DF + c * 64 + (tid & 63)] = rbf(ge * u);
    }
  }
}

__global__ __launch_bounds__(256) void k2(const float* q, const u16* kc, const u16* vc, float* po, float* pml) {
  __shared__ float sm[4 * 4 * (DH + 2)];
  const int s = blockIdx.x, h = blockIdx.y, b = blockIdx.z, tid = threadIdx.x, lane = tid & 63, w = tid >> 6, r = lane >> 4, c = lane & 15;
  float qf[8];
  for (int j = 0; j < 8; ++j) qf[j] = q[b * (H * DH) + h * DH + 8 * c + j];
  const size_t base = ((size_t)(b * H + h) * SK) * DH;
  const int per = SK / NSPLIT, k0 = s * per;
  float m = -INFINITY, l = 0.f, acc[8] = {};
  for (int k = k0 + w * 4 + r; k < k0 + per; k += 16) {
    const u32x4 kv = __builtin_nontemporal_load(reinterpret_cast<const u32x4*>(kc + base + (size_t)k * DH + 8 * c));
    const u32x4 vv = __builtin_nontemporal_load(reinterpret_cast<const u32x4*>(vc + base + (size_t)k * DH + 8 * c));
    float d = 0.f;
    for (int j = 0; j < 4; ++j) { d += lo(kv[j]) * qf[2 * j]; d += hi(kv[j]) * qf[2 * j + 1]; }
    for (int mm = 8; mm >= 1; mm >>= 1) d += __shfl_xor(d, mm);
    d *= 0.08838834764831845f;
    const float mn = fmaxf(m, d), al = __expf(m - mn), p = __expf(d - mn);
    l = l * al + p; m = mn;
    for (int j = 0; j < 4; ++j) { acc[2 * j] = acc[2 * j] * al + p * lo(vv[j]); acc[2 * j + 1] = acc[2 * j + 1] * al + p * hi(vv[j]); }
  }
  float* rec = sm + (w * 4 + r) * (DH + 2);
  for (int j = 0; j < 8; ++j) rec[2 + 8 * c + j] = acc[j];
  if (c == 0) { rec[0] = m; rec[1] = l; }
  __syncthreads();
  if (tid < DH) {
    float M = -INFINITY;
    for (int i = 0; i < 16; ++i) M = fmaxf(M, sm[i * (DH + 2)]);
    float L = 0.f, o = 0.f;
    for (int i = 0; i < 16; ++i) { const float e = __expf(sm[i * (DH + 2)] - M); L += e * sm[i * (DH + 2) + 1]; o += e * sm[i * (DH + 2) + 2 + tid]; }
    const int slot = (b * H + h) * NSPLIT + s;
    po[slot * DH + tid] = o;
    if (tid == 0) { pml[slot * 2] = M; pml[slot * 2 + 1] = L; }
  }
}

__global__ __launch_bounds__(256) void k3(const u16* x, const float* po, const float* pml, const float* h, const u16* wo, const u16* wd, u16* out) {
  __shared__ float o[2 * H * DH], hh[2 * DF], red[32 * 2 * 64], r0[128], r1[128];
  const int tid = threadIdx.x;
  for (int i = tid; i < 2 * H * DH; i += 256) {
    const int bh = i / DH, d = i % DH;
    float M = -INFINITY;
    for (int s = 0; s < NSPLIT; ++s) M = fmaxf(M, pml[(bh * NSPLIT + s) * 2]);
    float L = 0.f, a = 0.f;
    for (int s = 0; s < NSPLIT; ++s) { const float e = __expf(pml[(bh * NSPLIT + s) * 2] - M); L += e * pml[(bh * NSPLIT + s) * 2 + 1]; a += e * po[(bh * NSPLIT + s) * DH + d]; }
    o[i] = rbf(a / L);
  }
  for (int i = tid; i < 2 * DF; i += 256) hh[i] = h[i];
  __syncthreads();
  const int c = blockIdx.x;
  gemv64(o, H * DH, wo, D, c * 64, red, r0);
  gemv64(hh, DF, wd, D, c * 64, red, r1);
  if (tid < 128) {
    const int b = tid >> 6, col = c * 64 + (tid & 63);
    const float s = rbf(r0[tid]), f = rbf(r1[tid]);
    const float g = rbf(f / (1.f + __expf(-f)));
    const float v = bf2f(x[b * D + col]) + rbf(s * g);
    out[b * D + col] = __builtin_bit_cast(u16, (__bf16)v);
  }
}

extern "C" void kernel_launch(void* const* d_in, const int* in_sizes, int n_in, void* d_out, int out_size, void* d_ws, size_t ws_size, hipStream_t stream) {
  const u16* x = (const u16*)d_in[0]; const u16* kc = (const u16*)d_in[1]; const u16* vc = (const u16*)d_in[2];
  const u16* wq = (const u16*)d_in[3]; const u16* wo = (const u16*)d_in[4];
  const u16* wg = (const u16*)d_in[6]; const u16* wu = (const u16*)d_in[7]; const u16* wd = (const u16*)d_in[8];
  float* ws = (float*)d_ws;
  float* q = ws; float* h = ws + 8192; float* pml = ws + 16384; float* po = ws + 32768;
  k1<<<64 + 27, 256, 0, stream>>>(x, wq, wg, wu, q, h);
  k2<<<dim3(NSPLIT, H, B), 256, 0, stream>>>(q, kc, vc, po, pml);
  k3<<<10, 256, 0, stream>>>(x, po, pml, h, wo, wd, (u16*)d_out);
}
```

```cpp
#include <hip/hip_runtime.h>
#include <stdint.h>

#define SIF 3
#define SIFA 2
#define NE 268
#define LOCK 0
#define POLL0 1
#define POLL1 4
#define POLL2 8
#define KAUX 2
#define VAUX 2
#define DIAG 0
#define DELAYK 1
#define STAMPS 0

namespace {
constexpr int D = 640, H = 32, SK = 16384, DH = 128, DF = 1728;
constexpr int NWG = 256, NT = 512;

typedef unsigned u32;
typedef unsigned long long u64;
typedef unsigned short u16;
typedef u32 u32x2 __attribute__((ext_vector_type(2)));
typedef u32 u32x4 __attribute__((ext_vector_type(4)));
typedef float f32x2 __attribute__((ext_vector_type(2)));
typedef float f32x4 __attribute__((ext_vector_type(4)));
typedef __bf16 bf16x2 __attribute__((ext_vector_type(2)));
typedef __bf16 bf16x8 __attribute__((ext_vector_type(8)));
typedef __amdgpu_buffer_rsrc_t rsrc_t;
#define GAS __attribute__((address_space(1)))

constexpr int AUX_NT = 2, AUX_SC1 = 16;
constexpr u32 OOB = 0x80000000u;

constexpr u32 WS_EPOCH = 0;
constexpr u32 WS_QP = 4096;
constexpr u32 WS_F1 = WS_QP + NWG * 2048;
constexpr u32 WS_H1 = WS_F1 + NWG * 2048;
constexpr u32 H1_STRIDE = 144 * 8;
constexpr u32 WS_H2 = WS_H1 + NWG * H1_STRIDE;
constexpr u32 H2_STRIDE = 320 * 8;
constexpr u32 WS_STAMP = WS_H2 + NWG * H2_STRIDE;
constexpr u32 WS_BYTES = WS_STAMP + NWG * 8 * 16 * 4;

constexpr int L_Q = 0;
constexpr int L_REC = L_Q + 64;
constexpr int L_F1 = L_REC + 8 * 132;
constexpr int L_R1 = L_F1 + 8 * 256;
constexpr int L_SR = L_R1 + 8 * 132;
constexpr int L_FR = L_SR + 8 * 256;
constexpr int L_END = L_FR + 8 * 256;

constexpr float LOG2E = 1.4426950408889634f;
constexpr float SCALE = 0.08838834764831845f;
constexpr u32 SPIN_CAP = 1u << 18;

__device__ __forceinline__ float lo16(u32 v) { return __uint_as_float(v << 16); }
__device__ __forceinline__ float hi16(u32 v) { return __uint_as_float(v & 0xffff0000u); }
__device__ __forceinline__ u32 pk2(float a, float b) { bf16x2 r = {(__bf16)a, (__bf16)b}; return __builtin_bit_cast(u32, r); }
__device__ __forceinline__ float rbf(float a) { return lo16(pk2(a, a)); }
__device__ __forceinline__ float dot2(u32 a, u32 b, float c) { return __builtin_amdgcn_fdot2_f32_bf16(__builtin_bit_cast(bf16x2, a), __builtin_bit_cast(bf16x2, b), c, false); }
template <int CTRL> __device__ __forceinline__ float dppf(float x) { return __builtin_bit_cast(float, __builtin_amdgcn_mov_dpp(__builtin_bit_cast(int, x), CTRL, 0xf, 0xf, true)); }
template <int CTRL> __device__ __forceinline__ u32 dppu(u32 x) { return (u32)__builtin_amdgcn_mov_dpp((int)x, CTRL, 0xf, 0xf, true); }
constexpr int XOR1 = 0xB1, XOR2 = 0x4E, HMIR = 0x141, ROR4 = 0x124, ROR8 = 0x128, NEWB = 0x150;
__device__ __forceinline__ float xrow_max(float x) {
  auto s = __builtin_amdgcn_permlane16_swap(__float_as_uint(x), __float_as_uint(x), false, false);
  x = fmaxf(__uint_as_float(s[0]), __uint_as_float(s[1]));
  auto t = __builtin_amdgcn_permlane32_swap(__float_as_uint(x), __float_as_uint(x), false, false);
  return fmaxf(__uint_as_float(t[0]), __uint_as_float(t[1]));
}
__device__ __forceinline__ float xrow_sum(float x) {
  auto s = __builtin_amdgcn_permlane16_swap(__float_as_uint(x), __float_as_uint(x), false, false);
  x = __uint_as_float(s[0]) + __uint_as_float(s[1]);
  auto t = __builtin_amdgcn_permlane32_swap(__float_as_uint(x), __float_as_uint(x), false, false);
  return __uint_as_float(t[0]) + __uint_as_float(t[1]);
}
__device__ __forceinline__ f32x4 mfma(u32x4 a, u32x4 b, f32x4 c) {
  return __builtin_amdgcn_mfma_f32_16x16x32_bf16(__builtin_bit_cast(bf16x8, a), __builtin_bit_cast(bf16x8, b), c, 0, 0, 0);
}
__device__ __forceinline__ rsrc_t mk(const void* p, u32 bytes) { return __builtin_amdgcn_make_buffer_rsrc((void*)p, 0, bytes, 0x00020000); }
template <int AUX> __device__ __forceinline__ u32x4 ld128(rsrc_t r, u32 voff, u32 soff) { return __builtin_amdgcn_raw_buffer_load_b128(r, voff, soff, AUX); }
template <int AUX> __device__ __forceinline__ u32x2 ld64(rsrc_t r, u32 voff, u32 soff) { return __builtin_amdgcn_raw_buffer_load_b64(r, voff, soff, AUX); }
__device__ __forceinline__ void st_granule(rsrc_t r, u32 off, float v, u32 tag) { __builtin_amdgcn_raw_buffer_store_b64(u32x2{__float_as_uint(v), tag}, r, off, 0, AUX_SC1); }
__device__ __forceinline__ void st_granule2(rsrc_t r, u32 off, float v0, float v1, u32 tag) { __builtin_amdgcn_raw_buffer_store_b128(u32x4{__float_as_uint(v0), tag, __float_as_uint(v1), tag}, r, off, 0, AUX_SC1); }
#define SB() __builtin_amdgcn_sched_barrier(0)
#define STAMP(k) do { } while (0)

struct Params {
  const u16 *x, *kc, *vc, *wq, *wo, *wg, *wu, *wd;
  u16* out;
  unsigned char* ws;
};

struct Sel { u32 k0, k1, k2, k3; };
__device__ __forceinline__ u32x4 sel(u32 pp, const Sel& s) { return u32x4{pp & s.k0, pp & s.k1, pp & s.k2, pp & s.k3}; }

__global__ void __launch_bounds__(NT, 2) fused_decode(Params p) {
  __shared__ __attribute__((aligned(16))) float lds[L_END];
  const int tid = threadIdx.x, lane = tid & 63, w = __builtin_amdgcn_readfirstlane(tid >> 6);
  const int g = lane >> 4, c = lane & 15, bb = c >> 3;
  const int bid = blockIdx.x, xc = bid & 7, jj = bid >> 3;
  const int par = xc & 1, h = (xc >> 1) + 4 * (jj >> 2), b = (jj >> 1) & 1, half = jj & 1;
  const int j = par + 2 * half + 4 * b;
  const int pid = h * 8 + j;
  const bool ffn = h < DF / 64;
  STAMP(0);
  const rsrc_t rws = mk(p.ws, WS_BYTES);
  const u32 tag = *(const __attribute__((address_space(4))) u32*)(p.ws + WS_EPOCH) + 0x9E3779B9u;

  const bool proj = w == 0 || (w == 1 && ffn);
  u32x2 xs[5], xo[5];
  u32x4 wr[20];
  if (proj) {
    const rsrc_t rx = mk(p.x, 2 * D * 2);
#pragma unroll
    for (int i = 0; i < 5; ++i) xs[i] = ld64<0>(rx, (bb * D + 20 * (8 * g + (c & 7))) * 2 + i * 8, 0);
#pragma unroll
    for (int i = 0; i < 5; ++i) xo[i] = ld64<0>(rx, (bb * D + 80 * j + 20 * g) * 2 + i * 8, 0);
    const int ld = w == 0 ? H * DH : DF;
    const GAS u16* src = w == 0 ? (const GAS u16*)p.wq + h * DH + c * 8 : (const GAS u16*)(c < 8 ? p.wg : p.wu) + h * 64 + (c & 7) * 8;
    src += (80 * j + 20 * g) * ld;
#pragma unroll
    for (int i = 0; i < 20; ++i) wr[i] = __builtin_nontemporal_load((const GAS u32x4*)(src + i * ld));
  }
  SB();
  __builtin_amdgcn_s_barrier();
  SB();

  const size_t slab = ((size_t)(b * H + h) * SK + half * (SK / 2)) * DH;
  const rsrc_t rk = mk(p.kc + slab, SK / 2 * DH * 2), rv = mk(p.vc + slab, SK / 2 * DH * 2);
  const int blk0 = par ? NE : 0, blk1 = par ? 512 : NE;
  const int nst = (blk1 - blk0 - w + 7) >> 3;
  const u32 kvbase = (blk0 + w) * 4096u;
  const u32 vo16 = lane * 16;
  u32x4 KR[SIF][4], VR[SIF][4];
  auto fill = [&](int s, int step) {
    const u32 so = step < nst ? kvbase + step * 32768u : OOB;
#pragma unroll
    for (int i = 0; i < 4; ++i) KR[s][i] = ld128<KAUX>(rk, vo16 + i * 1024, so);
#pragma unroll
    for (int i = 0; i < 4; ++i) VR[s][i] = ld128<VAUX>(rv, vo16 + i * 1024, so);
    SB();
  };
  Sel sl;
  {
    const int mm = lane & 7;
    const u32 hm = (mm & 1) ? 0xffff0000u : 0x0000ffffu;
    sl.k0 = (mm >> 1) == 0 ? hm : 0u; sl.k1 = (mm >> 1) == 1 ? hm : 0u; sl.k2 = (mm >> 1) == 2 ? hm : 0u; sl.k3 = (mm >> 1) == 3 ? hm : 0u;
  }
#pragma unroll
  for (int s = 0; s < SIFA; ++s) fill(s, s);
  if (proj) {
    float xv[20];
#pragma unroll
    for (int i = 0; i < 5; ++i) { xv[4 * i] = lo16(xs[i][0]); xv[4 * i + 1] = hi16(xs[i][0]); xv[4 * i + 2] = lo16(xs[i][1]); xv[4 * i + 3] = hi16(xs[i][1]); }
    float s = 0.f;
#pragma unroll
    for (int i = 0; i < 20; ++i) s += xv[i];
    s += dppf<XOR1>(s); s += dppf<XOR2>(s); s += dppf<HMIR>(s); s = xrow_sum(s);
    const float mean = s * (1.f / D);
    float q2 = 0.f;
#pragma unroll
    for (int i = 0; i < 20; ++i) { const float d = xv[i] - mean; q2 = fmaf(d, d, q2); }
    q2 += dppf<XOR1>(q2); q2 += dppf<XOR2>(q2); q2 += dppf<HMIR>(q2); q2 = xrow_sum(q2);
    const float mean_b = rbf(mean);
    const float rden = 1.f / rbf(__builtin_sqrtf(rbf(rbf(q2 * (1.f / D)) + 1e-5f)));
    STAMP(8);
    auto xnorm = [&](float x) -> u32 { const float t = rbf(x - mean_b) * rden; return pk2(t, t); };
    f32x4 a = {0.f, 0.f, 0.f, 0.f};
#pragma unroll
    for (int i = 0; i < 5; ++i) {
      a = mfma(sel(xnorm(lo16(xo[i][0])), sl), wr[4 * i], a);
      a = mfma(sel(xnorm(hi16(xo[i][0])), sl), wr[4 * i + 1], a);
      a = mfma(sel(xnorm(lo16(xo[i][1])), sl), wr[4 * i + 2], a);
      a = mfma(sel(xnorm(hi16(xo[i][1])), sl), wr[4 * i + 3], a);
    }
    const u32 o = (w == 0 ? WS_QP : WS_F1) + pid * 2048 + lane * 16;
    st_granule2(rws, o, a[0], a[1], tag);
    st_granule2(rws, o + 1024, a[2], a[3], tag);
    STAMP(10);
  }
#pragma unroll
  for (int s = SIFA; s < SIF; ++s) fill(s, s);
  if (w == 2) {
    const u32 o = WS_QP + h * 8 * 2048 + (lane >> 5) * 1024 + (32 * b + (lane & 31)) * 16;
    f32x2 q = {0.f, 0.f};
    u32x4 a[8]; u32 need = 0xff;
    for (u32 spins = 0; need && spins < SPIN_CAP; ++spins) {
      asm volatile("" ::: "memory");
#pragma unroll
      for (int pp = 0; pp < 8; ++pp) if (need >> pp & 1) a[pp] = ld128<AUX_SC1>(rws, o + pp * 2048, 0);
#pragma unroll
      for (int pp = 0; pp < 8; ++pp) if ((need >> pp & 1) && __all(a[pp][1] == tag && a[pp][3] == tag)) need &= ~(1u << pp);
      if (need) __builtin_amdgcn_s_sleep(POLL0);
    }
#pragma unroll
    for (int pp = 0; pp < 8; ++pp) { q[0] += __uint_as_float(a[pp][0]); q[1] += __uint_as_float(a[pp][2]); }
    ((u32*)lds)[L_Q + 4 * (lane & 15) + 2 * ((lane >> 4) & 1) + (lane >> 5)] = pk2(q[0], q[1]);
    STAMP(9);
  }
  u32x4 wor[4], wdr[2];
  {
    const rsrc_t ro = mk(p.wo, H * DH * D * 2), rd = mk(p.wd, DF * D * 2);
    const u32 vo = c < 10 ? (128 * h + 16 * w + 4 * g) * (D * 2) + j * 160 + c * 16 : OOB;
    const u32 vd = (ffn && c < 10) ? (64 * h + 8 * w + 2 * g) * (D * 2) + j * 160 + c * 16 : OOB;
#pragma unroll
    for (int i = 0; i < 4; ++i) wor[i] = ld128<AUX_NT>(ro, vo, i * (D * 2));
#pragma unroll
    for (int i = 0; i < 2; ++i) wdr[i] = ld128<AUX_NT>(rd, vd, i * (D * 2));
  }
  SB();
  __syncthreads();
  STAMP(1);
  const u32x4 qv = *(const u32x4*)&lds[L_Q + 4 * c];

  float m = -INFINITY, l = 0.f;
  f32x4 acc = {0.f, 0.f, 0.f, 0.f};
  const bool c3 = (lane & 8) != 0, c2 = (lane & 4) != 0;
  auto consume = [&](int s, int step) {
    float xd[4];
#pragma unroll
    for (int t = 0; t < 4; ++t) {
      float d = dot2(KR[s][t][0], qv[0], 0.f);
      d = dot2(KR[s][t][1], qv[1], d); d = dot2(KR[s][t][2], qv[2], d); xd[t] = dot2(KR[s][t][3], qv[3], d);
    }
    const float y0 = (c3 ? xd[2] : xd[0]) + dppf<ROR8>(c3 ? xd[0] : xd[2]);
    const float y1 = (c3 ? xd[3] : xd[1]) + dppf<ROR8>(c3 ? xd[1] : xd[3]);
    float sc = (c2 ? y1 : y0) + dppf<HMIR>(c2 ? y0 : y1);
    sc += dppf<XOR1>(sc); sc += dppf<XOR2>(sc);
    sc = rbf(rbf(sc) * SCALE);
    sc = step < nst ? sc : -INFINITY;
    float pm = fmaxf(sc, dppf<ROR4>(sc)); pm = fmaxf(pm, dppf<ROR8>(pm)); pm = xrow_max(pm);
    const float mn = fmaxf(m, pm);
    const float al = __builtin_amdgcn_exp2f((m - mn) * LOG2E), pr = __builtin_amdgcn_exp2f((sc - mn) * LOG2E);
    m = mn; l = fmaf(l, al, pr);
    acc *= al;
    const u32 pp = pk2(pr, pr);
    acc = mfma(sel(dppu<NEWB + 0>(pp), sl), VR[s][0], acc);
    acc = mfma(sel(dppu<NEWB + 4>(pp), sl), VR[s][1], acc);
    acc = mfma(sel(dppu<NEWB + 8>(pp), sl), VR[s][2], acc);
    acc = mfma(sel(dppu<NEWB + 12>(pp), sl), VR[s][3], acc);
  };
  auto turn = [&](int t) {
#pragma unroll
    for (int s = 0; s < SIF; ++s) { consume(s, t * SIF + s); SB(); fill(s, (t + 1) * SIF + s); }
  };
  const int nturn = (((blk1 - blk0 + 7) >> 3) + SIF - 1) / SIF;
#pragma unroll 1
  for (int t = 0; t < nturn - 1; ++t) turn(t);
  const u32 of1 = ffn ? WS_F1 + (h * 8 + w) * 2048 + lane * 16 : OOB;
  u32x4 f0 = ld128<AUX_SC1>(rws, of1, 0), f1 = ld128<AUX_SC1>(rws, of1, 1024);
  SB();
  turn(nturn - 1);
  STAMP(3);
  if (ffn && !__all(f0[1] == tag && f0[3] == tag && f1[1] == tag && f1[3] == tag)) {
    for (u32 spins = 0; spins < SPIN_CAP; ++spins) {
      __builtin_amdgcn_s_sleep(8);
      asm volatile("" ::: "memory");
      f0 = ld128<AUX_SC1>(rws, of1, 0); f1 = ld128<AUX_SC1>(rws, of1, 1024);
      if (__all(f0[1] == tag && f0[3] == tag && f1[1] == tag && f1[3] == tag)) break;
    }
  }

  {
    float ls = l; ls += dppf<XOR1>(ls); ls += dppf<XOR2>(ls); ls += dppf<ROR4>(ls); ls += dppf<ROR8>(ls); ls = xrow_sum(ls) * 0.25f;
    float* rec = &lds[L_REC + w * 132];
    if (lane < 32) *(f32x4*)&rec[4 + 8 * c + 4 * g] = acc;
    if (lane == 0) { rec[0] = m; rec[1] = ls; }
    u32* fb = (u32*)&lds[L_F1 + w * 256 + (g >> 1) * 128 + 8 * c + 4 * (g & 1)];
    *(u32x4*)fb = u32x4{f0[0], f0[2], f1[0], f1[2]};
  }
  __syncthreads();
  STAMP(4);
  if (tid < 130) {
    float M = -INFINITY;
#pragma unroll
    for (int ww = 0; ww < 8; ++ww) M = fmaxf(M, lds[L_REC + ww * 132]);
    float v = 0.f;
    const int idx = tid < 128 ? 4 + tid : 1;
#pragma unroll
    for (int ww = 0; ww < 8; ++ww) v = fmaf(__builtin_amdgcn_exp2f((lds[L_REC + ww * 132] - M) * LOG2E), lds[L_REC + ww * 132 + idx], v);
    if (tid == 128) v = M;
    st_granule(rws, WS_H1 + pid * H1_STRIDE + tid * 8, v, tag);
  }
  {
    f32x4 af = {0.f, 0.f, 0.f, 0.f};
#pragma unroll
    for (int i = 0; i < 2; ++i) {
      float gs = 0.f, us = 0.f;
#pragma unroll
      for (int pp = 0; pp < 8; ++pp) { gs += lds[L_F1 + pp * 256 + bb * 128 + 8 * w + 2 * g + i]; us += lds[L_F1 + pp * 256 + bb * 128 + 64 + 8 * w + 2 * g + i]; }
      const float gg = rbf(gs), uu = rbf(us);
      const float z = 0.7978845608028654f * (gg + 0.044715f * gg * gg * gg);
      const float th = 1.f - 2.f / (__builtin_amdgcn_exp2f(z * (2.f * LOG2E)) + 1.f);
      const float hv = rbf(0.5f * gg * (1.f + th)) * uu;
      af = mfma(sel(pk2(hv, hv), sl), wdr[i], af);
    }
    *(f32x4*)&lds[L_FR + w * 256 + (g >> 1) * 128 + 8 * c + 4 * (g & 1)] = af;
  }
  {
    const u32 o1 = WS_H1 + (h * 8 + w) * H1_STRIDE;
    u32x4 a, ml; u32 spins = 0;
    for (;;) {
      asm volatile("" ::: "memory");
      ml = ld128<AUX_SC1>(rws, o1 + 1024, 0);
      if (ml[1] == tag && ml[3] == tag) {
        asm volatile("" ::: "memory");
        a = ld128<AUX_SC1>(rws, o1 + lane * 16, 0);
        if (__all(a[1] == tag && a[3] == tag)) break;
      }
      if (++spins >= SPIN_CAP) break;
      __builtin_amdgcn_s_sleep(POLL1);
    }
    float* r1 = &lds[L_R1 + w * 132];
    *(u32x2*)&r1[4 + 2 * lane] = u32x2{a[0], a[2]};
    if (lane == 0) *(u32x2*)&r1[0] = u32x2{ml[0], ml[2]};
  }
  __syncthreads();
  STAMP(5);
  {
    const float* r1 = &lds[L_R1 + bb * 4 * 132];
    const float m0 = r1[0], m1 = r1[132], m2 = r1[264], m3 = r1[396];
    const float M = fmaxf(fmaxf(m0, m1), fmaxf(m2, m3));
    const float e0 = __builtin_amdgcn_exp2f((m0 - M) * LOG2E), e1 = __builtin_amdgcn_exp2f((m1 - M) * LOG2E), e2 = __builtin_amdgcn_exp2f((m2 - M) * LOG2E), e3 = __builtin_amdgcn_exp2f((m3 - M) * LOG2E);
    const float L = e0 * r1[1] + e1 * r1[133] + e2 * r1[265] + e3 * r1[397];
    const int d0 = 4 + 16 * w + 4 * g;
    f32x4 o = *(const f32x4*)&r1[d0] * e0 + *(const f32x4*)&r1[132 + d0] * e1 + *(const f32x4*)&r1[264 + d0] * e2 + *(const f32x4*)&r1[396 + d0] * e3;
    o *= 1.f / L;
    f32x4 as = {0.f, 0.f, 0.f, 0.f};
#pragma unroll
    for (int i = 0; i < 4; ++i) as = mfma(sel(pk2(o[i], o[i]), sl), wor[i], as);
    *(f32x4*)&lds[L_SR + w * 256 + (g >> 1) * 128 + 8 * c + 4 * (g & 1)] = as;
  }
  __syncthreads();
  if (tid < 320) {
    const int k = tid < 160 ? tid : tid - 160, bo = k >= 80, col = k - 80 * bo;
    const float* src = &lds[(tid < 160 ? L_SR : L_FR) + bo * 128 + col];
    float v = 0.f;
#pragma unroll
    for (int ww = 0; ww < 8; ++ww) v += src[ww * 256];
    st_granule(rws, WS_H2 + pid * H2_STRIDE + tid * 8, v, tag);
  }
  STAMP(6);
  if (w == 7) {
    const int hp = lane & 31;
    const u32 o2 = WS_H2 + (hp * 8 + j) * H2_STRIDE + ((lane >> 5) * 160 + 5 * h) * 8;
    const int bo = h >> 4, col = 80 * j + 5 * (h & 15);
    const u16 xraw = p.x[bo * D + col + (lane < 5 ? lane : 0)];
    u32x4 a = {0u, 0u, 0u, 0u}, a2 = a; u32x2 a3 = {0u, 0u}; u32 spins = 0;
    bool need = true;
    for (;;) {
      asm volatile("" ::: "memory");
      if (need) {
        a = ld128<AUX_SC1>(rws, o2, 0); a2 = ld128<AUX_SC1>(rws, o2 + 16, 0); a3 = ld64<AUX_SC1>(rws, o2 + 32, 0);
        need = !(a[1] == tag && a[3] == tag && a2[1] == tag && a2[3] == tag && a3[1] == tag);
      }
      if (!__any(need) || ++spins >= SPIN_CAP) break;
      __builtin_amdgcn_s_sleep(POLL2);
    }
    float v[5] = {__uint_as_float(a[0]), __uint_as_float(a[2]), __uint_as_float(a2[0]), __uint_as_float(a2[2]), __uint_as_float(a3[0])};
    float sv = 0.f, fv = 0.f;
#pragma unroll
    for (int k = 0; k < 5; ++k) {
      float t = v[k];
      t += dppf<XOR1>(t); t += dppf<XOR2>(t); t += dppf<ROR4>(t); t += dppf<ROR8>(t);
      auto s16 = __builtin_amdgcn_permlane16_swap(__float_as_uint(t), __float_as_uint(t), false, false);
      t = __uint_as_float(s16[0]) + __uint_as_float(s16[1]);
      const float S = __builtin_bit_cast(float, __builtin_amdgcn_readlane(__builtin_bit_cast(int, t), 0));
      const float F = __builtin_bit_cast(float, __builtin_amdgcn_readlane(__builtin_bit_cast(int, t), 32));
      if (lane == k) { sv = S; fv = F; }
    }
    if (lane < 5) {
      const float s = rbf(sv), f = rbf(fv);
      const float gt = rbf(f / (1.f + __builtin_amdgcn_exp2f(-f * LOG2E)));
      const float r = __uint_as_float((u32)xraw << 16) + rbf(s * gt);
      p.out[bo * D + col + lane] = (u16)(pk2(r, r) & 0xffffu);
    }
    if (bid == 0 && lane == 0) __builtin_amdgcn_raw_buffer_store_b32(tag, rws, WS_EPOCH, 0, AUX_SC1);
    STAMP(7);
  }
}
}

extern "C" void kernel_launch(void* const* d_in, const int* in_sizes, int n_in, void* d_out, int out_size, void* d_ws, size_t ws_size, hipStream_t stream) {
  Params p;
  p.x = (const u16*)d_in[0]; p.kc = (const u16*)d_in[1]; p.vc = (const u16*)d_in[2]; p.wq = (const u16*)d_in[3]; p.wo = (const u16*)d_in[4];
  p.wg = (const u16*)d_in[6]; p.wu = (const u16*)d_in[7]; p.wd = (const u16*)d_in[8];
  p.out = (u16*)d_out; p.ws = (unsigned char*)d_ws;
  fused_decode<<<NWG, NT, 0, stream>>>(p);
}
```

```cpp
#include <hip/hip_runtime.h>
#include <stdint.h>

#define SIF 3
#define SIFA 2
#define NE 268
#define LOCK 0
#define POLL0 1
#define POLL1 4
#define POLL2 8
#define KAUX 2
#define VAUX 2
#define DIAG 3
#define DELAYK 10
#define STAMPS 0

namespace {
constexpr int D = 640, H = 32, SK = 16384, DH = 128, DF = 1728;
constexpr int NWG = 256, NT = 512;

typedef unsigned u32;
typedef unsigned long long u64;
typedef unsigned short u16;
typedef u32 u32x2 __attribute__((ext_vector_type(2)));
typedef u32 u32x4 __attribute__((ext_vector_type(4)));
typedef float f32x2 __attribute__((ext_vector_type(2)));
typedef float f32x4 __attribute__((ext_vector_type(4)));
typedef __bf16 bf16x2 __attribute__((ext_vector_type(2)));
typedef __bf16 bf16x8 __attribute__((ext_vector_type(8)));
typedef __amdgpu_buffer_rsrc_t rsrc_t;
#define GAS __attribute__((address_space(1)))

constexpr int AUX_NT = 2, AUX_SC1 = 16;
constexpr u32 OOB = 0x80000000u;

constexpr u32 WS_EPOCH = 0;
constexpr u32 WS_QP = 4096;
constexpr u32 WS_F1 = WS_QP + NWG * 2048;
constexpr u32 WS_H1 = WS_F1 + NWG * 2048;
constexpr u32 H1_STRIDE = 144 * 8;
constexpr u32 WS_H2 = WS_H1 + NWG * H1_STRIDE;
constexpr u32 H2_STRIDE = 320 * 8;
constexpr u32 WS_STAMP = WS_H2 + NWG * H2_STRIDE;
constexpr u32 WS_DIAG = WS_STAMP + NWG * 8 * 16 * 4;
constexpr u32 WS_BYTES = WS_DIAG + NWG * 16;

constexpr int L_Q = 0;
constexpr int L_REC = L_Q + 64;
constexpr int L_F1 = L_REC + 8 * 132;
constexpr int L_R1 = L_F1 + 8 * 256;
constexpr int L_SR = L_R1 + 8 * 132;
constexpr int L_FR = L_SR + 8 * 256;
constexpr int L_END = L_FR + 8 * 256;

constexpr float LOG2E = 1.4426950408889634f;
constexpr float SCALE = 0.08838834764831845f;
constexpr u32 SPIN_CAP = 1u << 18;

__device__ __forceinline__ float lo16(u32 v) { return __uint_as_float(v << 16); }
__device__ __forceinline__ float hi16(u32 v) { return __uint_as_float(v & 0xffff0000u); }
__device__ __forceinline__ u32 pk2(float a, float b) { bf16x2 r = {(__bf16)a, (__bf16)b}; return __builtin_bit_cast(u32, r); }
__device__ __forceinline__ float rbf(float a) { return lo16(pk2(a, a)); }
__device__ __forceinline__ float dot2(u32 a, u32 b, float c) { return __builtin_amdgcn_fdot2_f32_bf16(__builtin_bit_cast(bf16x2, a), __builtin_bit_cast(bf16x2, b), c, false); }
template <int CTRL> __device__ __forceinline__ float dppf(float x) { return __builtin_bit_cast(float, __builtin_amdgcn_mov_dpp(__builtin_bit_cast(int, x), CTRL, 0xf, 0xf, true)); }
template <int CTRL> __device__ __forceinline__ u32 dppu(u32 x) { return (u32)__builtin_amdgcn_mov_dpp((int)x, CTRL, 0xf, 0xf, true); }
constexpr int XOR1 = 0xB1, XOR2 = 0x4E, HMIR = 0x141, ROR4 = 0x124, ROR8 = 0x128, NEWB = 0x150;
__device__ __forceinline__ float xrow_max(float x) {
  auto s = __builtin_amdgcn_permlane16_swap(__float_as_uint(x), __float_as_uint(x), false, false);
  x = fmaxf(__uint_as_float(s[0]), __uint_as_float(s[1]));
  auto t = __builtin_amdgcn_permlane32_swap(__float_as_uint(x), __float_as_uint(x), false, false);
  return fmaxf(__uint_as_float(t[0]), __uint_as_float(t[1]));
}
__device__ __forceinline__ float xrow_sum(float x) {
  auto s = __builtin_amdgcn_permlane16_swap(__float_as_uint(x), __float_as_uint(x), false, false);
  x = __uint_as_float(s[0]) + __uint_as_float(s[1]);
  auto t = __builtin_amdgcn_permlane32_swap(__float_as_uint(x), __float_as_uint(x), false, false);
  return __uint_as_float(t[0]) + __uint_as_float(t[1]);
}
__device__ __forceinline__ f32x4 mfma(u32x4 a, u32x4 b, f32x4 c) {
  return __builtin_amdgcn_mfma_f32_16x16x32_bf16(__builtin_bit_cast(bf16x8, a), __builtin_bit_cast(bf16x8, b), c, 0, 0, 0);
}
__device__ __forceinline__ rsrc_t mk(const void* p, u32 bytes) { return __builtin_amdgcn_make_buffer_rsrc((void*)p, 0, bytes, 0x00020000); }
template <int AUX> __device__ __forceinline__ u32x4 ld128(rsrc_t r, u32 voff, u32 soff) { return __builtin_amdgcn_raw_buffer_load_b128(r, voff, soff, AUX); }
template <int AUX> __device__ __forceinline__ u32x2 ld64(rsrc_t r, u32 voff, u32 soff) { return __builtin_amdgcn_raw_buffer_load_b64(r, voff, soff, AUX); }
__device__ __forceinline__ void st_granule(rsrc_t r, u32 off, float v, u32 tag) { __builtin_amdgcn_raw_buffer_store_b64(u32x2{__float_as_uint(v), tag}, r, off, 0, AUX_SC1); }
__device__ __forceinline__ void st_granule2(rsrc_t r, u32 off, float v0, float v1, u32 tag) { __builtin_amdgcn_raw_buffer_store_b128(u32x4{__float_as_uint(v0), tag, __float_as_uint(v1), tag}, r, off, 0, AUX_SC1); }
#define SB() __builtin_amdgcn_sched_barrier(0)
#define STAMP(k) do { } while (0)

struct Params {
  const u16 *x, *kc, *vc, *wq, *wo, *wg, *wu, *wd;
  u16* out;
  unsigned char* ws;
};

struct Sel { u32 k0, k1, k2, k3; };
__device__ __forceinline__ u32x4 sel(u32 pp, const Sel& s) { return u32x4{pp & s.k0, pp & s.k1, pp & s.k2, pp & s.k3}; }

__global__ void __launch_bounds__(NT, 2) fused_decode(Params p) {
  __shared__ __attribute__((aligned(16))) float lds[L_END];
  const int tid = threadIdx.x, lane = tid & 63, w = __builtin_amdgcn_readfirstlane(tid >> 6);
  const int g = lane >> 4, c = lane & 15, bb = c >> 3;
  const int bid = blockIdx.x, xc = bid & 7, jj = bid >> 3;
  const int par = xc & 1, h = (xc >> 1) + 4 * (jj >> 2), b = (jj >> 1) & 1, half = jj & 1;
  const int j = par + 2 * half + 4 * b;
  const int pid = h * 8 + j;
  const bool ffn = h < DF / 64;
  STAMP(0);
  const rsrc_t rws = mk(p.ws, WS_BYTES);
  const u32 dgs = (u32)__builtin_amdgcn_s_memrealtime();
  if (tid == 448) { __builtin_amdgcn_raw_buffer_store_b64(u32x2{dgs, (u32)__builtin_amdgcn_s_getreg(20 | (3 << 11)) & 0xfu}, rws, WS_DIAG + bid * 16, 0, AUX_SC1); }
  const u32 tag = *(const __attribute__((address_space(4))) u32*)(p.ws + WS_EPOCH) + 0x9E3779B9u;

  const bool proj = w == 0 || (w == 1 && ffn);
  u32x2 xs[5], xo[5];
  u32x4 wr[20];
  if (proj) {
    const rsrc_t rx = mk(p.x, 2 * D * 2);
#pragma unroll
    for (int i = 0; i < 5; ++i) xs[i] = ld64<0>(rx, (bb * D + 20 * (8 * g + (c & 7))) * 2 + i * 8, 0);
#pragma unroll
    for (int i = 0; i < 5; ++i) xo[i] = ld64<0>(rx, (bb * D + 80 * j + 20 * g) * 2 + i * 8, 0);
    const int ld = w == 0 ? H * DH : DF;
    const GAS u16* src = w == 0 ? (const GAS u16*)p.wq + h * DH + c * 8 : (const GAS u16*)(c < 8 ? p.wg : p.wu) + h * 64 + (c & 7) * 8;
    src += (80 * j + 20 * g) * ld;
#pragma unroll
    for (int i = 0; i < 20; ++i) wr[i] = __builtin_nontemporal_load((const GAS u32x4*)(src + i * ld));
  }
  SB();
  __builtin_amdgcn_s_barrier();
  SB();

  const size_t slab = ((size_t)(b * H + h) * SK + half * (SK / 2)) * DH;
  const rsrc_t rk = mk(p.kc + slab, SK / 2 * DH * 2), rv = mk(p.vc + slab, SK / 2 * DH * 2);
  const int blk0 = par ? NE : 0, blk1 = par ? 512 : NE;
  const int nst = (blk1 - blk0 - w + 7) >> 3;
  const u32 kvbase = (blk0 + w) * 4096u;
  const u32 vo16 = lane * 16;
  u32x4 KR[SIF][4], VR[SIF][4];
  auto fill = [&](int s, int step) {
    const u32 so = step < nst ? kvbase + step * 32768u : OOB;
#pragma unroll
    for (int i = 0; i < 4; ++i) KR[s][i] = ld128<KAUX>(rk, vo16 + i * 1024, so);
#pragma unroll
    for (int i = 0; i < 4; ++i) VR[s][i] = ld128<VAUX>(rv, vo16 + i * 1024, so);
    SB();
  };
  Sel sl;
  {
    const int mm = lane & 7;
    const u32 hm = (mm & 1) ? 0xffff0000u : 0x0000ffffu;
    sl.k0 = (mm >> 1) == 0 ? hm : 0u; sl.k1 = (mm >> 1) == 1 ? hm : 0u; sl.k2 = (mm >> 1) == 2 ? hm : 0u; sl.k3 = (mm >> 1) == 3 ? hm : 0u;
  }
#pragma unroll
  for (int s = 0; s < SIFA; ++s) fill(s, s);
  if (proj) {
    float xv[20];
#pragma unroll
    for (int i = 0; i < 5; ++i) { xv[4 * i] = lo16(xs[i][0]); xv[4 * i + 1] = hi16(xs[i][0]); xv[4 * i + 2] = lo16(xs[i][1]); xv[4 * i + 3] = hi16(xs[i][1]); }
    float s = 0.f;
#pragma unroll
    for (int i = 0; i < 20; ++i) s += xv[i];
    s += dppf<XOR1>(s); s += dppf<XOR2>(s); s += dppf<HMIR>(s); s = xrow_sum(s);
    const float mean = s * (1.f / D);
    float q2 = 0.f;
#pragma unroll
    for (int i = 0; i < 20; ++i) { const float d = xv[i] - mean; q2 = fmaf(d, d, q2); }
    q2 += dppf<XOR1>(q2); q2 += dppf<XOR2>(q2); q2 += dppf<HMIR>(q2); q2 = xrow_sum(q2);
    const float mean_b = rbf(mean);
    const float rden = 1.f / rbf(__builtin_sqrtf(rbf(rbf(q2 * (1.f / D)) + 1e-5f)));
    STAMP(8);
    auto xnorm = [&](float x) -> u32 { const float t = rbf(x - mean_b) * rden; return pk2(t, t); };
    f32x4 a = {0.f, 0.f, 0.f, 0.f};
#pragma unroll
    for (int i = 0; i < 5; ++i) {
      a = mfma(sel(xnorm(lo16(xo[i][0])), sl), wr[4 * i], a);
      a = mfma(sel(xnorm(hi16(xo[i][0])), sl), wr[4 * i + 1], a);
      a = mfma(sel(xnorm(lo16(xo[i][1])), sl), wr[4 * i + 2], a);
      a = mfma(sel(xnorm(hi16(xo[i][1])), sl), wr[4 * i + 3], a);
    }
    const u32 o = (w == 0 ? WS_QP : WS_F1) + pid * 2048 + lane * 16;
    st_granule2(rws, o, a[0], a[1], tag);
    st_granule2(rws, o + 1024, a[2], a[3], tag);
    STAMP(10);
  }
#pragma unroll
  for (int s = SIFA; s < SIF; ++s) fill(s, s);
  if (w == 2) {
    const u32 o = WS_QP + h * 8 * 2048 + (lane >> 5) * 1024 + (32 * b + (lane & 31)) * 16;
    f32x2 q = {0.f, 0.f};
    u32x4 a[8]; u32 need = 0xff;
    for (u32 spins = 0; need && spins < SPIN_CAP; ++spins) {
      asm volatile("" ::: "memory");
#pragma unroll
      for (int pp = 0; pp < 8; ++pp) if (need >> pp & 1) a[pp] = ld128<AUX_SC1>(rws, o + pp * 2048, 0);
#pragma unroll
      for (int pp = 0; pp < 8; ++pp) if ((need >> pp & 1) && __all(a[pp][1] == tag && a[pp][3] == tag)) need &= ~(1u << pp);
      if (need) __builtin_amdgcn_s_sleep(POLL0);
    }
#pragma unroll
    for (int pp = 0; pp < 8; ++pp) { q[0] += __uint_as_float(a[pp][0]); q[1] += __uint_as_float(a[pp][2]); }
    ((u32*)lds)[L_Q + 4 * (lane & 15) + 2 * ((lane >> 4) & 1) + (lane >> 5)] = pk2(q[0], q[1]);
    STAMP(9);
  }
  u32x4 wor[4], wdr[2];
  {
    const rsrc_t ro = mk(p.wo, H * DH * D * 2), rd = mk(p.wd, DF * D * 2);
    const u32 vo = c < 10 ? (128 * h + 16 * w + 4 * g) * (D * 2) + j * 160 + c * 16 : OOB;
    const u32 vd = (ffn && c < 10) ? (64 * h + 8 * w + 2 * g) * (D * 2) + j * 160 + c * 16 : OOB;
#pragma unroll
    for (int i = 0; i < 4; ++i) wor[i] = ld128<AUX_NT>(ro, vo, i * (D * 2));
#pragma unroll
    for (int i = 0; i < 2; ++i) wdr[i] = ld128<AUX_NT>(rd, vd, i * (D * 2));
  }
  SB();
  __syncthreads();
  STAMP(1);
  const u32x4 qv = *(const u32x4*)&lds[L_Q + 4 * c];

  float m = -INFINITY, l = 0.f;
  f32x4 acc = {0.f, 0.f, 0.f, 0.f};
  const bool c3 = (lane & 8) != 0, c2 = (lane & 4) != 0;
  auto consume = [&](int s, int step) {
    float xd[4];
#pragma unroll
    for (int t = 0; t < 4; ++t) {
      float d = dot2(KR[s][t][0], qv[0], 0.f);
      d = dot2(KR[s][t][1], qv[1], d); d = dot2(KR[s][t][2], qv[2], d); xd[t] = dot2(KR[s][t][3], qv[3], d);
    }
    const float y0 = (c3 ? xd[2] : xd[0]) + dppf<ROR8>(c3 ? xd[0] : xd[2]);
    const float y1 = (c3 ? xd[3] : xd[1]) + dppf<ROR8>(c3 ? xd[1] : xd[3]);
    float sc = (c2 ? y1 : y0) + dppf<HMIR>(c2 ? y0 : y1);
    sc += dppf<XOR1>(sc); sc += dppf<XOR2>(sc);
    sc = rbf(rbf(sc) * SCALE);
    sc = step < nst ? sc : -INFINITY;
    float pm = fmaxf(sc, dppf<ROR4>(sc)); pm = fmaxf(pm, dppf<ROR8>(pm)); pm = xrow_max(pm);
    const float mn = fmaxf(m, pm);
    const float al = __builtin_amdgcn_exp2f((m - mn) * LOG2E), pr = __builtin_amdgcn_exp2f((sc - mn) * LOG2E);
    m = mn; l = fmaf(l, al, pr);
    acc *= al;
    const u32 pp = pk2(pr, pr);
    acc = mfma(sel(dppu<NEWB + 0>(pp), sl), VR[s][0], acc);
    acc = mfma(sel(dppu<NEWB + 4>(pp), sl), VR[s][1], acc);
    acc = mfma(sel(dppu<NEWB + 8>(pp), sl), VR[s][2], acc);
    acc = mfma(sel(dppu<NEWB + 12>(pp), sl), VR[s][3], acc);
  };
  auto turn = [&](int t) {
#pragma unroll
    for (int s = 0; s < SIF; ++s) { consume(s, t * SIF + s); SB(); fill(s, (t + 1) * SIF + s); }
  };
  const int nturn = (((blk1 - blk0 + 7) >> 3) + SIF - 1) / SIF;
#pragma unroll 1
  for (int t = 0; t < nturn - 1; ++t) turn(t);
  const u32 of1 = ffn ? WS_F1 + (h * 8 + w) * 2048 + lane * 16 : OOB;
  u32x4 f0 = ld128<AUX_SC1>(rws, of1, 0), f1 = ld128<AUX_SC1>(rws, of1, 1024);
  SB();
  turn(nturn - 1);
  STAMP(3);
  if (ffn && !__all(f0[1] == tag && f0[3] == tag && f1[1] == tag && f1[3] == tag)) {
    for (u32 spins = 0; spins < SPIN_CAP; ++spins) {
      __builtin_amdgcn_s_sleep(8);
      asm volatile("" ::: "memory");
      f0 = ld128<AUX_SC1>(rws, of1, 0); f1 = ld128<AUX_SC1>(rws, of1, 1024);
      if (__all(f0[1] == tag && f0[3] == tag && f1[1] == tag && f1[3] == tag)) break;
    }
  }

  {
    float ls = l; ls += dppf<XOR1>(ls); ls += dppf<XOR2>(ls); ls += dppf<ROR4>(ls); ls += dppf<ROR8>(ls); ls = xrow_sum(ls) * 0.25f;
    float* rec = &lds[L_REC + w * 132];
    if (lane < 32) *(f32x4*)&rec[4 + 8 * c + 4 * g] = acc;
    if (lane == 0) { rec[0] = m; rec[1] = ls; }
    u32* fb = (u32*)&lds[L_F1 + w * 256 + (g >> 1) * 128 + 8 * c + 4 * (g & 1)];
    *(u32x4*)fb = u32x4{f0[0], f0[2], f1[0], f1[2]};
  }
  __syncthreads();
  STAMP(4);
  if (tid == 448) { __builtin_amdgcn_raw_buffer_store_b32((u32)__builtin_amdgcn_s_memrealtime(), rws, WS_DIAG + bid * 16 + 8, 0, AUX_SC1); }
  if (tid < 130) {
    float M = -INFINITY;
#pragma unroll
    for (int ww = 0; ww < 8; ++ww) M = fmaxf(M, lds[L_REC + ww * 132]);
    float v = 0.f;
    const int idx = tid < 128 ? 4 + tid : 1;
#pragma unroll
    for (int ww = 0; ww < 8; ++ww) v = fmaf(__builtin_amdgcn_exp2f((lds[L_REC + ww * 132] - M) * LOG2E), lds[L_REC + ww * 132 + idx], v);
    if (tid == 128) v = M;
    st_granule(rws, WS_H1 + pid * H1_STRIDE + tid * 8, v, tag);
  }
  {
    f32x4 af = {0.f, 0.f, 0.f, 0.f};
#pragma unroll
    for (int i = 0; i < 2; ++i) {
      float gs = 0.f, us = 0.f;
#pragma unroll
      for (int pp = 0; pp < 8; ++pp) { gs += lds[L_F1 + pp * 256 + bb * 128 + 8 * w + 2 * g + i]; us += lds[L_F1 + pp * 256 + bb * 128 + 64 + 8 * w + 2 * g + i]; }
      const float gg = rbf(gs), uu = rbf(us);
      const float z = 0.7978845608028654f * (gg + 0.044715f * gg * gg * gg);
      const float th = 1.f - 2.f / (__builtin_amdgcn_exp2f(z * (2.f * LOG2E)) + 1.f);
      const float hv = rbf(0.5f * gg * (1.f + th)) * uu;
      af = mfma(sel(pk2(hv, hv), sl), wdr[i], af);
    }
    *(f32x4*)&lds[L_FR + w * 256 + (g >> 1) * 128 + 8 * c + 4 * (g & 1)] = af;
  }
  {
    const u32 o1 = WS_H1 + (h * 8 + w) * H1_STRIDE;
    u32x4 a, ml; u32 spins = 0;
    for (;;) {
      asm volatile("" ::: "memory");
      ml = ld128<AUX_SC1>(rws, o1 + 1024, 0);
      if (ml[1] == tag && ml[3] == tag) {
        asm volatile("" ::: "memory");
        a = ld128<AUX_SC1>(rws, o1 + lane * 16, 0);
        if (__all(a[1] == tag && a[3] == tag)) break;
      }
      if (++spins >= SPIN_CAP) break;
      __builtin_amdgcn_s_sleep(POLL1);
    }
    float* r1 = &lds[L_R1 + w * 132];
    *(u32x2*)&r1[4 + 2 * lane] = u32x2{a[0], a[2]};
    if (lane == 0) *(u32x2*)&r1[0] = u32x2{ml[0], ml[2]};
  }
  __syncthreads();
  STAMP(5);
  {
    const float* r1 = &lds[L_R1 + bb * 4 * 132];
    const float m0 = r1[0], m1 = r1[132], m2 = r1[264], m3 = r1[396];
    const float M = fmaxf(fmaxf(m0, m1), fmaxf(m2, m3));
    const float e0 = __builtin_amdgcn_exp2f((m0 - M) * LOG2E), e1 = __builtin_amdgcn_exp2f((m1 - M) * LOG2E), e2 = __builtin_amdgcn_exp2f((m2 - M) * LOG2E), e3 = __builtin_amdgcn_exp2f((m3 - M) * LOG2E);
    const float L = e0 * r1[1] + e1 * r1[133] + e2 * r1[265] + e3 * r1[397];
    const int d0 = 4 + 16 * w + 4 * g;
    f32x4 o = *(const f32x4*)&r1[d0] * e0 + *(const f32x4*)&r1[132 + d0] * e1 + *(const f32x4*)&r1[264 + d0] * e2 + *(const f32x4*)&r1[396 + d0] * e3;
    o *= 1.f / L;
    f32x4 as = {0.f, 0.f, 0.f, 0.f};
#pragma unroll
    for (int i = 0; i < 4; ++i) as = mfma(sel(pk2(o[i], o[i]), sl), wor[i], as);
    *(f32x4*)&lds[L_SR + w * 256 + (g >> 1) * 128 + 8 * c + 4 * (g & 1)] = as;
  }
  __syncthreads();
  if (tid < 320) {
    const int k = tid < 160 ? tid : tid - 160, bo = k >= 80, col = k - 80 * bo;
    const float* src = &lds[(tid < 160 ? L_SR : L_FR) + bo * 128 + col];
    float v = 0.f;
#pragma unroll
    for (int ww = 0; ww < 8; ++ww) v += src[ww * 256];
    st_granule(rws, WS_H2 + pid * H2_STRIDE + tid * 8, v, tag);
  }
  STAMP(6);
  if (w == 7) {
    const int hp = lane & 31;
    const u32 o2 = WS_H2 + (hp * 8 + j) * H2_STRIDE + ((lane >> 5) * 160 + 5 * h) * 8;
    const int bo = h >> 4, col = 80 * j + 5 * (h & 15);
    const u16 xraw = p.x[bo * D + col + (lane < 5 ? lane : 0)];
    u32x4 a = {0u, 0u, 0u, 0u}, a2 = a; u32x2 a3 = {0u, 0u}; u32 spins = 0;
    bool need = true;
    for (;;) {
      asm volatile("" ::: "memory");
      if (need) {
        a = ld128<AUX_SC1>(rws, o2, 0); a2 = ld128<AUX_SC1>(rws, o2 + 16, 0); a3 = ld64<AUX_SC1>(rws, o2 + 32, 0);
        need = !(a[1] == tag && a[3] == tag && a2[1] == tag && a2[3] == tag && a3[1] == tag);
      }
      if (!__any(need) || ++spins >= SPIN_CAP) break;
      __builtin_amdgcn_s_sleep(POLL2);
    }
    float v[5] = {__uint_as_float(a[0]), __uint_as_float(a[2]), __uint_as_float(a2[0]), __uint_as_float(a2[2]), __uint_as_float(a3[0])};
    float sv = 0.f, fv = 0.f;
#pragma unroll
    for (int k = 0; k < 5; ++k) {
      float t = v[k];
      t += dppf<XOR1>(t); t += dppf<XOR2>(t); t += dppf<ROR4>(t); t += dppf<ROR8>(t);
      auto s16 = __builtin_amdgcn_permlane16_swap(__float_as_uint(t), __float_as_uint(t), false, false);
      t = __uint_as_float(s16[0]) + __uint_as_float(s16[1]);
      const float S = __builtin_bit_cast(float, __builtin_amdgcn_readlane(__builtin_bit_cast(int, t), 0));
      const float F = __builtin_bit_cast(float, __builtin_amdgcn_readlane(__builtin_bit_cast(int, t), 32));
      if (lane == k) { sv = S; fv = F; }
    }
    if (lane < 5) {
      const float s = rbf(sv), f = rbf(fv);
      const float gt = rbf(f / (1.f + __builtin_amdgcn_exp2f(-f * LOG2E)));
      const float r = __uint_as_float((u32)xraw << 16) + rbf(s * gt);
      p.out[bo * D + col + lane] = (u16)(pk2(r, r) & 0xffffu);
    }
    if (bid == 0 && lane == 0) __builtin_amdgcn_raw_buffer_store_b32(tag, rws, WS_EPOCH, 0, AUX_SC1);
    STAMP(7);
    if (bid == 0) {
      const u32 tend = (u32)__builtin_amdgcn_s_memrealtime();
      int mnS = 1 << 30, mxS = -(1 << 30), mnE = 1 << 30, mxE = -(1 << 30), mism = 0;
      for (int i = 0; i < 4; ++i) {
        const u32x4 e = ld128<AUX_SC1>(rws, WS_DIAG + (lane + 64 * i) * 16, 0);
        const int ds = (int)(e[0] - dgs), de = (int)(e[2] - dgs);
        mnS = min(mnS, ds); mxS = max(mxS, ds); mnE = min(mnE, de); mxE = max(mxE, de); mism += (e[1] != (u32)((lane + 64 * i) & 7));
      }
      for (int o = 32; o >= 1; o >>= 1) { mnS = min(mnS, __shfl_xor(mnS, o)); mxS = max(mxS, __shfl_xor(mxS, o)); mnE = min(mnE, __shfl_xor(mnE, o)); mxE = max(mxE, __shfl_xor(mxE, o)); mism += __shfl_xor(mism, o); }
      int meas = 0;
      if (DIAG == 1) meas = -mnS;
      if (DIAG == 2) meas = mxS - mnS;
      if (DIAG == 3) meas = mism;
      if (DIAG == 4) meas = mxE - mnS;
      if (DIAG == 5) meas = mnE - mnS;
      if (DIAG == 6) meas = (int)(tend - dgs) - mnS;
      const u32 t1 = (u32)__builtin_amdgcn_s_memrealtime();
      const u32 dl = (u32)max(meas, 0) * DELAYK;
      while ((u32)__builtin_amdgcn_s_memrealtime() - t1 < dl) __builtin_amdgcn_s_sleep(1);
    }
  }
}
}

extern "C" void kernel_launch(void* const* d_in, const int* in_sizes, int n_in, void* d_out, int out_size, void* d_ws, size_t ws_size, hipStream_t stream) {
  Params p;
  p.x = (const u16*)d_in[0]; p.kc = (const u16*)d_in[1]; p.vc = (const u16*)d_in[2]; p.wq = (const u16*)d_in[3]; p.wo = (const u16*)d_in[4];
  p.wg = (const u16*)d_in[6]; p.wu = (const u16*)d_in[7]; p.wd = (const u16*)d_in[8];
  p.out = (u16*)d_out; p.ws = (unsigned char*)d_ws;
  fused_decode<<<NWG, NT, 0, stream>>>(p);
}
```

```cpp
#include <hip/hip_runtime.h>
#include <stdint.h>

#define SIF 3
#define NE 268
#define LOCK 0
#define POLL0 1
#define POLL1 4
#define POLL2 8
#define KAUX 2
#define VAUX 2
#define PACE 4
#define QSLEEP 0
#define ABL 0
#define PROBE 1
#define STAMPS 0

namespace {
constexpr int D = 640, H = 32, SK = 16384, DH = 128, DF = 1728;
constexpr int NWG = 256, NT = 512;

typedef unsigned u32;
typedef unsigned long long u64;
typedef unsigned short u16;
typedef u32 u32x2 __attribute__((ext_vector_type(2)));
typedef u32 u32x4 __attribute__((ext_vector_type(4)));
typedef float f32x2 __attribute__((ext_vector_type(2)));
typedef float f32x4 __attribute__((ext_vector_type(4)));
typedef __bf16 bf16x2 __attribute__((ext_vector_type(2)));
typedef __bf16 bf16x8 __attribute__((ext_vector_type(8)));
typedef __amdgpu_buffer_rsrc_t rsrc_t;
#define GAS __attribute__((address_space(1)))

constexpr int AUX_NT = 2, AUX_SC1 = 16;
constexpr u32 OOB = 0x80000000u;

constexpr u32 WS_EPOCH = 0;
constexpr u32 WS_QP = 4096;
constexpr u32 WS_F1 = WS_QP + NWG * 2048;
constexpr u32 WS_H1 = WS_F1 + NWG * 2048;
constexpr u32 H1_STRIDE = 144 * 8;
constexpr u32 WS_H2 = WS_H1 + NWG * H1_STRIDE;
constexpr u32 H2_STRIDE = 320 * 8;
constexpr u32 WS_STAMP = WS_H2 + NWG * H2_STRIDE;
constexpr u32 WS_BYTES = WS_STAMP + NWG * 8 * 16 * 4;

constexpr int L_Q = 0;
constexpr int L_REC = L_Q + 64;
constexpr int L_F1 = L_REC + 8 * 132;
constexpr int L_R1 = L_F1 + 8 * 256;
constexpr int L_SR = L_R1 + 8 * 132;
constexpr int L_FR = L_SR + 8 * 256;
constexpr int L_END = L_FR + 8 * 256;

constexpr float LOG2E = 1.4426950408889634f;
constexpr float SCALE = 0.08838834764831845f;
constexpr u32 SPIN_CAP = 1u << 18;

__device__ __forceinline__ float lo16(u32 v) { return __uint_as_float(v << 16); }
__device__ __forceinline__ float hi16(u32 v) { return __uint_as_float(v & 0xffff0000u); }
__device__ __forceinline__ u32 pk2(float a, float b) { bf16x2 r = {(__bf16)a, (__bf16)b}; return __builtin_bit_cast(u32, r); }
__device__ __forceinline__ float rbf(float a) { return lo16(pk2(a, a)); }
__device__ __forceinline__ float dot2(u32 a, u32 b, float c) { return __builtin_amdgcn_fdot2_f32_bf16(__builtin_bit_cast(bf16x2, a), __builtin_bit_cast(bf16x2, b), c, false); }
template <int CTRL> __device__ __forceinline__ float dppf(float x) { return __builtin_bit_cast(float, __builtin_amdgcn_mov_dpp(__builtin_bit_cast(int, x), CTRL, 0xf, 0xf, true)); }
template <int CTRL> __device__ __forceinline__ u32 dppu(u32 x) { return (u32)__builtin_amdgcn_mov_dpp((int)x, CTRL, 0xf, 0xf, true); }
constexpr int XOR1 = 0xB1, XOR2 = 0x4E, HMIR = 0x141, ROR4 = 0x124, ROR8 = 0x128, NEWB = 0x150;
__device__ __forceinline__ float xrow_max(float x) {
  auto s = __builtin_amdgcn_permlane16_swap(__float_as_uint(x), __float_as_uint(x), false, false);
  x = fmaxf(__uint_as_float(s[0]), __uint_as_float(s[1]));
  auto t = __builtin_amdgcn_permlane32_swap(__float_as_uint(x), __float_as_uint(x), false, false);
  return fmaxf(__uint_as_float(t[0]), __uint_as_float(t[1]));
}
__device__ __forceinline__ float xrow_sum(float x) {
  auto s = __builtin_amdgcn_permlane16_swap(__float_as_uint(x), __float_as_uint(x), false, false);
  x = __uint_as_float(s[0]) + __uint_as_float(s[1]);
  auto t = __builtin_amdgcn_permlane32_swap(__float_as_uint(x), __float_as_uint(x), false, false);
  return __uint_as_float(t[0]) + __uint_as_float(t[1]);
}
__device__ __forceinline__ f32x4 mfma(u32x4 a, u32x4 b, f32x4 c) {
  return __builtin_amdgcn_mfma_f32_16x16x32_bf16(__builtin_bit_cast(bf16x8, a), __builtin_bit_cast(bf16x8, b), c, 0, 0, 0);
}
__device__ __forceinline__ rsrc_t mk(const void* p, u32 bytes) { return __builtin_amdgcn_make_buffer_rsrc((void*)p, 0, bytes, 0x00020000); }
template <int AUX> __device__ __forceinline__ u32x4 ld128(rsrc_t r, u32 voff, u32 soff) { return __builtin_amdgcn_raw_buffer_load_b128(r, voff, soff, AUX); }
template <int AUX> __device__ __forceinline__ u32x2 ld64(rsrc_t r, u32 voff, u32 soff) { return __builtin_amdgcn_raw_buffer_load_b64(r, voff, soff, AUX); }
__device__ __forceinline__ void st_granule(rsrc_t r, u32 off, float v, u32 tag) { __builtin_amdgcn_raw_buffer_store_b64(u32x2{__float_as_uint(v), tag}, r, off, 0, AUX_SC1); }
__device__ __forceinline__ void st_granule2(rsrc_t r, u32 off, float v0, float v1, u32 tag) { __builtin_amdgcn_raw_buffer_store_b128(u32x4{__float_as_uint(v0), tag, __float_as_uint(v1), tag}, r, off, 0, AUX_SC1); }
#define SB() __builtin_amdgcn_sched_barrier(0)
#define STAMP(k) do { } while (0)

struct Params {
  const u16 *x, *kc, *vc, *wq, *wo, *wg, *wu, *wd;
  u16* out;
  unsigned char* ws;
};

struct Sel { u32 k0, k1, k2, k3; };
__device__ __forceinline__ u32x4 sel(u32 pp, const Sel& s) { return u32x4{pp & s.k0, pp & s.k1, pp & s.k2, pp & s.k3}; }

__global__ void __launch_bounds__(NT, 2) fused_decode(Params p) {
  __shared__ __attribute__((aligned(16))) float lds[L_END];
  const int tid = threadIdx.x, lane = tid & 63, w = __builtin_amdgcn_readfirstlane(tid >> 6);
  const int g = lane >> 4, c = lane & 15, bb = c >> 3;
  const int bid = blockIdx.x, xc = bid & 7, jj = bid >> 3;
  const int par = xc & 1, h = (xc >> 1) + 4 * (jj >> 2), b = (jj >> 1) & 1, half = jj & 1;
  const int j = par + 2 * half + 4 * b;
  const int pid = h * 8 + j;
  const bool ffn = h < DF / 64;
  STAMP(0);
  const rsrc_t rws = mk(p.ws, WS_BYTES);
  u32 probe_ticks = 0;
  {
    const u64 tb = __builtin_amdgcn_s_memrealtime();
    if (bid == 0 && w == 7) {
      const unsigned char* base =
        PROBE == 1 ? (const unsigned char*)p.kc :
        PROBE == 2 ? (const unsigned char*)p.kc + (254u << 20) :
        PROBE == 3 ? (const unsigned char*)p.vc :
        PROBE == 4 ? (const unsigned char*)p.vc + (128u << 20) :
        PROBE == 5 ? (const unsigned char*)p.vc + (254u << 20) :
        PROBE == 6 ? p.ws + (8u << 20) :
        PROBE == 7 ? p.ws + (1020u << 20) :
        PROBE == 8 ? (const unsigned char*)p.kc + (128u << 20) :
        PROBE == 9 ? p.ws + (512u << 20) : (const unsigned char*)p.wo;
      const rsrc_t rp = mk(base, 2u << 20);
      u32 v = __builtin_amdgcn_raw_buffer_load_b32(rp, 0, 0, 2);
      v = __builtin_amdgcn_readfirstlane(v) & 0;
      asm volatile("" : "+s"(v));
      const u64 t0 = __builtin_amdgcn_s_memrealtime();
      for (int i = 1; i <= 32; ++i) { u32 x = __builtin_amdgcn_raw_buffer_load_b32(rp, v + i * 8448, 0, 2); v = __builtin_amdgcn_readfirstlane(x) & 0; asm volatile("" : "+s"(v)); }
      probe_ticks = (u32)(__builtin_amdgcn_s_memrealtime() - t0);
    }
    while (__builtin_amdgcn_s_memrealtime() - tb < 4000) __builtin_amdgcn_s_sleep(4);
  }
  const u32 tag = *(const __attribute__((address_space(4))) u32*)(p.ws + WS_EPOCH) + 0x9E3779B9u;

  const size_t slab = ((size_t)(b * H + h) * SK + half * (SK / 2)) * DH;
  const rsrc_t rk = mk(p.kc + slab, SK / 2 * DH * 2), rv = mk(p.vc + slab, SK / 2 * DH * 2);
  const int blk0 = par ? NE : 0, blk1 = par ? 512 : NE;
  const int nst = (blk1 - blk0 - w + 7) >> 3;
  const u32 kvbase = (blk0 + w) * 4096u;
  const u32 vo16 = lane * 16;
  u32x4 KR[SIF][4], VR[SIF][4];
  auto fillk = [&](int s, int step) __attribute__((always_inline)) {
    const u32 so = step < nst ? kvbase + step * 32768u : OOB;
#pragma unroll
    for (int i = 0; i < 4; ++i) KR[s][i] = ld128<KAUX>(rk, vo16 + i * 1024, so);
    SB();
  };
  auto fillv = [&](int s, int step) __attribute__((always_inline)) {
    const u32 so = step < nst ? kvbase + step * 32768u : OOB;
#pragma unroll
    for (int i = 0; i < 4; ++i) VR[s][i] = ld128<VAUX>(rv, vo16 + i * 1024, so);
    SB();
  };
  auto fill = [&](int s, int step) __attribute__((always_inline)) { fillk(s, step); fillv(s, step); };
  u32x4 wor[4], wdr[2];
  auto fillw = [&]() __attribute__((always_inline)) {
    const rsrc_t ro = mk(p.wo, H * DH * D * 2), rd = mk(p.wd, DF * D * 2);
    const u32 vo = (c < 10 && !(ABL & 2)) ? (128 * h + 16 * w + 4 * g) * (D * 2) + j * 160 + c * 16 : OOB;
    const u32 vd = (ffn && c < 10 && !(ABL & 2)) ? (64 * h + 8 * w + 2 * g) * (D * 2) + j * 160 + c * 16 : OOB;
#pragma unroll
    for (int i = 0; i < 4; ++i) wor[i] = ld128<AUX_NT>(ro, vo, i * (D * 2));
#pragma unroll
    for (int i = 0; i < 2; ++i) wdr[i] = ld128<AUX_NT>(rd, vd, i * (D * 2));
    SB();
  };
  Sel sl;
  {
    const int mm = lane & 7;
    const u32 hm = (mm & 1) ? 0xffff0000u : 0x0000ffffu;
    sl.k0 = (mm >> 1) == 0 ? hm : 0u; sl.k1 = (mm >> 1) == 1 ? hm : 0u; sl.k2 = (mm >> 1) == 2 ? hm : 0u; sl.k3 = (mm >> 1) == 3 ? hm : 0u;
  }

  if (!(ABL & 4) && (w == 0 || (w == 1 && ffn))) {
    u32x2 xs[5], xo[5];
    u32x4 wr[20];
    const rsrc_t rx = mk(p.x, 2 * D * 2);
#pragma unroll
    for (int i = 0; i < 5; ++i) xs[i] = ld64<0>(rx, (bb * D + 20 * (8 * g + (c & 7))) * 2 + i * 8, 0);
#pragma unroll
    for (int i = 0; i < 5; ++i) xo[i] = ld64<0>(rx, (bb * D + 80 * j + 20 * g) * 2 + i * 8, 0);
    const int ld = w == 0 ? H * DH : DF;
    const GAS u16* src = w == 0 ? (const GAS u16*)p.wq + h * DH + c * 8 : (const GAS u16*)(c < 8 ? p.wg : p.wu) + h * 64 + (c & 7) * 8;
    src += (80 * j + 20 * g) * ld;
#pragma unroll
    for (int i = 0; i < 20; ++i) wr[i] = __builtin_nontemporal_load((const GAS u32x4*)(src + i * ld));
    SB();
    __builtin_amdgcn_s_barrier();
    SB();
    float xv[20];
#pragma unroll
    for (int i = 0; i < 5; ++i) { xv[4 * i] = lo16(xs[i][0]); xv[4 * i + 1] = hi16(xs[i][0]); xv[4 * i + 2] = lo16(xs[i][1]); xv[4 * i + 3] = hi16(xs[i][1]); }
    float s = 0.f;
#pragma unroll
    for (int i = 0; i < 20; ++i) s += xv[i];
    s += dppf<XOR1>(s); s += dppf<XOR2>(s); s += dppf<HMIR>(s); s = xrow_sum(s);
    const float mean = s * (1.f / D);
    float q2 = 0.f;
#pragma unroll
    for (int i = 0; i < 20; ++i) { const float d = xv[i] - mean; q2 = fmaf(d, d, q2); }
    q2 += dppf<XOR1>(q2); q2 += dppf<XOR2>(q2); q2 += dppf<HMIR>(q2); q2 = xrow_sum(q2);
    const float mean_b = rbf(mean);
    const float rden = 1.f / rbf(__builtin_sqrtf(rbf(rbf(q2 * (1.f / D)) + 1e-5f)));
    STAMP(8);
    u32 xn[20];
#pragma unroll
    for (int i = 0; i < 5; ++i) {
      const float t0 = rbf(lo16(xo[i][0]) - mean_b) * rden, t1 = rbf(hi16(xo[i][0]) - mean_b) * rden, t2 = rbf(lo16(xo[i][1]) - mean_b) * rden, t3 = rbf(hi16(xo[i][1]) - mean_b) * rden;
      xn[4 * i] = pk2(t0, t0); xn[4 * i + 1] = pk2(t1, t1); xn[4 * i + 2] = pk2(t2, t2); xn[4 * i + 3] = pk2(t3, t3);
    }
    f32x4 a = {0.f, 0.f, 0.f, 0.f}, a2 = a;
#pragma unroll
    for (int i = 0; i < 20; i += 2) { a = mfma(sel(xn[i], sl), wr[i], a); a2 = mfma(sel(xn[i + 1], sl), wr[i + 1], a2); }
    a += a2;
    const u32 o = (w == 0 ? WS_QP : WS_F1) + pid * 2048 + lane * 16;
    st_granule2(rws, o, a[0], a[1], tag);
    st_granule2(rws, o + 1024, a[2], a[3], tag);
    STAMP(10);
    if (w == 0) {
      const u32 oq = WS_QP + h * 8 * 2048 + (lane >> 5) * 1024 + (32 * b + (lane & 31)) * 16;
      u32x4 qa[8];
      if (QSLEEP) __builtin_amdgcn_s_sleep(QSLEEP);
      for (u32 spins = 0; spins < SPIN_CAP; ++spins) {
        asm volatile("" ::: "memory");
#pragma unroll
        for (int pp = 0; pp < 8; ++pp) qa[pp] = ld128<AUX_SC1>(rws, oq + pp * 2048, 0);
        bool ok = true;
#pragma unroll
        for (int pp = 0; pp < 8; ++pp) ok = ok && qa[pp][1] == tag && qa[pp][3] == tag;
        if (__all(ok)) break;
        if (POLL0) __builtin_amdgcn_s_sleep(POLL0);
      }
      f32x2 q = {0.f, 0.f};
#pragma unroll
      for (int pp = 0; pp < 8; ++pp) { q[0] += __uint_as_float(qa[pp][0]); q[1] += __uint_as_float(qa[pp][2]); }
      ((u32*)lds)[L_Q + 4 * (lane & 15) + 2 * ((lane >> 4) & 1) + (lane >> 5)] = pk2(q[0], q[1]);
      STAMP(9);
      __syncthreads();
#pragma unroll
      for (int s = 0; s < SIF; ++s) fill(s, s);
      fillw();
    } else {
#pragma unroll
      for (int s = 0; s < SIF; ++s) fill(s, s);
      fillw();
      __syncthreads();
    }
  } else {
    __builtin_amdgcn_s_barrier();
    SB();
    fillk(0, 0); fillv(0, 0);
#pragma unroll
    for (int s = 1; s < SIF; ++s) {
      asm volatile("s_waitcnt vmcnt(%0)" ::"n"(PACE) : "memory"); fillk(s, s);
      asm volatile("s_waitcnt vmcnt(%0)" ::"n"(PACE) : "memory"); fillv(s, s);
    }
    fillw();
    __syncthreads();
  }
  STAMP(1);
  const u32x4 qv = *(const u32x4*)&lds[L_Q + 4 * c];

  float m = -INFINITY, l = 0.f;
  f32x4 acc = {0.f, 0.f, 0.f, 0.f};
  const bool c3 = (lane & 8) != 0, c2 = (lane & 4) != 0;
  auto consume = [&](int s, int step) __attribute__((always_inline)) {
    float xd[4];
#pragma unroll
    for (int t = 0; t < 4; ++t) {
      float d = dot2(KR[s][t][0], qv[0], 0.f);
      d = dot2(KR[s][t][1], qv[1], d); d = dot2(KR[s][t][2], qv[2], d); xd[t] = dot2(KR[s][t][3], qv[3], d);
    }
    const float y0 = (c3 ? xd[2] : xd[0]) + dppf<ROR8>(c3 ? xd[0] : xd[2]);
    const float y1 = (c3 ? xd[3] : xd[1]) + dppf<ROR8>(c3 ? xd[1] : xd[3]);
    float sc = (c2 ? y1 : y0) + dppf<HMIR>(c2 ? y0 : y1);
    sc += dppf<XOR1>(sc); sc += dppf<XOR2>(sc);
    sc = rbf(rbf(sc) * SCALE);
    sc = step < nst ? sc : -INFINITY;
    float pm = fmaxf(sc, dppf<ROR4>(sc)); pm = fmaxf(pm, dppf<ROR8>(pm)); pm = xrow_max(pm);
    const float mn = fmaxf(m, pm);
    const float al = __builtin_amdgcn_exp2f((m - mn) * LOG2E), pr = __builtin_amdgcn_exp2f((sc - mn) * LOG2E);
    m = mn; l = fmaf(l, al, pr);
    acc *= al;
    const u32 pp = pk2(pr, pr);
    acc = mfma(sel(dppu<NEWB + 0>(pp), sl), VR[s][0], acc);
    acc = mfma(sel(dppu<NEWB + 4>(pp), sl), VR[s][1], acc);
    acc = mfma(sel(dppu<NEWB + 8>(pp), sl), VR[s][2], acc);
    acc = mfma(sel(dppu<NEWB + 12>(pp), sl), VR[s][3], acc);
  };
  auto turn = [&](int t) __attribute__((always_inline)) {
#pragma unroll
    for (int s = 0; s < SIF; ++s) { consume(s, t * SIF + s); SB(); fill(s, (t + 1) * SIF + s); }
  };
  const int nturn = (((blk1 - blk0 + 7) >> 3) + SIF - 1) / SIF;
#pragma unroll 1
  for (int t = 0; t < nturn - 1; ++t) turn(t);
  const u32 of1 = ffn ? WS_F1 + (h * 8 + w) * 2048 + lane * 16 : OOB;
  u32x4 f0 = ld128<AUX_SC1>(rws, of1, 0), f1 = ld128<AUX_SC1>(rws, of1, 1024);
  SB();
  turn(nturn - 1);
  STAMP(3);
  if (ffn && !(ABL & 4) && !__all(f0[1] == tag && f0[3] == tag && f1[1] == tag && f1[3] == tag)) {
    for (u32 spins = 0; spins < SPIN_CAP; ++spins) {
      __builtin_amdgcn_s_sleep(8);
      asm volatile("" ::: "memory");
      f0 = ld128<AUX_SC1>(rws, of1, 0); f1 = ld128<AUX_SC1>(rws, of1, 1024);
      if (__all(f0[1] == tag && f0[3] == tag && f1[1] == tag && f1[3] == tag)) break;
    }
  }

  {
    float ls = l; ls += dppf<XOR1>(ls); ls += dppf<XOR2>(ls); ls += dppf<ROR4>(ls); ls += dppf<ROR8>(ls); ls = xrow_sum(ls) * 0.25f;
    float* rec = &lds[L_REC + w * 132];
    if (lane < 32) *(f32x4*)&rec[4 + 8 * c + 4 * g] = acc;
    if (lane == 0) { rec[0] = m; rec[1] = ls; }
    u32* fb = (u32*)&lds[L_F1 + w * 256 + (g >> 1) * 128 + 8 * c + 4 * (g & 1)];
    *(u32x4*)fb = u32x4{f0[0], f0[2], f1[0], f1[2]};
  }
  __syncthreads();
  STAMP(4);
  if (tid < 130) {
    float M = -INFINITY;
#pragma unroll
    for (int ww = 0; ww < 8; ++ww) M = fmaxf(M, lds[L_REC + ww * 132]);
    float v = 0.f;
    const int idx = tid < 128 ? 4 + tid : 1;
#pragma unroll
    for (int ww = 0; ww < 8; ++ww) v = fmaf(__builtin_amdgcn_exp2f((lds[L_REC + ww * 132] - M) * LOG2E), lds[L_REC + ww * 132 + idx], v);
    if (tid == 128) v = M;
    st_granule(rws, WS_H1 + pid * H1_STRIDE + tid * 8, v, tag);
  }
  {
    f32x4 af = {0.f, 0.f, 0.f, 0.f};
#pragma unroll
    for (int i = 0; i < 2; ++i) {
      float gs = 0.f, us = 0.f;
#pragma unroll
      for (int pp = 0; pp < 8; ++pp) { gs += lds[L_F1 + pp * 256 + bb * 128 + 8 * w + 2 * g + i]; us += lds[L_F1 + pp * 256 + bb * 128 + 64 + 8 * w + 2 * g + i]; }
      const float gg = rbf(gs), uu = rbf(us);
      const float z = 0.7978845608028654f * (gg + 0.044715f * gg * gg * gg);
      const float th = 1.f - 2.f / (__builtin_amdgcn_exp2f(z * (2.f * LOG2E)) + 1.f);
      const float hv = rbf(0.5f * gg * (1.f + th)) * uu;
      af = mfma(sel(pk2(hv, hv), sl), wdr[i], af);
    }
    *(f32x4*)&lds[L_FR + w * 256 + (g >> 1) * 128 + 8 * c + 4 * (g & 1)] = af;
  }
  {
    const u32 o1 = WS_H1 + (h * 8 + w) * H1_STRIDE;
    u32x4 a, ml; u32 spins = 0;
    for (;;) {
      asm volatile("" ::: "memory");
      ml = ld128<AUX_SC1>(rws, o1 + 1024, 0);
      if (ml[1] == tag && ml[3] == tag) {
        asm volatile("" ::: "memory");
        a = ld128<AUX_SC1>(rws, o1 + lane * 16, 0);
        if (__all(a[1] == tag && a[3] == tag)) break;
      }
      if (++spins >= SPIN_CAP) break;
      __builtin_amdgcn_s_sleep(POLL1);
    }
    float* r1 = &lds[L_R1 + w * 132];
    *(u32x2*)&r1[4 + 2 * lane] = u32x2{a[0], a[2]};
    if (lane == 0) *(u32x2*)&r1[0] = u32x2{ml[0], ml[2]};
  }
  __syncthreads();
  STAMP(5);
  {
    const float* r1 = &lds[L_R1 + bb * 4 * 132];
    const float m0 = r1[0], m1 = r1[132], m2 = r1[264], m3 = r1[396];
    const float M = fmaxf(fmaxf(m0, m1), fmaxf(m2, m3));
    const float e0 = __builtin_amdgcn_exp2f((m0 - M) * LOG2E), e1 = __builtin_amdgcn_exp2f((m1 - M) * LOG2E), e2 = __builtin_amdgcn_exp2f((m2 - M) * LOG2E), e3 = __builtin_amdgcn_exp2f((m3 - M) * LOG2E);
    const float L = e0 * r1[1] + e1 * r1[133] + e2 * r1[265] + e3 * r1[397];
    const int d0 = 4 + 16 * w + 4 * g;
    f32x4 o = *(const f32x4*)&r1[d0] * e0 + *(const f32x4*)&r1[132 + d0] * e1 + *(const f32x4*)&r1[264 + d0] * e2 + *(const f32x4*)&r1[396 + d0] * e3;
    o *= 1.f / L;
    f32x4 as = {0.f, 0.f, 0.f, 0.f};
#pragma unroll
    for (int i = 0; i < 4; ++i) as = mfma(sel(pk2(o[i], o[i]), sl), wor[i], as);
    *(f32x4*)&lds[L_SR + w * 256 + (g >> 1) * 128 + 8 * c + 4 * (g & 1)] = as;
  }
  __syncthreads();
  if (tid < 320) {
    const int k = tid < 160 ? tid : tid - 160, bo = k >= 80, col = k - 80 * bo;
    const float* src = &lds[(tid < 160 ? L_SR : L_FR) + bo * 128 + col];
    float v = 0.f;
#pragma unroll
    for (int ww = 0; ww < 8; ++ww) v += src[ww * 256];
    st_granule(rws, WS_H2 + pid * H2_STRIDE + tid * 8, v, tag);
  }
  STAMP(6);
  if (w == 7) {
    const int hp = lane & 31;
    const u32 o2 = WS_H2 + (hp * 8 + j) * H2_STRIDE + ((lane >> 5) * 160 + 5 * h) * 8;
    const int bo = h >> 4, col = 80 * j + 5 * (h & 15);
    const u16 xraw = p.x[bo * D + col + (lane < 5 ? lane : 0)];
    u32x4 a = {0u, 0u, 0u, 0u}, a2 = a; u32x2 a3 = {0u, 0u}; u32 spins = 0;
    bool need = true;
    for (;;) {
      asm volatile("" ::: "memory");
      if (need) {
        a = ld128<AUX_SC1>(rws, o2, 0); a2 = ld128<AUX_SC1>(rws, o2 + 16, 0); a3 = ld64<AUX_SC1>(rws, o2 + 32, 0);
        need = !(a[1] == tag && a[3] == tag && a2[1] == tag && a2[3] == tag && a3[1] == tag);
      }
      if (!__any(need) || ++spins >= SPIN_CAP) break;
      __builtin_amdgcn_s_sleep(POLL2);
    }
    float v[5] = {__uint_as_float(a[0]), __uint_as_float(a[2]), __uint_as_float(a2[0]), __uint_as_float(a2[2]), __uint_as_float(a3[0])};
    float sv = 0.f, fv = 0.f;
#pragma unroll
    for (int k = 0; k < 5; ++k) {
      float t = v[k];
      t += dppf<XOR1>(t); t += dppf<XOR2>(t); t += dppf<ROR4>(t); t += dppf<ROR8>(t);
      auto s16 = __builtin_amdgcn_permlane16_swap(__float_as_uint(t), __float_as_uint(t), false, false);
      t = __uint_as_float(s16[0]) + __uint_as_float(s16[1]);
      const float S = __builtin_bit_cast(float, __builtin_amdgcn_readlane(__builtin_bit_cast(int, t), 0));
      const float F = __builtin_bit_cast(float, __builtin_amdgcn_readlane(__builtin_bit_cast(int, t), 32));
      if (lane == k) { sv = S; fv = F; }
    }
    if (lane < 5) {
      const float s = rbf(sv), f = rbf(fv);
      const float gt = rbf(f / (1.f + __builtin_amdgcn_exp2f(-f * LOG2E)));
      const float r = __uint_as_float((u32)xraw << 16) + rbf(s * gt);
      p.out[bo * D + col + lane] = (u16)(pk2(r, r) & 0xffffu);
    }
    if (bid == 0 && lane == 0) __builtin_amdgcn_raw_buffer_store_b32(tag, rws, WS_EPOCH, 0, AUX_SC1);
    STAMP(7);
    if (bid == 0) { const u64 t1 = __builtin_amdgcn_s_memrealtime(); while (__builtin_amdgcn_s_memrealtime() - t1 < (u64)probe_ticks * 4) __builtin_amdgcn_s_sleep(1); }
  }
}
}

extern "C" void kernel_launch(void* const* d_in, const int* in_sizes, int n_in, void* d_out, int out_size, void* d_ws, size_t ws_size, hipStream_t stream) {
  Params p;
  p.x = (const u16*)d_in[0]; p.kc = (const u16*)d_in[1]; p.vc = (const u16*)d_in[2]; p.wq = (const u16*)d_in[3]; p.wo = (const u16*)d_in[4];
  p.wg = (const u16*)d_in[6]; p.wu = (const u16*)d_in[7]; p.wd = (const u16*)d_in[8];
  p.out = (u16*)d_out; p.ws = (unsigned char*)d_ws;
  fused_decode<<<NWG, NT, 0, stream>>>(p);
}
```

```cpp
#include <hip/hip_runtime.h>
#include <stdint.h>

#define SIF 3
#define NE 268
#define LOCK 0
#define POLL0 1
#define POLL1 4
#define POLL2 8
#define KAUX 2
#define VAUX 2
#define PACE 4
#define QSLEEP 0
#define ABL 0
#define PROBE 3
#define STAMPS 0

namespace {
constexpr int D = 640, H = 32, SK = 16384, DH = 128, DF = 1728;
constexpr int NWG = 256, NT = 512;

typedef unsigned u32;
typedef unsigned long long u64;
typedef unsigned short u16;
typedef u32 u32x2 __attribute__((ext_vector_type(2)));
typedef u32 u32x4 __attribute__((ext_vector_type(4)));
typedef float f32x2 __attribute__((ext_vector_type(2)));
typedef float f32x4 __attribute__((ext_vector_type(4)));
typedef __bf16 bf16x2 __attribute__((ext_vector_type(2)));
typedef __bf16 bf16x8 __attribute__((ext_vector_type(8)));
typedef __amdgpu_buffer_rsrc_t rsrc_t;
#define GAS __attribute__((address_space(1)))

constexpr int AUX_NT = 2, AUX_SC1 = 16;
constexpr u32 OOB = 0x80000000u;

constexpr u32 WS_EPOCH = 0;
constexpr u32 WS_QP = 4096;
constexpr u32 WS_F1 = WS_QP + NWG * 2048;
constexpr u32 WS_H1 = WS_F1 + NWG * 2048;
constexpr u32 H1_STRIDE = 144 * 8;
constexpr u32 WS_H2 = WS_H1 + NWG * H1_STRIDE;
constexpr u32 H2_STRIDE = 320 * 8;
constexpr u32 WS_STAMP = WS_H2 + NWG * H2_STRIDE;
constexpr u32 WS_BYTES = WS_STAMP + NWG * 8 * 16 * 4;

constexpr int L_Q = 0;
constexpr int L_REC = L_Q + 64;
constexpr int L_F1 = L_REC + 8 * 132;
constexpr int L_R1 = L_F1 + 8 * 256;
constexpr int L_SR = L_R1 + 8 * 132;
constexpr int L_FR = L_SR + 8 * 256;
constexpr int L_END = L_FR + 8 * 256;

constexpr float LOG2E = 1.4426950408889634f;
constexpr float SCALE = 0.08838834764831845f;
constexpr u32 SPIN_CAP = 1u << 18;

__device__ __forceinline__ float lo16(u32 v) { return __uint_as_float(v << 16); }
__device__ __forceinline__ float hi16(u32 v) { return __uint_as_float(v & 0xffff0000u); }
__device__ __forceinline__ u32 pk2(float a, float b) { bf16x2 r = {(__bf16)a, (__bf16)b}; return __builtin_bit_cast(u32, r); }
__device__ __forceinline__ float rbf(float a) { return lo16(pk2(a, a)); }
__device__ __forceinline__ float dot2(u32 a, u32 b, float c) { return __builtin_amdgcn_fdot2_f32_bf16(__builtin_bit_cast(bf16x2, a), __builtin_bit_cast(bf16x2, b), c, false); }
template <int CTRL> __device__ __forceinline__ float dppf(float x) { return __builtin_bit_cast(float, __builtin_amdgcn_mov_dpp(__builtin_bit_cast(int, x), CTRL, 0xf, 0xf, true)); }
template <int CTRL> __device__ __forceinline__ u32 dppu(u32 x) { return (u32)__builtin_amdgcn_mov_dpp((int)x, CTRL, 0xf, 0xf, true); }
constexpr int XOR1 = 0xB1, XOR2 = 0x4E, HMIR = 0x141, ROR4 = 0x124, ROR8 = 0x128, NEWB = 0x150;
__device__ __forceinline__ float xrow_max(float x) {
  auto s = __builtin_amdgcn_permlane16_swap(__float_as_uint(x), __float_as_uint(x), false, false);
  x = fmaxf(__uint_as_float(s[0]), __uint_as_float(s[1]));
  auto t = __builtin_amdgcn_permlane32_swap(__float_as_uint(x), __float_as_uint(x), false, false);
  return fmaxf(__uint_as_float(t[0]), __uint_as_float(t[1]));
}
__device__ __forceinline__ float xrow_sum(float x) {
  auto s = __builtin_amdgcn_permlane16_swap(__float_as_uint(x), __float_as_uint(x), false, false);
  x = __uint_as_float(s[0]) + __uint_as_float(s[1]);
  auto t = __builtin_amdgcn_permlane32_swap(__float_as_uint(x), __float_as_uint(x), false, false);
  return __uint_as_float(t[0]) + __uint_as_float(t[1]);
}
__device__ __forceinline__ f32x4 mfma(u32x4 a, u32x4 b, f32x4 c) {
  return __builtin_amdgcn_mfma_f32_16x16x32_bf16(__builtin_bit_cast(bf16x8, a), __builtin_bit_cast(bf16x8, b), c, 0, 0, 0);
}
__device__ __forceinline__ rsrc_t mk(const void* p, u32 bytes) { return __builtin_amdgcn_make_buffer_rsrc((void*)p, 0, bytes, 0x00020000); }
template <int AUX> __device__ __forceinline__ u32x4 ld128(rsrc_t r, u32 voff, u32 soff) { return __builtin_amdgcn_raw_buffer_load_b128(r, voff, soff, AUX); }
template <int AUX> __device__ __forceinline__ u32x2 ld64(rsrc_t r, u32 voff, u32 soff) { return __builtin_amdgcn_raw_buffer_load_b64(r, voff, soff, AUX); }
__device__ __forceinline__ void st_granule(rsrc_t r, u32 off, float v, u32 tag) { __builtin_amdgcn_raw_buffer_store_b64(u32x2{__float_as_uint(v), tag}, r, off, 0, AUX_SC1); }
__device__ __forceinline__ void st_granule2(rsrc_t r, u32 off, float v0, float v1, u32 tag) { __builtin_amdgcn_raw_buffer_store_b128(u32x4{__float_as_uint(v0), tag, __float_as_uint(v1), tag}, r, off, 0, AUX_SC1); }
#define SB() __builtin_amdgcn_sched_barrier(0)
#define STAMP(k) do { } while (0)

struct Params {
  const u16 *x, *kc, *vc, *wq, *wo, *wg, *wu, *wd;
  u16* out;
  unsigned char* ws;
};

struct Sel { u32 k0, k1, k2, k3; };
__device__ __forceinline__ u32x4 sel(u32 pp, const Sel& s) { return u32x4{pp & s.k0, pp & s.k1, pp & s.k2, pp & s.k3}; }

__global__ void __launch_bounds__(NT, 2) fused_decode(Params p) {
  __shared__ __attribute__((aligned(16))) float lds[L_END];
  const int tid = threadIdx.x, lane = tid & 63, w = __builtin_amdgcn_readfirstlane(tid >> 6);
  const int g = lane >> 4, c = lane & 15, bb = c >> 3;
  const int bid = blockIdx.x, xc = bid & 7, jj = bid >> 3;
  const int par = xc & 1, h = (xc >> 1) + 4 * (jj >> 2), b = (jj >> 1) & 1, half = jj & 1;
  const int j = par + 2 * half + 4 * b;
  const int pid = h * 8 + j;
  const bool ffn = h < DF / 64;
  STAMP(0);
  const rsrc_t rws = mk(p.ws, WS_BYTES);
  u32 probe_ticks = 0;
  {
    const u64 tb = __builtin_amdgcn_s_memrealtime();
    if (bid == 0 && w == 7) {
      const unsigned char* base =
        PROBE == 1 ? (const unsigned char*)p.kc :
        PROBE == 2 ? (const unsigned char*)p.kc + (254u << 20) :
        PROBE == 3 ? (const unsigned char*)p.vc :
        PROBE == 4 ? (const unsigned char*)p.vc + (128u << 20) :
        PROBE == 5 ? (const unsigned char*)p.vc + (254u << 20) :
        PROBE == 6 ? p.ws + (8u << 20) :
        PROBE == 7 ? p.ws + (1020u << 20) :
        PROBE == 8 ? (const unsigned char*)p.kc + (128u << 20) :
        PROBE == 9 ? p.ws + (512u << 20) : (const unsigned char*)p.wo;
      const rsrc_t rp = mk(base, 2u << 20);
      u32 v = __builtin_amdgcn_raw_buffer_load_b32(rp, 0, 0, 2);
      { const u32 y = __builtin_amdgcn_readfirstlane(v); asm volatile("s_and_b32 %0, %1, 0" : "=s"(v) : "s"(y) : "scc"); }
      const u64 t0 = __builtin_amdgcn_s_memrealtime();
      for (int i = 1; i <= 32; ++i) { u32 x = __builtin_amdgcn_raw_buffer_load_b32(rp, v + i * 8448, 0, 2); const u32 y = __builtin_amdgcn_readfirstlane(x); asm volatile("s_and_b32 %0, %1, 0" : "=s"(v) : "s"(y) : "scc"); }
      probe_ticks = (u32)(__builtin_amdgcn_s_memrealtime() - t0);
    }
    while (__builtin_amdgcn_s_memrealtime() - tb < 4000) __builtin_amdgcn_s_sleep(4);
  }
  const u32 tag = *(const __attribute__((address_space(4))) u32*)(p.ws + WS_EPOCH) + 0x9E3779B9u;

  const size_t slab = ((size_t)(b * H + h) * SK + half * (SK / 2)) * DH;
  const rsrc_t rk = mk(p.kc + slab, SK / 2 * DH * 2), rv = mk(p.vc + slab, SK / 2 * DH * 2);
  const int blk0 = par ? NE : 0, blk1 = par ? 512 : NE;
  const int nst = (blk1 - blk0 - w + 7) >> 3;
  const u32 kvbase = (blk0 + w) * 4096u;
  const u32 vo16 = lane * 16;
  u32x4 KR[SIF][4], VR[SIF][4];
  auto fillk = [&](int s, int step) __attribute__((always_inline)) {
    const u32 so = step < nst ? kvbase + step * 32768u : OOB;
#pragma unroll
    for (int i = 0; i < 4; ++i) KR[s][i] = ld128<KAUX>(rk, vo16 + i * 1024, so);
    SB();
  };
  auto fillv = [&](int s, int step) __attribute__((always_inline)) {
    const u32 so = step < nst ? kvbase + step * 32768u : OOB;
#pragma unroll
    for (int i = 0; i < 4; ++i) VR[s][i] = ld128<VAUX>(rv, vo16 + i * 1024, so);
    SB();
  };
  auto fill = [&](int s, int step) __attribute__((always_inline)) { fillk(s, step); fillv(s, step); };
  u32x4 wor[4], wdr[2];
  auto fillw = [&]() __attribute__((always_inline)) {
    const rsrc_t ro = mk(p.wo, H * DH * D * 2), rd = mk(p.wd, DF * D * 2);
    const u32 vo = (c < 10 && !(ABL & 2)) ? (128 * h + 16 * w + 4 * g) * (D * 2) + j * 160 + c * 16 : OOB;
    const u32 vd = (ffn && c < 10 && !(ABL & 2)) ? (64 * h + 8 * w + 2 * g) * (D * 2) + j * 160 + c * 16 : OOB;
#pragma unroll
    for (int i = 0; i < 4; ++i) wor[i] = ld128<AUX_NT>(ro, vo, i * (D * 2));
#pragma unroll
    for (int i = 0; i < 2; ++i) wdr[i] = ld128<AUX_NT>(rd, vd, i * (D * 2));
    SB();
  };
  Sel sl;
  {
    const int mm = lane & 7;
    const u32 hm = (mm & 1) ? 0xffff0000u : 0x0000ffffu;
    sl.k0 = (mm >> 1) == 0 ? hm : 0u; sl.k1 = (mm >> 1) == 1 ? hm : 0u; sl.k2 = (mm >> 1) == 2 ? hm : 0u; sl.k3 = (mm >> 1) == 3 ? hm : 0u;
  }

  if (!(ABL & 4) && (w == 0 || (w == 1 && ffn))) {
    u32x2 xs[5], xo[5];
    u32x4 wr[20];
    const rsrc_t rx = mk(p.x, 2 * D * 2);
#pragma unroll
    for (int i = 0; i < 5; ++i) xs[i] = ld64<0>(rx, (bb * D + 20 * (8 * g + (c & 7))) * 2 + i * 8, 0);
#pragma unroll
    for (int i = 0; i < 5; ++i) xo[i] = ld64<0>(rx, (bb * D + 80 * j + 20 * g) * 2 + i * 8, 0);
    const int ld = w == 0 ? H * DH : DF;
    const GAS u16* src = w == 0 ? (const GAS u16*)p.wq + h * DH + c * 8 : (const GAS u16*)(c < 8 ? p.wg : p.wu) + h * 64 + (c & 7) * 8;
    src += (80 * j + 20 * g) * ld;
#pragma unroll
    for (int i = 0; i < 20; ++i) wr[i] = __builtin_nontemporal_load((const GAS u32x4*)(src + i * ld));
    SB();
    __builtin_amdgcn_s_barrier();
    SB();
    float xv[20];
#pragma unroll
    for (int i = 0; i < 5; ++i) { xv[4 * i] = lo16(xs[i][0]); xv[4 * i + 1] = hi16(xs[i][0]); xv[4 * i + 2] = lo16(xs[i][1]); xv[4 * i + 3] = hi16(xs[i][1]); }
    float s = 0.f;
#pragma unroll
    for (int i = 0; i < 20; ++i) s += xv[i];
    s += dppf<XOR1>(s); s += dppf<XOR2>(s); s += dppf<HMIR>(s); s = xrow_sum(s);
    const float mean = s * (1.f / D);
    float q2 = 0.f;
#pragma unroll
    for (int i = 0; i < 20; ++i) { const float d = xv[i] - mean; q2 = fmaf(d, d, q2); }
    q2 += dppf<XOR1>(q2); q2 += dppf<XOR2>(q2); q2 += dppf<HMIR>(q2); q2 = xrow_sum(q2);
    const float mean_b = rbf(mean);
    const float rden = 1.f / rbf(__builtin_sqrtf(rbf(rbf(q2 * (1.f / D)) + 1e-5f)));
    STAMP(8);
    u32 xn[20];
#pragma unroll
    for (int i = 0; i < 5; ++i) {
      const float t0 = rbf(lo16(xo[i][0]) - mean_b) * rden, t1 = rbf(hi16(xo[i][0]) - mean_b) * rden, t2 = rbf(lo16(xo[i][1]) - mean_b) * rden, t3 = rbf(hi16(xo[i][1]) - mean_b) * rden;
      xn[4 * i] = pk2(t0, t0); xn[4 * i + 1] = pk2(t1, t1); xn[4 * i + 2] = pk2(t2, t2); xn[4 * i + 3] = pk2(t3, t3);
    }
    f32x4 a = {0.f, 0.f, 0.f, 0.f}, a2 = a;
#pragma unroll
    for (int i = 0; i < 20; i += 2) { a = mfma(sel(xn[i], sl), wr[i], a); a2 = mfma(sel(xn[i + 1], sl), wr[i + 1], a2); }
    a += a2;
    const u32 o = (w == 0 ? WS_QP : WS_F1) + pid * 2048 + lane * 16;
    st_granule2(rws, o, a[0], a[1], tag);
    st_granule2(rws, o + 1024, a[2], a[3], tag);
    STAMP(10);
    if (w == 0) {
      const u32 oq = WS_QP + h * 8 * 2048 + (lane >> 5) * 1024 + (32 * b + (lane & 31)) * 16;
      u32x4 qa[8];
      if (QSLEEP) __builtin_amdgcn_s_sleep(QSLEEP);
      for (u32 spins = 0; spins < SPIN_CAP; ++spins) {
        asm volatile("" ::: "memory");
#pragma unroll
        for (int pp = 0; pp < 8; ++pp) qa[pp] = ld128<AUX_SC1>(rws, oq + pp * 2048, 0);
        bool ok = true;
#pragma unroll
        for (int pp = 0; pp < 8; ++pp) ok = ok && qa[pp][1] == tag && qa[pp][3] == tag;
        if (__all(ok)) break;
        if (POLL0) __builtin_amdgcn_s_sleep(POLL0);
      }
      f32x2 q = {0.f, 0.f};
#pragma unroll
      for (int pp = 0; pp < 8; ++pp) { q[0] += __uint_as_float(qa[pp][0]); q[1] += __uint_as_float(qa[pp][2]); }
      ((u32*)lds)[L_Q + 4 * (lane & 15) + 2 * ((lane >> 4) & 1) + (lane >> 5)] = pk2(q[0], q[1]);
      STAMP(9);
      __syncthreads();
#pragma unroll
      for (int s = 0; s < SIF; ++s) fill(s, s);
      fillw();
    } else {
#pragma unroll
      for (int s = 0; s < SIF; ++s) fill(s, s);
      fillw();
      __syncthreads();
    }
  } else {
    __builtin_amdgcn_s_barrier();
    SB();
    fillk(0, 0); fillv(0, 0);
#pragma unroll
    for (int s = 1; s < SIF; ++s) {
      asm volatile("s_waitcnt vmcnt(%0)" ::"n"(PACE) : "memory"); fillk(s, s);
      asm volatile("s_waitcnt vmcnt(%0)" ::"n"(PACE) : "memory"); fillv(s, s);
    }
    fillw();
    __syncthreads();
  }
  STAMP(1);
  const u32x4 qv = *(const u32x4*)&lds[L_Q + 4 * c];

  float m = -INFINITY, l = 0.f;
  f32x4 acc = {0.f, 0.f, 0.f, 0.f};
  const bool c3 = (lane & 8) != 0, c2 = (lane & 4) != 0;
  auto consume = [&](int s, int step) __attribute__((always_inline)) {
    float xd[4];
#pragma unroll
    for (int t = 0; t < 4; ++t) {
      float d = dot2(KR[s][t][0], qv[0], 0.f);
      d = dot2(KR[s][t][1], qv[1], d); d = dot2(KR[s][t][2], qv[2], d); xd[t] = dot2(KR[s][t][3], qv[3], d);
    }
    const float y0 = (c3 ? xd[2] : xd[0]) + dppf<ROR8>(c3 ? xd[0] : xd[2]);
    const float y1 = (c3 ? xd[3] : xd[1]) + dppf<ROR8>(c3 ? xd[1] : xd[3]);
    float sc = (c2 ? y1 : y0) + dppf<HMIR>(c2 ? y0 : y1);
    sc += dppf<XOR1>(sc); sc += dppf<XOR2>(sc);
    sc = rbf(rbf(sc) * SCALE);
    sc = step < nst ? sc : -INFINITY;
    float pm = fmaxf(sc, dppf<ROR4>(sc)); pm = fmaxf(pm, dppf<ROR8>(pm)); pm = xrow_max(pm);
    const float mn = fmaxf(m, pm);
    const float al = __builtin_amdgcn_exp2f((m - mn) * LOG2E), pr = __builtin_amdgcn_exp2f((sc - mn) * LOG2E);
    m = mn; l = fmaf(l, al, pr);
    acc *= al;
    const u32 pp = pk2(pr, pr);
    acc = mfma(sel(dppu<NEWB + 0>(pp), sl), VR[s][0], acc);
    acc = mfma(sel(dppu<NEWB + 4>(pp), sl), VR[s][1], acc);
    acc = mfma(sel(dppu<NEWB + 8>(pp), sl), VR[s][2], acc);
    acc = mfma(sel(dppu<NEWB + 12>(pp), sl), VR[s][3], acc);
  };
  auto turn = [&](int t) __attribute__((always_inline)) {
#pragma unroll
    for (int s = 0; s < SIF; ++s) { consume(s, t * SIF + s); SB(); fill(s, (t + 1) * SIF + s); }
  };
  const int nturn = (((blk1 - blk0 + 7) >> 3) + SIF - 1) / SIF;
#pragma unroll 1
  for (int t = 0; t < nturn - 1; ++t) turn(t);
  const u32 of1 = ffn ? WS_F1 + (h * 8 + w) * 2048 + lane * 16 : OOB;
  u32x4 f0 = ld128<AUX_SC1>(rws, of1, 0), f1 = ld128<AUX_SC1>(rws, of1, 1024);
  SB();
  turn(nturn - 1);
  STAMP(3);
  if (ffn && !(ABL & 4) && !__all(f0[1] == tag && f0[3] == tag && f1[1] == tag && f1[3] == tag)) {
    for (u32 spins = 0; spins < SPIN_CAP; ++spins) {
      __builtin_amdgcn_s_sleep(8);
      asm volatile("" ::: "memory");
      f0 = ld128<AUX_SC1>(rws, of1, 0); f1 = ld128<AUX_SC1>(rws, of1, 1024);
      if (__all(f0[1] == tag && f0[3] == tag && f1[1] == tag && f1[3] == tag)) break;
    }
  }

  {
    float ls = l; ls += dppf<XOR1>(ls); ls += dppf<XOR2>(ls); ls += dppf<ROR4>(ls); ls += dppf<ROR8>(ls); ls = xrow_sum(ls) * 0.25f;
    float* rec = &lds[L_REC + w * 132];
    if (lane < 32) *(f32x4*)&rec[4 + 8 * c + 4 * g] = acc;
    if (lane == 0) { rec[0] = m; rec[1] = ls; }
    u32* fb = (u32*)&lds[L_F1 + w * 256 + (g >> 1) * 128 + 8 * c + 4 * (g & 1)];
    *(u32x4*)fb = u32x4{f0[0], f0[2], f1[0], f1[2]};
  }
  __syncthreads();
  STAMP(4);
  if (tid < 130) {
    float M = -INFINITY;
#pragma unroll
    for (int ww = 0; ww < 8; ++ww) M = fmaxf(M, lds[L_REC + ww * 132]);
    float v = 0.f;
    const int idx = tid < 128 ? 4 + tid : 1;
#pragma unroll
    for (int ww = 0; ww < 8; ++ww) v = fmaf(__builtin_amdgcn_exp2f((lds[L_REC + ww * 132] - M) * LOG2E), lds[L_REC + ww * 132 + idx], v);
    if (tid == 128) v = M;
    st_granule(rws, WS_H1 + pid * H1_STRIDE + tid * 8, v, tag);
  }
  {
    f32x4 af = {0.f, 0.f, 0.f, 0.f};
#pragma unroll
    for (int i = 0; i < 2; ++i) {
      float gs = 0.f, us = 0.f;
#pragma unroll
      for (int pp = 0; pp < 8; ++pp) { gs += lds[L_F1 + pp * 256 + bb * 128 + 8 * w + 2 * g + i]; us += lds[L_F1 + pp * 256 + bb * 128 + 64 + 8 * w + 2 * g + i]; }
      const float gg = rbf(gs), uu = rbf(us);
      const float z = 0.7978845608028654f * (gg + 0.044715f * gg * gg * gg);
      const float th = 1.f - 2.f / (__builtin_amdgcn_exp2f(z * (2.f * LOG2E)) + 1.f);
      const float hv = rbf(0.5f * gg * (1.f + th)) * uu;
      af = mfma(sel(pk2(hv, hv), sl), wdr[i], af);
    }
    *(f32x4*)&lds[L_FR + w * 256 + (g >> 1) * 128 + 8 * c + 4 * (g & 1)] = af;
  }
  {
    const u32 o1 = WS_H1 + (h * 8 + w) * H1_STRIDE;
    u32x4 a, ml; u32 spins = 0;
    for (;;) {
      asm volatile("" ::: "memory");
      ml = ld128<AUX_SC1>(rws, o1 + 1024, 0);
      if (ml[1] == tag && ml[3] == tag) {
        asm volatile("" ::: "memory");
        a = ld128<AUX_SC1>(rws, o1 + lane * 16, 0);
        if (__all(a[1] == tag && a[3] == tag)) break;
      }
      if (++spins >= SPIN_CAP) break;
      __builtin_amdgcn_s_sleep(POLL1);
    }
    float* r1 = &lds[L_R1 + w * 132];
    *(u32x2*)&r1[4 + 2 * lane] = u32x2{a[0], a[2]};
    if (lane == 0) *(u32x2*)&r1[0] = u32x2{ml[0], ml[2]};
  }
  __syncthreads();
  STAMP(5);
  {
    const float* r1 = &lds[L_R1 + bb * 4 * 132];
    const float m0 = r1[0], m1 = r1[132], m2 = r1[264], m3 = r1[396];
    const float M = fmaxf(fmaxf(m0, m1), fmaxf(m2, m3));
    const float e0 = __builtin_amdgcn_exp2f((m0 - M) * LOG2E), e1 = __builtin_amdgcn_exp2f((m1 - M) * LOG2E), e2 = __builtin_amdgcn_exp2f((m2 - M) * LOG2E), e3 = __builtin_amdgcn_exp2f((m3 - M) * LOG2E);
    const float L = e0 * r1[1] + e1 * r1[133] + e2 * r1[265] + e3 * r1[397];
    const int d0 = 4 + 16 * w + 4 * g;
    f32x4 o = *(const f32x4*)&r1[d0] * e0 + *(const f32x4*)&r1[132 + d0] * e1 + *(const f32x4*)&r1[264 + d0] * e2 + *(const f32x4*)&r1[396 + d0] * e3;
    o *= 1.f / L;
    f32x4 as = {0.f, 0.f, 0.f, 0.f};
#pragma unroll
    for (int i = 0; i < 4; ++i) as = mfma(sel(pk2(o[i], o[i]), sl), wor[i], as);
    *(f32x4*)&lds[L_SR + w * 256 + (g >> 1) * 128 + 8 * c + 4 * (g & 1)] = as;
  }
  __syncthreads();
  if (tid < 320) {
    const int k = tid < 160 ? tid : tid - 160, bo = k >= 80, col = k - 80 * bo;
    const float* src = &lds[(tid < 160 ? L_SR : L_FR) + bo * 128 + col];
    float v = 0.f;
#pragma unroll
    for (int ww = 0; ww < 8; ++ww) v += src[ww * 256];
    st_granule(rws, WS_H2 + pid * H2_STRIDE + tid * 8, v, tag);
  }
  STAMP(6);
  if (w == 7) {
    const int hp = lane & 31;
    const u32 o2 = WS_H2 + (hp * 8 + j) * H2_STRIDE + ((lane >> 5) * 160 + 5 * h) * 8;
    const int bo = h >> 4, col = 80 * j + 5 * (h & 15);
    const u16 xraw = p.x[bo * D + col + (lane < 5 ? lane : 0)];
    u32x4 a = {0u, 0u, 0u, 0u}, a2 = a; u32x2 a3 = {0u, 0u}; u32 spins = 0;
    bool need = true;
    for (;;) {
      asm volatile("" ::: "memory");
      if (need) {
        a = ld128<AUX_SC1>(rws, o2, 0); a2 = ld128<AUX_SC1>(rws, o2 + 16, 0); a3 = ld64<AUX_SC1>(rws, o2 + 32, 0);
        need = !(a[1] == tag && a[3] == tag && a2[1] == tag && a2[3] == tag && a3[1] == tag);
      }
      if (!__any(need) || ++spins >= SPIN_CAP) break;
      __builtin_amdgcn_s_sleep(POLL2);
    }
    float v[5] = {__uint_as_float(a[0]), __uint_as_float(a[2]), __uint_as_float(a2[0]), __uint_as_float(a2[2]), __uint_as_float(a3[0])};
    float sv = 0.f, fv = 0.f;
#pragma unroll
    for (int k = 0; k < 5; ++k) {
      float t = v[k];
      t += dppf<XOR1>(t); t += dppf<XOR2>(t); t += dppf<ROR4>(t); t += dppf<ROR8>(t);
      auto s16 = __builtin_amdgcn_permlane16_swap(__float_as_uint(t), __float_as_uint(t), false, false);
      t = __uint_as_float(s16[0]) + __uint_as_float(s16[1]);
      const float S = __builtin_bit_cast(float, __builtin_amdgcn_readlane(__builtin_bit_cast(int, t), 0));
      const float F = __builtin_bit_cast(float, __builtin_amdgcn_readlane(__builtin_bit_cast(int, t), 32));
      if (lane == k) { sv = S; fv = F; }
    }
    if (lane < 5) {
      const float s = rbf(sv), f = rbf(fv);
      const float gt = rbf(f / (1.f + __builtin_amdgcn_exp2f(-f * LOG2E)));
      const float r = __uint_as_float((u32)xraw << 16) + rbf(s * gt);
      p.out[bo * D + col + lane] = (u16)(pk2(r, r) & 0xffffu);
    }
    if (bid == 0 && lane == 0) __builtin_amdgcn_raw_buffer_store_b32(tag, rws, WS_EPOCH, 0, AUX_SC1);
    STAMP(7);
    if (bid == 0) { const u64 t1 = __builtin_amdgcn_s_memrealtime(); while (__builtin_amdgcn_s_memrealtime() - t1 < (u64)probe_ticks * 4) __builtin_amdgcn_s_sleep(1); }
  }
}
}

extern "C" void kernel_launch(void* const* d_in, const int* in_sizes, int n_in, void* d_out, int out_size, void* d_ws, size_t ws_size, hipStream_t stream) {
  Params p;
  p.x = (const u16*)d_in[0]; p.kc = (const u16*)d_in[1]; p.vc = (const u16*)d_in[2]; p.wq = (const u16*)d_in[3]; p.wo = (const u16*)d_in[4];
  p.wg = (const u16*)d_in[6]; p.wu = (const u16*)d_in[7]; p.wd = (const u16*)d_in[8];
  p.out = (u16*)d_out; p.ws = (unsigned char*)d_ws;
  fused_decode<<<NWG, NT, 0, stream>>>(p);
}
```

```cpp
#include <hip/hip_runtime.h>
#include <stdint.h>

#define SIF 3
#define NE 268
#define LOCK 0
#define POLL0 1
#define POLL1 4
#define POLL2 8
#define KAUX 2
#define VAUX 2
#define PACE 63
#define QSLEEP 0
#define ABL 0
#define MAPV 0
#define FINE 1
#define STAMPS 0

namespace {
constexpr int D = 640, H = 32, SK = 16384, DH = 128, DF = 1728;
constexpr int NWG = 256, NT = 512;

typedef unsigned u32;
typedef unsigned long long u64;
typedef unsigned short u16;
typedef u32 u32x2 __attribute__((ext_vector_type(2)));
typedef u32 u32x4 __attribute__((ext_vector_type(4)));
typedef float f32x2 __attribute__((ext_vector_type(2)));
typedef float f32x4 __attribute__((ext_vector_type(4)));
typedef __bf16 bf16x2 __attribute__((ext_vector_type(2)));
typedef __bf16 bf16x8 __attribute__((ext_vector_type(8)));
typedef __amdgpu_buffer_rsrc_t rsrc_t;
#define GAS __attribute__((address_space(1)))

constexpr int AUX_NT = 2, AUX_SC1 = 16;
constexpr u32 OOB = 0x80000000u;

constexpr u32 WS_EPOCH = 0;
constexpr u32 WS_QP = 4096;
constexpr u32 WS_F1 = WS_QP + NWG * 2048;
constexpr u32 WS_H1 = WS_F1 + NWG * 2048;
constexpr u32 H1_STRIDE = 144 * 8;
constexpr u32 WS_H2 = WS_H1 + NWG * H1_STRIDE;
constexpr u32 H2_STRIDE = 320 * 8;
constexpr u32 WS_STAMP = WS_H2 + NWG * H2_STRIDE;
constexpr u32 WS_BYTES = WS_STAMP + NWG * 8 * 16 * 4;

constexpr int L_Q = 0;
constexpr int L_REC = L_Q + 64;
constexpr int L_F1 = L_REC + 8 * 132;
constexpr int L_R1 = L_F1 + 8 * 256;
constexpr int L_SR = L_R1 + 8 * 132;
constexpr int L_FR = L_SR + 8 * 256;
constexpr int L_USED = L_FR + 8 * 256;
constexpr int L_END = 21504;
static_assert(L_USED <= L_END, "");

constexpr float LOG2E = 1.4426950408889634f;
constexpr float SCALE = 0.08838834764831845f;
constexpr u32 SPIN_CAP = 1u << 18;

__device__ __forceinline__ float lo16(u32 v) { return __uint_as_float(v << 16); }
__device__ __forceinline__ float hi16(u32 v) { return __uint_as_float(v & 0xffff0000u); }
__device__ __forceinline__ u32 pk2(float a, float b) { bf16x2 r = {(__bf16)a, (__bf16)b}; return __builtin_bit_cast(u32, r); }
__device__ __forceinline__ float rbf(float a) { return lo16(pk2(a, a)); }
__device__ __forceinline__ float dot2(u32 a, u32 b, float c) { return __builtin_amdgcn_fdot2_f32_bf16(__builtin_bit_cast(bf16x2, a), __builtin_bit_cast(bf16x2, b), c, false); }
template <int CTRL> __device__ __forceinline__ float dppf(float x) { return __builtin_bit_cast(float, __builtin_amdgcn_mov_dpp(__builtin_bit_cast(int, x), CTRL, 0xf, 0xf, true)); }
template <int CTRL> __device__ __forceinline__ u32 dppu(u32 x) { return (u32)__builtin_amdgcn_mov_dpp((int)x, CTRL, 0xf, 0xf, true); }
constexpr int XOR1 = 0xB1, XOR2 = 0x4E, HMIR = 0x141, ROR4 = 0x124, ROR8 = 0x128, NEWB = 0x150;
__device__ __forceinline__ float xrow_max(float x) {
  auto s = __builtin_amdgcn_permlane16_swap(__float_as_uint(x), __float_as_uint(x), false, false);
  x = fmaxf(__uint_as_float(s[0]), __uint_as_float(s[1]));
  auto t = __builtin_amdgcn_permlane32_swap(__float_as_uint(x), __float_as_uint(x), false, false);
  return fmaxf(__uint_as_float(t[0]), __uint_as_float(t[1]));
}
__device__ __forceinline__ float xrow_sum(float x) {
  auto s = __builtin_amdgcn_permlane16_swap(__float_as_uint(x), __float_as_uint(x), false, false);
  x = __uint_as_float(s[0]) + __uint_as_float(s[1]);
  auto t = __builtin_amdgcn_permlane32_swap(__float_as_uint(x), __float_as_uint(x), false, false);
  return __uint_as_float(t[0]) + __uint_as_float(t[1]);
}
__device__ __forceinline__ f32x4 mfma(u32x4 a, u32x4 b, f32x4 c) {
  return __builtin_amdgcn_mfma_f32_16x16x32_bf16(__builtin_bit_cast(bf16x8, a), __builtin_bit_cast(bf16x8, b), c, 0, 0, 0);
}
__device__ __forceinline__ rsrc_t mk(const void* p, u32 bytes) { return __builtin_amdgcn_make_buffer_rsrc((void*)p, 0, bytes, 0x00020000); }
template <int AUX> __device__ __forceinline__ u32x4 ld128(rsrc_t r, u32 voff, u32 soff) { return __builtin_amdgcn_raw_buffer_load_b128(r, voff, soff, AUX); }
template <int AUX> __device__ __forceinline__ u32x2 ld64(rsrc_t r, u32 voff, u32 soff) { return __builtin_amdgcn_raw_buffer_load_b64(r, voff, soff, AUX); }
__device__ __forceinline__ void st_granule(rsrc_t r, u32 off, float v, u32 tag) { __builtin_amdgcn_raw_buffer_store_b64(u32x2{__float_as_uint(v), tag}, r, off, 0, AUX_SC1); }
__device__ __forceinline__ void st_granule2(rsrc_t r, u32 off, float v0, float v1, u32 tag) { __builtin_amdgcn_raw_buffer_store_b128(u32x4{__float_as_uint(v0), tag, __float_as_uint(v1), tag}, r, off, 0, AUX_SC1); }
#define SB() __builtin_amdgcn_sched_barrier(0)
#define STAMP(k) do { } while (0)
#define CSTAMP(k) do { } while (0)

struct Params {
  const u16 *x, *kc, *vc, *wq, *wo, *wg, *wu, *wd;
  u16* out;
  unsigned char* ws;
};

struct Sel { u32 k0, k1, k2, k3; };
__device__ __forceinline__ u32x4 sel(u32 pp, const Sel& s) { return u32x4{pp & s.k0, pp & s.k1, pp & s.k2, pp & s.k3}; }

__global__ void __launch_bounds__(NT, 2) fused_decode(Params p) {
  __shared__ __attribute__((aligned(16))) float lds[L_END];
  const int tid = threadIdx.x, lane = tid & 63, w = __builtin_amdgcn_readfirstlane(tid >> 6);
  const int g = lane >> 4, c = lane & 15, bb = c >> 3;
  const int bid = blockIdx.x, xc = bid & 7, jj = bid >> 3;
  const int xp = xc >> 1;
  const int par = xc & 1, b = (jj >> 1) & 1, half = jj & 1;
  const int h = MAPV == 0 ? xp + 4 * (jj >> 2) : MAPV == 1 ? (xp == 1 ? 3 : xp == 3 ? 1 : xp) + 4 * (jj >> 2) : MAPV == 2 ? ((xp + (jj >> 2)) & 3) + 4 * (jj >> 2) : ((xp ^ (jj >> 2)) & 3) + 4 * (jj >> 2);
  const int j = par + 2 * half + 4 * b;
  const int pid = h * 8 + j;
  const bool ffn = h < DF / 64;
  STAMP(0);
  const rsrc_t rws = mk(p.ws, WS_BYTES);
  const u32 tag = *(const __attribute__((address_space(4))) u32*)(p.ws + WS_EPOCH) + 0x9E3779B9u;

  const size_t slab = ((size_t)(b * H + h) * SK + half * (SK / 2)) * DH;
  const rsrc_t rk = mk(p.kc + slab, SK / 2 * DH * 2), rv = mk(p.vc + slab, SK / 2 * DH * 2);
  const int blk0 = par ? NE : 0, blk1 = par ? 512 : NE;
  const int nst = (blk1 - blk0 - w + 7) >> 3;
  const u32 kvbase = (blk0 + w) * 4096u;
  const u32 vo16 = lane * 16;
  u32x4 KR[SIF][4], VR[SIF][4];
  auto fillk = [&](int s, int step) __attribute__((always_inline)) {
    const u32 so = kvbase + step * 32768u;
#pragma unroll
    for (int i = 0; i < 4; ++i) KR[s][i] = ld128<KAUX>(rk, vo16 + i * 1024, so);
    SB();
  };
  auto fillv = [&](int s, int step) __attribute__((always_inline)) {
    const u32 so = kvbase + step * 32768u;
#pragma unroll
    for (int i = 0; i < 4; ++i) VR[s][i] = ld128<VAUX>(rv, vo16 + i * 1024, so);
    SB();
  };
  auto fill = [&](int s, int step) __attribute__((always_inline)) { fillk(s, step); fillv(s, step); };
  u32x4 wor[4], wdr[2];
  auto fillw = [&]() __attribute__((always_inline)) {
    const rsrc_t ro = mk(p.wo, H * DH * D * 2), rd = mk(p.wd, DF * D * 2);
    const u32 vo = (c < 10 && !(ABL & 2)) ? (128 * h + 16 * w + 4 * g) * (D * 2) + j * 160 + c * 16 : OOB;
    const u32 vd = (ffn && c < 10 && !(ABL & 2)) ? (64 * h + 8 * w + 2 * g) * (D * 2) + j * 160 + c * 16 : OOB;
#pragma unroll
    for (int i = 0; i < 4; ++i) wor[i] = ld128<AUX_NT>(ro, vo, i * (D * 2));
#pragma unroll
    for (int i = 0; i < 2; ++i) wdr[i] = ld128<AUX_NT>(rd, vd, i * (D * 2));
    SB();
  };
  Sel sl;
  {
    const int mm = lane & 7;
    const u32 hm = (mm & 1) ? 0xffff0000u : 0x0000ffffu;
    sl.k0 = (mm >> 1) == 0 ? hm : 0u; sl.k1 = (mm >> 1) == 1 ? hm : 0u; sl.k2 = (mm >> 1) == 2 ? hm : 0u; sl.k3 = (mm >> 1) == 3 ? hm : 0u;
  }

  if (!(ABL & 4) && (w == 0 || (w == 1 && ffn))) {
    u32x2 xs[5], xo[5];
    u32x4 wr[20];
    const rsrc_t rx = mk(p.x, 2 * D * 2);
#pragma unroll
    for (int i = 0; i < 5; ++i) xs[i] = ld64<0>(rx, (bb * D + 20 * (8 * g + (c & 7))) * 2 + i * 8, 0);
#pragma unroll
    for (int i = 0; i < 5; ++i) xo[i] = ld64<0>(rx, (bb * D + 80 * j + 20 * g) * 2 + i * 8, 0);
    const int ld = w == 0 ? H * DH : DF;
    const GAS u16* src = w == 0 ? (const GAS u16*)p.wq + h * DH + c * 8 : (const GAS u16*)(c < 8 ? p.wg : p.wu) + h * 64 + (c & 7) * 8;
    src += (80 * j + 20 * g) * ld;
#pragma unroll
    for (int i = 0; i < 20; ++i) wr[i] = __builtin_nontemporal_load((const GAS u32x4*)(src + i * ld));
    SB();
    __builtin_amdgcn_s_barrier();
    SB();
    float xv[20];
#pragma unroll
    for (int i = 0; i < 5; ++i) { xv[4 * i] = lo16(xs[i][0]); xv[4 * i + 1] = hi16(xs[i][0]); xv[4 * i + 2] = lo16(xs[i][1]); xv[4 * i + 3] = hi16(xs[i][1]); }
    float s = 0.f;
#pragma unroll
    for (int i = 0; i < 20; ++i) s += xv[i];
    s += dppf<XOR1>(s); s += dppf<XOR2>(s); s += dppf<HMIR>(s); s = xrow_sum(s);
    const float mean = s * (1.f / D);
    float q2 = 0.f;
#pragma unroll
    for (int i = 0; i < 20; ++i) { const float d = xv[i] - mean; q2 = fmaf(d, d, q2); }
    q2 += dppf<XOR1>(q2); q2 += dppf<XOR2>(q2); q2 += dppf<HMIR>(q2); q2 = xrow_sum(q2);
    const float mean_b = rbf(mean);
    const float rden = 1.f / rbf(__builtin_sqrtf(rbf(rbf(q2 * (1.f / D)) + 1e-5f)));
    STAMP(8);
    u32 xn[20];
#pragma unroll
    for (int i = 0; i < 5; ++i) {
      const float t0 = rbf(lo16(xo[i][0]) - mean_b) * rden, t1 = rbf(hi16(xo[i][0]) - mean_b) * rden, t2 = rbf(lo16(xo[i][1]) - mean_b) * rden, t3 = rbf(hi16(xo[i][1]) - mean_b) * rden;
      xn[4 * i] = pk2(t0, t0); xn[4 * i + 1] = pk2(t1, t1); xn[4 * i + 2] = pk2(t2, t2); xn[4 * i + 3] = pk2(t3, t3);
    }
    f32x4 a = {0.f, 0.f, 0.f, 0.f}, a2 = a;
#pragma unroll
    for (int i = 0; i < 20; i += 2) { a = mfma(sel(xn[i], sl), wr[i], a); a2 = mfma(sel(xn[i + 1], sl), wr[i + 1], a2); }
    a += a2;
    const u32 o = (w == 0 ? WS_QP : WS_F1) + pid * 2048 + lane * 16;
    st_granule2(rws, o, a[0], a[1], tag);
    st_granule2(rws, o + 1024, a[2], a[3], tag);
    STAMP(10);
    if (w == 0) {
      const u32 oq = WS_QP + h * 8 * 2048 + (lane >> 5) * 1024 + (32 * b + (lane & 31)) * 16;
      u32x4 qa[8];
      if (QSLEEP) __builtin_amdgcn_s_sleep(QSLEEP);
      for (u32 spins = 0; spins < SPIN_CAP; ++spins) {
        asm volatile("" ::: "memory");
#pragma unroll
        for (int pp = 0; pp < 8; ++pp) qa[pp] = ld128<AUX_SC1>(rws, oq + pp * 2048, 0);
        bool ok = true;
#pragma unroll
        for (int pp = 0; pp < 8; ++pp) ok = ok && qa[pp][1] == tag && qa[pp][3] == tag;
        if (__all(ok)) break;
        if (POLL0) __builtin_amdgcn_s_sleep(POLL0);
      }
      f32x2 q = {0.f, 0.f};
#pragma unroll
      for (int pp = 0; pp < 8; ++pp) { q[0] += __uint_as_float(qa[pp][0]); q[1] += __uint_as_float(qa[pp][2]); }
      ((u32*)lds)[L_Q + 4 * (lane & 15) + 2 * ((lane >> 4) & 1) + (lane >> 5)] = pk2(q[0], q[1]);
      STAMP(9);
      __syncthreads();
#pragma unroll
      for (int s = 0; s < SIF; ++s) fill(s, s);
    } else {
#pragma unroll
      for (int s = 0; s < SIF; ++s) fill(s, s);
      __syncthreads();
    }
  } else {
    __builtin_amdgcn_s_barrier();
    SB();
    fillk(0, 0); fillv(0, 0);
#pragma unroll
    for (int s = 1; s < SIF; ++s) {
      asm volatile("s_waitcnt vmcnt(%0)" ::"n"(PACE) : "memory"); fillk(s, s);
      asm volatile("s_waitcnt vmcnt(%0)" ::"n"(PACE) : "memory"); fillv(s, s);
    }
    __syncthreads();
  }
  STAMP(1); CSTAMP(14);
  const u32x4 qv = *(const u32x4*)&lds[L_Q + 4 * c];

  float m = -INFINITY, l = 0.f;
  f32x4 acc = {0.f, 0.f, 0.f, 0.f};
  const bool c3 = (lane & 8) != 0, c2 = (lane & 4) != 0;
  auto consume = [&](int s, int next) __attribute__((always_inline)) {
    float xd[4];
#pragma unroll
    for (int t = 0; t < 4; ++t) {
      float d = dot2(KR[s][t][0], qv[0], 0.f);
      d = dot2(KR[s][t][1], qv[1], d); d = dot2(KR[s][t][2], qv[2], d); xd[t] = dot2(KR[s][t][3], qv[3], d);
    }
    if (FINE && next >= 0) { asm volatile("" : "+v"(xd[0]), "+v"(xd[1]), "+v"(xd[2]), "+v"(xd[3])); SB(); fillk(s, next); }
    const float y0 = (c3 ? xd[2] : xd[0]) + dppf<ROR8>(c3 ? xd[0] : xd[2]);
    const float y1 = (c3 ? xd[3] : xd[1]) + dppf<ROR8>(c3 ? xd[1] : xd[3]);
    float sc = (c2 ? y1 : y0) + dppf<HMIR>(c2 ? y0 : y1);
    sc += dppf<XOR1>(sc); sc += dppf<XOR2>(sc);
    sc = rbf(rbf(sc) * SCALE);
    float pm = fmaxf(sc, dppf<ROR4>(sc)); pm = fmaxf(pm, dppf<ROR8>(pm)); pm = xrow_max(pm);
    const float mn = fmaxf(m, pm);
    const float al = __builtin_amdgcn_exp2f((m - mn) * LOG2E), pr = __builtin_amdgcn_exp2f((sc - mn) * LOG2E);
    m = mn; l = fmaf(l, al, pr);
    acc *= al;
    const u32 pp = pk2(pr, pr);
    acc = mfma(sel(dppu<NEWB + 0>(pp), sl), VR[s][0], acc);
    acc = mfma(sel(dppu<NEWB + 4>(pp), sl), VR[s][1], acc);
    acc = mfma(sel(dppu<NEWB + 8>(pp), sl), VR[s][2], acc);
    acc = mfma(sel(dppu<NEWB + 12>(pp), sl), VR[s][3], acc);
    SB();
    if (next >= 0) { if (!FINE) fillk(s, next); fillv(s, next); }
  };
  consume(0, SIF);
#pragma unroll
  for (int s = 1; s < SIF; ++s) consume(s, -1);
  STAMP(13);
  fillw();
#pragma unroll 1
  for (int t = SIF; t < nst - 1; ++t) consume(0, t + 1);
  const u32 of1 = ffn ? WS_F1 + (h * 8 + w) * 2048 + lane * 16 : OOB;
  u32x4 f0 = ld128<AUX_SC1>(rws, of1, 0), f1 = ld128<AUX_SC1>(rws, of1, 1024);
  SB();
  consume(0, -1);
  STAMP(3); CSTAMP(15);
  if (ffn && !(ABL & 4) && !__all(f0[1] == tag && f0[3] == tag && f1[1] == tag && f1[3] == tag)) {
    for (u32 spins = 0; spins < SPIN_CAP; ++spins) {
      __builtin_amdgcn_s_sleep(8);
      asm volatile("" ::: "memory");
      f0 = ld128<AUX_SC1>(rws, of1, 0); f1 = ld128<AUX_SC1>(rws, of1, 1024);
      if (__all(f0[1] == tag && f0[3] == tag && f1[1] == tag && f1[3] == tag)) break;
    }
  }

  {
    float ls = l; ls += dppf<XOR1>(ls); ls += dppf<XOR2>(ls); ls += dppf<ROR4>(ls); ls += dppf<ROR8>(ls); ls = xrow_sum(ls) * 0.25f;
    float* rec = &lds[L_REC + w * 132];
    if (lane < 32) *(f32x4*)&rec[4 + 8 * c + 4 * g] = acc;
    if (lane == 0) { rec[0] = m; rec[1] = ls; }
    u32* fb = (u32*)&lds[L_F1 + w * 256 + (g >> 1) * 128 + 8 * c + 4 * (g & 1)];
    *(u32x4*)fb = u32x4{f0[0], f0[2], f1[0], f1[2]};
  }
  __syncthreads();
  STAMP(4);
  if (!(ABL & 8)) {
  const int mj = j ^ w;
  const u32 o1 = WS_H1 + (h * 8 + mj) * H1_STRIDE;
  u32x4 ra, rml;
  if (w != 0) { ra = ld128<AUX_SC1>(rws, o1 + lane * 16, 0); rml = ld128<AUX_SC1>(rws, o1 + 1024, 0); }
  else {
    float mw[8], M = -INFINITY;
#pragma unroll
    for (int ww = 0; ww < 8; ++ww) { mw[ww] = lds[L_REC + ww * 132]; M = fmaxf(M, mw[ww]); }
    f32x2 o = {0.f, 0.f}; float L = 0.f;
#pragma unroll
    for (int ww = 0; ww < 8; ++ww) {
      const float e = __builtin_amdgcn_exp2f((mw[ww] - M) * LOG2E);
      o += *(const f32x2*)&lds[L_REC + ww * 132 + 4 + 2 * lane] * e; L = fmaf(e, lds[L_REC + ww * 132 + 1], L);
    }
    st_granule2(rws, o1 + lane * 16, o[0], o[1], tag);
    if (lane == 0) st_granule2(rws, o1 + 1024, M, L, tag);
    ra = u32x4{__float_as_uint(o[0]), tag, __float_as_uint(o[1]), tag}; rml = u32x4{__float_as_uint(M), tag, __float_as_uint(L), tag};
  }
  SB();
  {
    f32x4 af = {0.f, 0.f, 0.f, 0.f};
#pragma unroll
    for (int i = 0; i < 2; ++i) {
      float gs = 0.f, us = 0.f;
#pragma unroll
      for (int pp = 0; pp < 8; ++pp) { gs += lds[L_F1 + pp * 256 + bb * 128 + 8 * w + 2 * g + i]; us += lds[L_F1 + pp * 256 + bb * 128 + 64 + 8 * w + 2 * g + i]; }
      const float gg = rbf(gs), uu = rbf(us);
      const float z = 0.7978845608028654f * (gg + 0.044715f * gg * gg * gg);
      const float th = 1.f - 2.f / (__builtin_amdgcn_exp2f(z * (2.f * LOG2E)) + 1.f);
      const float hv = rbf(0.5f * gg * (1.f + th)) * uu;
      af = mfma(sel(pk2(hv, hv), sl), wdr[i], af);
    }
    *(f32x4*)&lds[L_FR + w * 256 + (g >> 1) * 128 + 8 * c + 4 * (g & 1)] = af;
  }
  asm volatile("s_waitcnt lgkmcnt(0)" ::: "memory");
  __builtin_amdgcn_s_barrier();
  asm volatile("" ::: "memory");
  bool have = true;
  if (w != 0) have = __all(ra[1] == tag && ra[3] == tag && rml[1] == tag && rml[3] == tag);
  SB();
  if (tid >= 64 && tid < 224) {
    const int k = tid - 64, bo = k >= 80, col = k - 80 * bo;
    float v = 0.f;
#pragma unroll
    for (int ww = 0; ww < 8; ++ww) v += lds[L_FR + ww * 256 + bo * 128 + col];
    st_granule(rws, WS_H2 + pid * H2_STRIDE + (160 + k) * 8, v, tag);
  }
  if (!have) {
    for (u32 spins = 0; spins < SPIN_CAP; ++spins) {
      __builtin_amdgcn_s_sleep(POLL1);
      asm volatile("" ::: "memory");
      rml = ld128<AUX_SC1>(rws, o1 + 1024, 0);
      if (rml[1] == tag && rml[3] == tag) {
        asm volatile("" ::: "memory");
        ra = ld128<AUX_SC1>(rws, o1 + lane * 16, 0);
        if (__all(ra[1] == tag && ra[3] == tag)) break;
      }
    }
  }
  {
    float* r1 = &lds[L_R1 + mj * 132];
    *(u32x2*)&r1[4 + 2 * lane] = u32x2{ra[0], ra[2]};
    if (lane == 0) *(u32x2*)&r1[0] = u32x2{rml[0], rml[2]};
  }
  __syncthreads();
  STAMP(5);
  {
    const float* r1 = &lds[L_R1 + bb * 4 * 132];
    const float m0 = r1[0], m1 = r1[132], m2 = r1[264], m3 = r1[396];
    const float M = fmaxf(fmaxf(m0, m1), fmaxf(m2, m3));
    const float e0 = __builtin_amdgcn_exp2f((m0 - M) * LOG2E), e1 = __builtin_amdgcn_exp2f((m1 - M) * LOG2E), e2 = __builtin_amdgcn_exp2f((m2 - M) * LOG2E), e3 = __builtin_amdgcn_exp2f((m3 - M) * LOG2E);
    const float L = e0 * r1[1] + e1 * r1[133] + e2 * r1[265] + e3 * r1[397];
    const int d0 = 4 + 16 * w + 4 * g;
    f32x4 o = *(const f32x4*)&r1[d0] * e0 + *(const f32x4*)&r1[132 + d0] * e1 + *(const f32x4*)&r1[264 + d0] * e2 + *(const f32x4*)&r1[396 + d0] * e3;
    o *= 1.f / L;
    f32x4 as = {0.f, 0.f, 0.f, 0.f};
#pragma unroll
    for (int i = 0; i < 4; ++i) as = mfma(sel(pk2(o[i], o[i]), sl), wor[i], as);
    *(f32x4*)&lds[L_SR + w * 256 + (g >> 1) * 128 + 8 * c + 4 * (g & 1)] = as;
  }
  __syncthreads();
  if (tid < 160) {
    const int bo = tid >= 80, col = tid - 80 * bo;
    float v = 0.f;
#pragma unroll
    for (int ww = 0; ww < 8; ++ww) v += lds[L_SR + ww * 256 + bo * 128 + col];
    st_granule(rws, WS_H2 + pid * H2_STRIDE + tid * 8, v, tag);
  }
  STAMP(6);
  if (w == 7 && h < 5) {
    const int k0 = 32 * h + 2 * c, bo = k0 >= 80, col = 80 * j + k0 - 80 * bo;
    const u32 xraw = *(const u32*)(p.x + bo * D + col);
    const u32 o2 = WS_H2 + (g * 8 + j) * H2_STRIDE + k0 * 8;
    u32x4 a[16];
    u32 need = 0xffffu;
    for (u32 spins = 0; spins < SPIN_CAP; ++spins) {
      asm volatile("" ::: "memory");
#pragma unroll
      for (int i = 0; i < 16; ++i) if (need >> i & 1) a[i] = ld128<AUX_SC1>(rws, o2 + (i & 7) * (32 * H2_STRIDE) + (i >> 3) * 1280, 0);
#pragma unroll
      for (int i = 0; i < 16; ++i) if ((need >> i & 1) && a[i][1] == tag && a[i][3] == tag) need &= ~(1u << i);
      if (!__any(need != 0)) break;
      __builtin_amdgcn_s_sleep(POLL2);
    }
    f32x2 S = {0.f, 0.f}, F = {0.f, 0.f};
#pragma unroll
    for (int i = 0; i < 8; ++i) { S += f32x2{__uint_as_float(a[i][0]), __uint_as_float(a[i][2])}; F += f32x2{__uint_as_float(a[8 + i][0]), __uint_as_float(a[8 + i][2])}; }
    const float s0 = rbf(xrow_sum(S[0])), s1 = rbf(xrow_sum(S[1])), f0 = rbf(xrow_sum(F[0])), f1 = rbf(xrow_sum(F[1]));
    const float g0 = rbf(f0 / (1.f + __builtin_amdgcn_exp2f(-f0 * LOG2E))), g1 = rbf(f1 / (1.f + __builtin_amdgcn_exp2f(-f1 * LOG2E)));
    if (g == 0) *(u32*)(p.out + bo * D + col) = pk2(lo16(xraw) + rbf(s0 * g0), hi16(xraw) + rbf(s1 * g1));
    if (bid == 0 && lane == 0) __builtin_amdgcn_raw_buffer_store_b32(tag, rws, WS_EPOCH, 0, AUX_SC1);
    STAMP(7);
  }
  }
}
}

extern "C" void kernel_launch(void* const* d_in, const int* in_sizes, int n_in, void* d_out, int out_size, void* d_ws, size_t ws_size, hipStream_t stream) {
  Params p;
  p.x = (const u16*)d_in[0]; p.kc = (const u16*)d_in[1]; p.vc = (const u16*)d_in[2]; p.wq = (const u16*)d_in[3]; p.wo = (const u16*)d_in[4];
  p.wg = (const u16*)d_in[6]; p.wu = (const u16*)d_in[7]; p.wd = (const u16*)d_in[8];
  p.out = (u16*)d_out; p.ws = (unsigned char*)d_ws;
  fused_decode<<<NWG, NT, 0, stream>>>(p);
}
```

```cpp
#include <hip/hip_runtime.h>
#include <stdint.h>

#define SIF 3
#define NE 268
#define LOCK 0
#define POLL0 1
#define POLL1 4
#define POLL2 8
#define KAUX 2
#define VAUX 2
#define PACE 63
#define QSLEEP 0
#define ABL 0
#define MAPV 0
#define FINE 1
#define DIAG 9
#define DELAYK 1
#define STAMPS 0

namespace {
constexpr int D = 640, H = 32, SK = 16384, DH = 128, DF = 1728;
constexpr int NWG = 256, NT = 512;

typedef unsigned u32;
typedef unsigned long long u64;
typedef unsigned short u16;
typedef u32 u32x2 __attribute__((ext_vector_type(2)));
typedef u32 u32x4 __attribute__((ext_vector_type(4)));
typedef float f32x2 __attribute__((ext_vector_type(2)));
typedef float f32x4 __attribute__((ext_vector_type(4)));
typedef __bf16 bf16x2 __attribute__((ext_vector_type(2)));
typedef __bf16 bf16x8 __attribute__((ext_vector_type(8)));
typedef __amdgpu_buffer_rsrc_t rsrc_t;
#define GAS __attribute__((address_space(1)))

constexpr int AUX_NT = 2, AUX_SC1 = 16;
constexpr u32 OOB = 0x80000000u;

constexpr u32 WS_EPOCH = 0;
constexpr u32 WS_QP = 4096;
constexpr u32 WS_F1 = WS_QP + NWG * 2048;
constexpr u32 WS_H1 = WS_F1 + NWG * 2048;
constexpr u32 H1_STRIDE = 144 * 8;
constexpr u32 WS_H2 = WS_H1 + NWG * H1_STRIDE;
constexpr u32 H2_STRIDE = 320 * 8;
constexpr u32 WS_STAMP = WS_H2 + NWG * H2_STRIDE;
constexpr u32 WS_DIAG = WS_STAMP + NWG * 8 * 16 * 4;
constexpr u32 WS_BYTES = WS_DIAG + NWG * 16;

constexpr int L_Q = 0;
constexpr int L_REC = L_Q + 64;
constexpr int L_F1 = L_REC + 8 * 132;
constexpr int L_R1 = L_F1 + 8 * 256;
constexpr int L_SR = L_R1 + 8 * 132;
constexpr int L_FR = L_SR + 8 * 256;
constexpr int L_USED = L_FR + 8 * 256;
constexpr int L_END = 21504;
static_assert(L_USED <= L_END, "");

constexpr float LOG2E = 1.4426950408889634f;
constexpr float SCALE = 0.08838834764831845f;
constexpr u32 SPIN_CAP = 1u << 18;

__device__ __forceinline__ float lo16(u32 v) { return __uint_as_float(v << 16); }
__device__ __forceinline__ float hi16(u32 v) { return __uint_as_float(v & 0xffff0000u); }
__device__ __forceinline__ u32 pk2(float a, float b) { bf16x2 r = {(__bf16)a, (__bf16)b}; return __builtin_bit_cast(u32, r); }
__device__ __forceinline__ float rbf(float a) { return lo16(pk2(a, a)); }
__device__ __forceinline__ float dot2(u32 a, u32 b, float c) { return __builtin_amdgcn_fdot2_f32_bf16(__builtin_bit_cast(bf16x2, a), __builtin_bit_cast(bf16x2, b), c, false); }
template <int CTRL> __device__ __forceinline__ float dppf(float x) { return __builtin_bit_cast(float, __builtin_amdgcn_mov_dpp(__builtin_bit_cast(int, x), CTRL, 0xf, 0xf, true)); }
template <int CTRL> __device__ __forceinline__ u32 dppu(u32 x) { return (u32)__builtin_amdgcn_mov_dpp((int)x, CTRL, 0xf, 0xf, true); }
constexpr int XOR1 = 0xB1, XOR2 = 0x4E, HMIR = 0x141, ROR4 = 0x124, ROR8 = 0x128, NEWB = 0x150;
__device__ __forceinline__ float xrow_max(float x) {
  auto s = __builtin_amdgcn_permlane16_swap(__float_as_uint(x), __float_as_uint(x), false, false);
  x = fmaxf(__uint_as_float(s[0]), __uint_as_float(s[1]));
  auto t = __builtin_amdgcn_permlane32_swap(__float_as_uint(x), __float_as_uint(x), false, false);
  return fmaxf(__uint_as_float(t[0]), __uint_as_float(t[1]));
}
__device__ __forceinline__ float xrow_sum(float x) {
  auto s = __builtin_amdgcn_permlane16_swap(__float_as_uint(x), __float_as_uint(x), false, false);
  x = __uint_as_float(s[0]) + __uint_as_float(s[1]);
  auto t = __builtin_amdgcn_permlane32_swap(__float_as_uint(x), __float_as_uint(x), false, false);
  return __uint_as_float(t[0]) + __uint_as_float(t[1]);
}
__device__ __forceinline__ f32x4 mfma(u32x4 a, u32x4 b, f32x4 c) {
  return __builtin_amdgcn_mfma_f32_16x16x32_bf16(__builtin_bit_cast(bf16x8, a), __builtin_bit_cast(bf16x8, b), c, 0, 0, 0);
}
__device__ __forceinline__ rsrc_t mk(const void* p, u32 bytes) { return __builtin_amdgcn_make_buffer_rsrc((void*)p, 0, bytes, 0x00020000); }
template <int AUX> __device__ __forceinline__ u32x4 ld128(rsrc_t r, u32 voff, u32 soff) { return __builtin_amdgcn_raw_buffer_load_b128(r, voff, soff, AUX); }
template <int AUX> __device__ __forceinline__ u32x2 ld64(rsrc_t r, u32 voff, u32 soff) { return __builtin_amdgcn_raw_buffer_load_b64(r, voff, soff, AUX); }
__device__ __forceinline__ void st_granule(rsrc_t r, u32 off, float v, u32 tag) { __builtin_amdgcn_raw_buffer_store_b64(u32x2{__float_as_uint(v), tag}, r, off, 0, AUX_SC1); }
__device__ __forceinline__ void st_granule2(rsrc_t r, u32 off, float v0, float v1, u32 tag) { __builtin_amdgcn_raw_buffer_store_b128(u32x4{__float_as_uint(v0), tag, __float_as_uint(v1), tag}, r, off, 0, AUX_SC1); }
#define SB() __builtin_amdgcn_sched_barrier(0)
#define STAMP(k) do { } while (0)
#define CSTAMP(k) do { } while (0)

struct Params {
  const u16 *x, *kc, *vc, *wq, *wo, *wg, *wu, *wd;
  u16* out;
  unsigned char* ws;
};

struct Sel { u32 k0, k1, k2, k3; };
__device__ __forceinline__ u32x4 sel(u32 pp, const Sel& s) { return u32x4{pp & s.k0, pp & s.k1, pp & s.k2, pp & s.k3}; }

__global__ void __launch_bounds__(NT, 2) fused_decode(Params p) {
  __shared__ __attribute__((aligned(16))) float lds[L_END];
  const int tid = threadIdx.x, lane = tid & 63, w = __builtin_amdgcn_readfirstlane(tid >> 6);
  const int g = lane >> 4, c = lane & 15, bb = c >> 3;
  const int bid = blockIdx.x, xc = bid & 7, jj = bid >> 3;
  const int xp = xc >> 1;
  const int par = xc & 1, b = (jj >> 1) & 1, half = jj & 1;
  const int h = MAPV == 0 ? xp + 4 * (jj >> 2) : MAPV == 1 ? (xp == 1 ? 3 : xp == 3 ? 1 : xp) + 4 * (jj >> 2) : MAPV == 2 ? ((xp + (jj >> 2)) & 3) + 4 * (jj >> 2) : ((xp ^ (jj >> 2)) & 3) + 4 * (jj >> 2);
  const int j = par + 2 * half + 4 * b;
  const int pid = h * 8 + j;
  const bool ffn = h < DF / 64;
  STAMP(0);
  const rsrc_t rws = mk(p.ws, WS_BYTES);
  const u32 dgs = (u32)__builtin_amdgcn_s_memrealtime();
  if (tid == 448) { __builtin_amdgcn_raw_buffer_store_b64(u32x2{dgs, (u32)__builtin_amdgcn_s_getreg(20 | (3 << 11)) & 0xfu}, rws, WS_DIAG + bid * 16, 0, AUX_SC1); }
  const u32 tag = *(const __attribute__((address_space(4))) u32*)(p.ws + WS_EPOCH) + 0x9E3779B9u;

  const size_t slab = ((size_t)(b * H + h) * SK + half * (SK / 2)) * DH;
  const rsrc_t rk = mk(p.kc + slab, SK / 2 * DH * 2), rv = mk(p.vc + slab, SK / 2 * DH * 2);
  const int blk0 = par ? NE : 0, blk1 = par ? 512 : NE;
  const int nst = (blk1 - blk0 - w + 7) >> 3;
  const u32 kvbase = (blk0 + w) * 4096u;
  const u32 vo16 = lane * 16;
  u32x4 KR[SIF][4], VR[SIF][4];
  auto fillk = [&](int s, int step) __attribute__((always_inline)) {
    const u32 so = kvbase + step * 32768u;
#pragma unroll
    for (int i = 0; i < 4; ++i) KR[s][i] = ld128<KAUX>(rk, vo16 + i * 1024, so);
    SB();
  };
  auto fillv = [&](int s, int step) __attribute__((always_inline)) {
    const u32 so = kvbase + step * 32768u;
#pragma unroll
    for (int i = 0; i < 4; ++i) VR[s][i] = ld128<VAUX>(rv, vo16 + i * 1024, so);
    SB();
  };
  auto fill = [&](int s, int step) __attribute__((always_inline)) { fillk(s, step); fillv(s, step); };
  u32x4 wor[4], wdr[2];
  auto fillw = [&]() __attribute__((always_inline)) {
    const rsrc_t ro = mk(p.wo, H * DH * D * 2), rd = mk(p.wd, DF * D * 2);
    const u32 vo = (c < 10 && !(ABL & 2)) ? (128 * h + 16 * w + 4 * g) * (D * 2) + j * 160 + c * 16 : OOB;
    const u32 vd = (ffn && c < 10 && !(ABL & 2)) ? (64 * h + 8 * w + 2 * g) * (D * 2) + j * 160 + c * 16 : OOB;
#pragma unroll
    for (int i = 0; i < 4; ++i) wor[i] = ld128<AUX_NT>(ro, vo, i * (D * 2));
#pragma unroll
    for (int i = 0; i < 2; ++i) wdr[i] = ld128<AUX_NT>(rd, vd, i * (D * 2));
    SB();
  };
  Sel sl;
  {
    const int mm = lane & 7;
    const u32 hm = (mm & 1) ? 0xffff0000u : 0x0000ffffu;
    sl.k0 = (mm >> 1) == 0 ? hm : 0u; sl.k1 = (mm >> 1) == 1 ? hm : 0u; sl.k2 = (mm >> 1) == 2 ? hm : 0u; sl.k3 = (mm >> 1) == 3 ? hm : 0u;
  }

  if (!(ABL & 4) && (w == 0 || (w == 1 && ffn))) {
    u32x2 xs[5], xo[5];
    u32x4 wr[20];
    const rsrc_t rx = mk(p.x, 2 * D * 2);
#pragma unroll
    for (int i = 0; i < 5; ++i) xs[i] = ld64<0>(rx, (bb * D + 20 * (8 * g + (c & 7))) * 2 + i * 8, 0);
#pragma unroll
    for (int i = 0; i < 5; ++i) xo[i] = ld64<0>(rx, (bb * D + 80 * j + 20 * g) * 2 + i * 8, 0);
    const int ld = w == 0 ? H * DH : DF;
    const GAS u16* src = w == 0 ? (const GAS u16*)p.wq + h * DH + c * 8 : (const GAS u16*)(c < 8 ? p.wg : p.wu) + h * 64 + (c & 7) * 8;
    src += (80 * j + 20 * g) * ld;
#pragma unroll
    for (int i = 0; i < 20; ++i) wr[i] = __builtin_nontemporal_load((const GAS u32x4*)(src + i * ld));
    SB();
    __builtin_amdgcn_s_barrier();
    SB();
    float xv[20];
#pragma unroll
    for (int i = 0; i < 5; ++i) { xv[4 * i] = lo16(xs[i][0]); xv[4 * i + 1] = hi16(xs[i][0]); xv[4 * i + 2] = lo16(xs[i][1]); xv[4 * i + 3] = hi16(xs[i][1]); }
    float s = 0.f;
#pragma unroll
    for (int i = 0; i < 20; ++i) s += xv[i];
    s += dppf<XOR1>(s); s += dppf<XOR2>(s); s += dppf<HMIR>(s); s = xrow_sum(s);
    const float mean = s * (1.f / D);
    float q2 = 0.f;
#pragma unroll
    for (int i = 0; i < 20; ++i) { const float d = xv[i] - mean; q2 = fmaf(d, d, q2); }
    q2 += dppf<XOR1>(q2); q2 += dppf<XOR2>(q2); q2 += dppf<HMIR>(q2); q2 = xrow_sum(q2);
    const float mean_b = rbf(mean);
    const float rden = 1.f / rbf(__builtin_sqrtf(rbf(rbf(q2 * (1.f / D)) + 1e-5f)));
    STAMP(8);
    u32 xn[20];
#pragma unroll
    for (int i = 0; i < 5; ++i) {
      const float t0 = rbf(lo16(xo[i][0]) - mean_b) * rden, t1 = rbf(hi16(xo[i][0]) - mean_b) * rden, t2 = rbf(lo16(xo[i][1]) - mean_b) * rden, t3 = rbf(hi16(xo[i][1]) - mean_b) * rden;
      xn[4 * i] = pk2(t0, t0); xn[4 * i + 1] = pk2(t1, t1); xn[4 * i + 2] = pk2(t2, t2); xn[4 * i + 3] = pk2(t3, t3);
    }
    f32x4 a = {0.f, 0.f, 0.f, 0.f}, a2 = a;
#pragma unroll
    for (int i = 0; i < 20; i += 2) { a = mfma(sel(xn[i], sl), wr[i], a); a2 = mfma(sel(xn[i + 1], sl), wr[i + 1], a2); }
    a += a2;
    const u32 o = (w == 0 ? WS_QP : WS_F1) + pid * 2048 + lane * 16;
    st_granule2(rws, o, a[0], a[1], tag);
    st_granule2(rws, o + 1024, a[2], a[3], tag);
    STAMP(10);
    if (w == 0) {
      const u32 oq = WS_QP + h * 8 * 2048 + (lane >> 5) * 1024 + (32 * b + (lane & 31)) * 16;
      u32x4 qa[8];
      if (QSLEEP) __builtin_amdgcn_s_sleep(QSLEEP);
      for (u32 spins = 0; spins < SPIN_CAP; ++spins) {
        asm volatile("" ::: "memory");
#pragma unroll
        for (int pp = 0; pp < 8; ++pp) qa[pp] = ld128<AUX_SC1>(rws, oq + pp * 2048, 0);
        bool ok = true;
#pragma unroll
        for (int pp = 0; pp < 8; ++pp) ok = ok && qa[pp][1] == tag && qa[pp][3] == tag;
        if (__all(ok)) break;
        if (POLL0) __builtin_amdgcn_s_sleep(POLL0);
      }
      f32x2 q = {0.f, 0.f};
#pragma unroll
      for (int pp = 0; pp < 8; ++pp) { q[0] += __uint_as_float(qa[pp][0]); q[1] += __uint_as_float(qa[pp][2]); }
      ((u32*)lds)[L_Q + 4 * (lane & 15) + 2 * ((lane >> 4) & 1) + (lane >> 5)] = pk2(q[0], q[1]);
      STAMP(9);
      __syncthreads();
#pragma unroll
      for (int s = 0; s < SIF; ++s) fill(s, s);
    } else {
#pragma unroll
      for (int s = 0; s < SIF; ++s) fill(s, s);
      __syncthreads();
    }
  } else {
    __builtin_amdgcn_s_barrier();
    SB();
    fillk(0, 0); fillv(0, 0);
#pragma unroll
    for (int s = 1; s < SIF; ++s) {
      asm volatile("s_waitcnt vmcnt(%0)" ::"n"(PACE) : "memory"); fillk(s, s);
      asm volatile("s_waitcnt vmcnt(%0)" ::"n"(PACE) : "memory"); fillv(s, s);
    }
    __syncthreads();
  }
  STAMP(1); CSTAMP(14);
  const u32x4 qv = *(const u32x4*)&lds[L_Q + 4 * c];

  float m = -INFINITY, l = 0.f;
  f32x4 acc = {0.f, 0.f, 0.f, 0.f};
  const bool c3 = (lane & 8) != 0, c2 = (lane & 4) != 0;
  auto consume = [&](int s, int next) __attribute__((always_inline)) {
    float xd[4];
#pragma unroll
    for (int t = 0; t < 4; ++t) {
      float d = dot2(KR[s][t][0], qv[0], 0.f);
      d = dot2(KR[s][t][1], qv[1], d); d = dot2(KR[s][t][2], qv[2], d); xd[t] = dot2(KR[s][t][3], qv[3], d);
    }
    if (FINE && next >= 0) { asm volatile("" : "+v"(xd[0]), "+v"(xd[1]), "+v"(xd[2]), "+v"(xd[3])); SB(); fillk(s, next); }
    const float y0 = (c3 ? xd[2] : xd[0]) + dppf<ROR8>(c3 ? xd[0] : xd[2]);
    const float y1 = (c3 ? xd[3] : xd[1]) + dppf<ROR8>(c3 ? xd[1] : xd[3]);
    float sc = (c2 ? y1 : y0) + dppf<HMIR>(c2 ? y0 : y1);
    sc += dppf<XOR1>(sc); sc += dppf<XOR2>(sc);
    sc = rbf(rbf(sc) * SCALE);
    float pm = fmaxf(sc, dppf<ROR4>(sc)); pm = fmaxf(pm, dppf<ROR8>(pm)); pm = xrow_max(pm);
    const float mn = fmaxf(m, pm);
    const float al = __builtin_amdgcn_exp2f((m - mn) * LOG2E), pr = __builtin_amdgcn_exp2f((sc - mn) * LOG2E);
    m = mn; l = fmaf(l, al, pr);
    acc *= al;
    const u32 pp = pk2(pr, pr);
    acc = mfma(sel(dppu<NEWB + 0>(pp), sl), VR[s][0], acc);
    acc = mfma(sel(dppu<NEWB + 4>(pp), sl), VR[s][1], acc);
    acc = mfma(sel(dppu<NEWB + 8>(pp), sl), VR[s][2], acc);
    acc = mfma(sel(dppu<NEWB + 12>(pp), sl), VR[s][3], acc);
    SB();
    if (next >= 0) { if (!FINE) fillk(s, next); fillv(s, next); }
  };
  consume(0, SIF);
#pragma unroll
  for (int s = 1; s < SIF; ++s) consume(s, -1);
  STAMP(13);
  fillw();
#pragma unroll 1
  for (int t = SIF; t < nst - 1; ++t) consume(0, t + 1);
  const u32 of1 = ffn ? WS_F1 + (h * 8 + w) * 2048 + lane * 16 : OOB;
  u32x4 f0 = ld128<AUX_SC1>(rws, of1, 0), f1 = ld128<AUX_SC1>(rws, of1, 1024);
  SB();
  consume(0, -1);
  STAMP(3); CSTAMP(15);
  if (ffn && !(ABL & 4) && !__all(f0[1] == tag && f0[3] == tag && f1[1] == tag && f1[3] == tag)) {
    for (u32 spins = 0; spins < SPIN_CAP; ++spins) {
      __builtin_amdgcn_s_sleep(8);
      asm volatile("" ::: "memory");
      f0 = ld128<AUX_SC1>(rws, of1, 0); f1 = ld128<AUX_SC1>(rws, of1, 1024);
      if (__all(f0[1] == tag && f0[3] == tag && f1[1] == tag && f1[3] == tag)) break;
    }
  }

  {
    float ls = l; ls += dppf<XOR1>(ls); ls += dppf<XOR2>(ls); ls += dppf<ROR4>(ls); ls += dppf<ROR8>(ls); ls = xrow_sum(ls) * 0.25f;
    float* rec = &lds[L_REC + w * 132];
    if (lane < 32) *(f32x4*)&rec[4 + 8 * c + 4 * g] = acc;
    if (lane == 0) { rec[0] = m; rec[1] = ls; }
    u32* fb = (u32*)&lds[L_F1 + w * 256 + (g >> 1) * 128 + 8 * c + 4 * (g & 1)];
    *(u32x4*)fb = u32x4{f0[0], f0[2], f1[0], f1[2]};
  }
  __syncthreads();
  STAMP(4);
  if (tid == 448) { __builtin_amdgcn_raw_buffer_store_b32((u32)__builtin_amdgcn_s_memrealtime(), rws, WS_DIAG + bid * 16 + 8, 0, AUX_SC1); }
  if (!(ABL & 8)) {
  const int mj = j ^ w;
  const u32 o1 = WS_H1 + (h * 8 + mj) * H1_STRIDE;
  u32x4 ra, rml;
  if (w != 0) { ra = ld128<AUX_SC1>(rws, o1 + lane * 16, 0); rml = ld128<AUX_SC1>(rws, o1 + 1024, 0); }
  else {
    float mw[8], M = -INFINITY;
#pragma unroll
    for (int ww = 0; ww < 8; ++ww) { mw[ww] = lds[L_REC + ww * 132]; M = fmaxf(M, mw[ww]); }
    f32x2 o = {0.f, 0.f}; float L = 0.f;
#pragma unroll
    for (int ww = 0; ww < 8; ++ww) {
      const float e = __builtin_amdgcn_exp2f((mw[ww] - M) * LOG2E);
      o += *(const f32x2*)&lds[L_REC + ww * 132 + 4 + 2 * lane] * e; L = fmaf(e, lds[L_REC + ww * 132 + 1], L);
    }
    st_granule2(rws, o1 + lane * 16, o[0], o[1], tag);
    if (lane == 0) st_granule2(rws, o1 + 1024, M, L, tag);
    ra = u32x4{__float_as_uint(o[0]), tag, __float_as_uint(o[1]), tag}; rml = u32x4{__float_as_uint(M), tag, __float_as_uint(L), tag};
  }
  SB();
  {
    f32x4 af = {0.f, 0.f, 0.f, 0.f};
#pragma unroll
    for (int i = 0; i < 2; ++i) {
      float gs = 0.f, us = 0.f;
#pragma unroll
      for (int pp = 0; pp < 8; ++pp) { gs += lds[L_F1 + pp * 256 + bb * 128 + 8 * w + 2 * g + i]; us += lds[L_F1 + pp * 256 + bb * 128 + 64 + 8 * w + 2 * g + i]; }
      const float gg = rbf(gs), uu = rbf(us);
      const float z = 0.7978845608028654f * (gg + 0.044715f * gg * gg * gg);
      const float th = 1.f - 2.f / (__builtin_amdgcn_exp2f(z * (2.f * LOG2E)) + 1.f);
      const float hv = rbf(0.5f * gg * (1.f + th)) * uu;
      af = mfma(sel(pk2(hv, hv), sl), wdr[i], af);
    }
    *(f32x4*)&lds[L_FR + w * 256 + (g >> 1) * 128 + 8 * c + 4 * (g & 1)] = af;
  }
  asm volatile("s_waitcnt lgkmcnt(0)" ::: "memory");
  __builtin_amdgcn_s_barrier();
  asm volatile("" ::: "memory");
  bool have = true;
  if (w != 0) have = __all(ra[1] == tag && ra[3] == tag && rml[1] == tag && rml[3] == tag);
  SB();
  if (tid >= 64 && tid < 224) {
    const int k = tid - 64, bo = k >= 80, col = k - 80 * bo;
    float v = 0.f;
#pragma unroll
    for (int ww = 0; ww < 8; ++ww) v += lds[L_FR + ww * 256 + bo * 128 + col];
    st_granule(rws, WS_H2 + pid * H2_STRIDE + (160 + k) * 8, v, tag);
  }
  if (!have) {
    for (u32 spins = 0; spins < SPIN_CAP; ++spins) {
      __builtin_amdgcn_s_sleep(POLL1);
      asm volatile("" ::: "memory");
      rml = ld128<AUX_SC1>(rws, o1 + 1024, 0);
      if (rml[1] == tag && rml[3] == tag) {
        asm volatile("" ::: "memory");
        ra = ld128<AUX_SC1>(rws, o1 + lane * 16, 0);
        if (__all(ra[1] == tag && ra[3] == tag)) break;
      }
    }
  }
  {
    float* r1 = &lds[L_R1 + mj * 132];
    *(u32x2*)&r1[4 + 2 * lane] = u32x2{ra[0], ra[2]};
    if (lane == 0) *(u32x2*)&r1[0] = u32x2{rml[0], rml[2]};
  }
  __syncthreads();
  STAMP(5);
  {
    const float* r1 = &lds[L_R1 + bb * 4 * 132];
    const float m0 = r1[0], m1 = r1[132], m2 = r1[264], m3 = r1[396];
    const float M = fmaxf(fmaxf(m0, m1), fmaxf(m2, m3));
    const float e0 = __builtin_amdgcn_exp2f((m0 - M) * LOG2E), e1 = __builtin_amdgcn_exp2f((m1 - M) * LOG2E), e2 = __builtin_amdgcn_exp2f((m2 - M) * LOG2E), e3 = __builtin_amdgcn_exp2f((m3 - M) * LOG2E);
    const float L = e0 * r1[1] + e1 * r1[133] + e2 * r1[265] + e3 * r1[397];
    const int d0 = 4 + 16 * w + 4 * g;
    f32x4 o = *(const f32x4*)&r1[d0] * e0 + *(const f32x4*)&r1[132 + d0] * e1 + *(const f32x4*)&r1[264 + d0] * e2 + *(const f32x4*)&r1[396 + d0] * e3;
    o *= 1.f / L;
    f32x4 as = {0.f, 0.f, 0.f, 0.f};
#pragma unroll
    for (int i = 0; i < 4; ++i) as = mfma(sel(pk2(o[i], o[i]), sl), wor[i], as);
    *(f32x4*)&lds[L_SR + w * 256 + (g >> 1) * 128 + 8 * c + 4 * (g & 1)] = as;
  }
  __syncthreads();
  if (tid < 160) {
    const int bo = tid >= 80, col = tid - 80 * bo;
    float v = 0.f;
#pragma unroll
    for (int ww = 0; ww < 8; ++ww) v += lds[L_SR + ww * 256 + bo * 128 + col];
    st_granule(rws, WS_H2 + pid * H2_STRIDE + tid * 8, v, tag);
  }
  STAMP(6);
  if (w == 7 && h < 5) {
    const int k0 = 32 * h + 2 * c, bo = k0 >= 80, col = 80 * j + k0 - 80 * bo;
    const u32 xraw = *(const u32*)(p.x + bo * D + col);
    const u32 o2 = WS_H2 + (g * 8 + j) * H2_STRIDE + k0 * 8;
    u32x4 a[16];
    u32 need = 0xffffu;
    for (u32 spins = 0; spins < SPIN_CAP; ++spins) {
      asm volatile("" ::: "memory");
#pragma unroll
      for (int i = 0; i < 16; ++i) if (need >> i & 1) a[i] = ld128<AUX_SC1>(rws, o2 + (i & 7) * (32 * H2_STRIDE) + (i >> 3) * 1280, 0);
#pragma unroll
      for (int i = 0; i < 16; ++i) if ((need >> i & 1) && a[i][1] == tag && a[i][3] == tag) need &= ~(1u << i);
      if (!__any(need != 0)) break;
      __builtin_amdgcn_s_sleep(POLL2);
    }
    f32x2 S = {0.f, 0.f}, F = {0.f, 0.f};
#pragma unroll
    for (int i = 0; i < 8; ++i) { S += f32x2{__uint_as_float(a[i][0]), __uint_as_float(a[i][2])}; F += f32x2{__uint_as_float(a[8 + i][0]), __uint_as_float(a[8 + i][2])}; }
    const float s0 = rbf(xrow_sum(S[0])), s1 = rbf(xrow_sum(S[1])), f0 = rbf(xrow_sum(F[0])), f1 = rbf(xrow_sum(F[1]));
    const float g0 = rbf(f0 / (1.f + __builtin_amdgcn_exp2f(-f0 * LOG2E))), g1 = rbf(f1 / (1.f + __builtin_amdgcn_exp2f(-f1 * LOG2E)));
    if (g == 0) *(u32*)(p.out + bo * D + col) = pk2(lo16(xraw) + rbf(s0 * g0), hi16(xraw) + rbf(s1 * g1));
    if (bid == 0 && lane == 0) __builtin_amdgcn_raw_buffer_store_b32(tag, rws, WS_EPOCH, 0, AUX_SC1);
    STAMP(7);
    if (bid == 0) {
      const u32 tend = (u32)__builtin_amdgcn_s_memrealtime();
      int mnS = 1 << 30, mnE = 1 << 30, mxE = -(1 << 30), sA = 0, sB = 0;
      for (int i = 0; i < 4; ++i) {
        const u32x4 e = ld128<AUX_SC1>(rws, WS_DIAG + (lane + 64 * i) * 16, 0);
        const int ds = (int)(e[0] - dgs), de = (int)(e[2] - dgs);
        mnS = min(mnS, ds); mnE = min(mnE, de); mxE = max(mxE, de);
        const int x = lane & 7; if (x == 0 || x == 1 || x == 6 || x == 7) sA += de; else sB += de;
      }
      for (int o = 32; o >= 1; o >>= 1) { mnS = min(mnS, __shfl_xor(mnS, o)); mnE = min(mnE, __shfl_xor(mnE, o)); mxE = max(mxE, __shfl_xor(mxE, o)); sA += __shfl_xor(sA, o); sB += __shfl_xor(sB, o); }
      int meas = 0;
      if (DIAG == 4) meas = mxE - mnS;
      if (DIAG == 5) meas = mnE - mnS;
      if (DIAG == 6) meas = (int)(tend - dgs) - mnS;
      if (DIAG == 7) meas = sA / 128;
      if (DIAG == 8) meas = sB / 128;
      if (DIAG == 9) meas = (int)(((size_t)p.kc >> 12) & 0x1ff) * 10;
      if (DIAG == 10) meas = (int)(((size_t)p.vc >> 12) & 0x1ff) * 10;
      if (DIAG == 11) meas = (int)(((size_t)p.kc >> 21) & 0x7f) * 100;
      if (DIAG == 12) meas = (int)(((size_t)p.vc >> 21) & 0x7f) * 100;
      const u32 t1 = (u32)__builtin_amdgcn_s_memrealtime();
      const u32 dl = (u32)max(meas, 0) * DELAYK;
      while ((u32)__builtin_amdgcn_s_memrealtime() - t1 < dl) __builtin_amdgcn_s_sleep(1);
    }
  }
  }
}
}

extern "C" void kernel_launch(void* const* d_in, const int* in_sizes, int n_in, void* d_out, int out_size, void* d_ws, size_t ws_size, hipStream_t stream) {
  Params p;
  p.x = (const u16*)d_in[0]; p.kc = (const u16*)d_in[1]; p.vc = (const u16*)d_in[2]; p.wq = (const u16*)d_in[3]; p.wo = (const u16*)d_in[4];
  p.wg = (const u16*)d_in[6]; p.wu = (const u16*)d_in[7]; p.wd = (const u16*)d_in[8];
  p.out = (u16*)d_out; p.ws = (unsigned char*)d_ws;
  fused_decode<<<NWG, NT, 0, stream>>>(p);
}
```

```cpp
#include <hip/hip_runtime.h>
#include <stdint.h>

#define SIF 3
#define NE 266
#define LOCK 0
#define POLL0 1
#define POLL1 1
#define POLL2 1
#define KAUX 2
#define VAUX 2
#define PACE 63
#define QSLEEP 0
#define ABL 0
#define MAPV 0
#define FINE 1
#define AMAP 3
#define LOCKN 0
#define PRIO 0
#define EXTRA 0
#define WIN 4
#define STAG 0
#define ALIGN 0
#define ONELANE 1
#define STAMPS 0

namespace {
constexpr int D = 640, H = 32, SK = 16384, DH = 128, DF = 1728;
constexpr int NWG = 256, NT = 512;

typedef unsigned u32;
typedef unsigned long long u64;
typedef unsigned short u16;
typedef u32 u32x2 __attribute__((ext_vector_type(2)));
typedef u32 u32x4 __attribute__((ext_vector_type(4)));
typedef float f32x2 __attribute__((ext_vector_type(2)));
typedef float f32x4 __attribute__((ext_vector_type(4)));
typedef __bf16 bf16x2 __attribute__((ext_vector_type(2)));
typedef __bf16 bf16x8 __attribute__((ext_vector_type(8)));
typedef __amdgpu_buffer_rsrc_t rsrc_t;
#define GAS __attribute__((address_space(1)))

constexpr int AUX_NT = 2, AUX_SC1 = 16;
constexpr u32 OOB = 0x80000000u;

constexpr u32 WS_EPOCH = 0;
constexpr u32 WS_QP = 4096;
constexpr u32 WS_F1 = WS_QP + NWG * 2048;
constexpr u32 WS_H1 = WS_F1 + NWG * 2048;
constexpr u32 H1_STRIDE = 144 * 8;
constexpr u32 WS_H2 = WS_H1 + NWG * H1_STRIDE;
constexpr u32 H2_STRIDE = 320 * 8;
constexpr u32 WS_STAMP = WS_H2 + NWG * H2_STRIDE;
constexpr u32 WS_BYTES = WS_STAMP + NWG * 8 * 16 * 4;

constexpr int L_Q = 0;
constexpr int L_REC = L_Q + 64;
constexpr int L_F1 = L_REC + 8 * 132;
constexpr int L_R1 = L_F1 + 8 * 256;
constexpr int L_SR = L_R1 + 8 * 132;
constexpr int L_FR = L_SR + 8 * 256;
constexpr int L_FIN = L_FR + 8 * 256;
constexpr int L_USED = L_FIN + 8 * 64;
constexpr int L_END = L_USED;
static_assert(L_USED <= L_END, "");

constexpr float LOG2E = 1.4426950408889634f;
constexpr float SCALE = 0.08838834764831845f;
constexpr u32 SPIN_CAP = 1u << 18;

__device__ __forceinline__ float lo16(u32 v) { return __uint_as_float(v << 16); }
__device__ __forceinline__ float hi16(u32 v) { return __uint_as_float(v & 0xffff0000u); }
__device__ __forceinline__ u32 pk2(float a, float b) { bf16x2 r = {(__bf16)a, (__bf16)b}; return __builtin_bit_cast(u32, r); }
__device__ __forceinline__ float rbf(float a) { return lo16(pk2(a, a)); }
__device__ __forceinline__ float dot2(u32 a, u32 b, float c) { return __builtin_amdgcn_fdot2_f32_bf16(__builtin_bit_cast(bf16x2, a), __builtin_bit_cast(bf16x2, b), c, false); }
template <int CTRL> __device__ __forceinline__ float dppf(float x) { return __builtin_bit_cast(float, __builtin_amdgcn_mov_dpp(__builtin_bit_cast(int, x), CTRL, 0xf, 0xf, true)); }
template <int CTRL> __device__ __forceinline__ u32 dppu(u32 x) { return (u32)__builtin_amdgcn_mov_dpp((int)x, CTRL, 0xf, 0xf, true); }
constexpr int XOR1 = 0xB1, XOR2 = 0x4E, HMIR = 0x141, ROR4 = 0x124, ROR8 = 0x128, NEWB = 0x150;
__device__ __forceinline__ float xrow_max(float x) {
  auto s = __builtin_amdgcn_permlane16_swap(__float_as_uint(x), __float_as_uint(x), false, false);
  x = fmaxf(__uint_as_float(s[0]), __uint_as_float(s[1]));
  auto t = __builtin_amdgcn_permlane32_swap(__float_as_uint(x), __float_as_uint(x), false, false);
  return fmaxf(__uint_as_float(t[0]), __uint_as_float(t[1]));
}
__device__ __forceinline__ float xrow_sum(float x) {
  auto s = __builtin_amdgcn_permlane16_swap(__float_as_uint(x), __float_as_uint(x), false, false);
  x = __uint_as_float(s[0]) + __uint_as_float(s[1]);
  auto t = __builtin_amdgcn_permlane32_swap(__float_as_uint(x), __float_as_uint(x), false, false);
  return __uint_as_float(t[0]) + __uint_as_float(t[1]);
}
__device__ __forceinline__ f32x4 mfma(u32x4 a, u32x4 b, f32x4 c) {
  return __builtin_amdgcn_mfma_f32_16x16x32_bf16(__builtin_bit_cast(bf16x8, a), __builtin_bit_cast(bf16x8, b), c, 0, 0, 0);
}
__device__ __forceinline__ rsrc_t mk(const void* p, u32 bytes) { return __builtin_amdgcn_make_buffer_rsrc((void*)p, 0, bytes, 0x00020000); }
template <int AUX> __device__ __forceinline__ u32x4 ld128(rsrc_t r, u32 voff, u32 soff) { return __builtin_amdgcn_raw_buffer_load_b128(r, voff, soff, AUX); }
template <int AUX> __device__ __forceinline__ u32x2 ld64(rsrc_t r, u32 voff, u32 soff) { return __builtin_amdgcn_raw_buffer_load_b64(r, voff, soff, AUX); }
__device__ __forceinline__ void st_granule(rsrc_t r, u32 off, float v, u32 tag) { __builtin_amdgcn_raw_buffer_store_b64(u32x2{__float_as_uint(v), tag}, r, off, 0, AUX_SC1); }
__device__ __forceinline__ void st_granule2(rsrc_t r, u32 off, float v0, float v1, u32 tag) { __builtin_amdgcn_raw_buffer_store_b128(u32x4{__float_as_uint(v0), tag, __float_as_uint(v1), tag}, r, off, 0, AUX_SC1); }
#define SB() __builtin_amdgcn_sched_barrier(0)
#define STAMP(k) do { } while (0)
#define CSTAMP(k) do { } while (0)

struct Params {
  const u16 *x, *kc, *vc, *wq, *wo, *wg, *wu, *wd;
  u16* out;
  unsigned char* ws;
};

struct Sel { u32 k0, k1, k2, k3; };
__device__ __forceinline__ u32x4 sel(u32 pp, const Sel& s) { return u32x4{pp & s.k0, pp & s.k1, pp & s.k2, pp & s.k3}; }

__global__ void __launch_bounds__(NT, 2) fused_decode(Params p) {
  __shared__ __attribute__((aligned(16))) float lds[L_END];
  const int tid = threadIdx.x, lane = tid & 63, w = __builtin_amdgcn_readfirstlane(tid >> 6);
  const int g = lane >> 4, c = lane & 15, bb = c >> 3;
  const int bid = blockIdx.x, xc = bid & 7, jj = bid >> 3;
  const int xp = xc >> 1;
  const int par = xc & 1, b = (jj >> 1) & 1, half = jj & 1;
  const int h = MAPV == 0 ? xp + 4 * (jj >> 2) : MAPV == 1 ? (xp == 1 ? 3 : xp == 3 ? 1 : xp) + 4 * (jj >> 2) : MAPV == 2 ? ((xp + (jj >> 2)) & 3) + 4 * (jj >> 2) : ((xp ^ (jj >> 2)) & 3) + 4 * (jj >> 2);
  const int j = par + 2 * half + 4 * b;
  const int pid = h * 8 + j;
  const bool ffn = h < DF / 64;
  asm volatile("" ::: "v140");
  STAMP(0);
  const rsrc_t rws = mk(p.ws, WS_BYTES);
  const u32 tag = *(const __attribute__((address_space(4))) u32*)(p.ws + WS_EPOCH) + 0x9E3779B9u;

  const size_t slab = ((size_t)(b * H + h) * SK + half * (SK / 2)) * DH;
  const rsrc_t rk = mk(p.kc + slab, SK / 2 * DH * 2), rv = mk(p.vc + slab, SK / 2 * DH * 2);
  const int split = NE / 8 + (w < NE % 8 ? 1 : 0);
  const int nst = par ? 64 - split : split;
  const u32 kvbase = (par ? split : 0) * 32768u;
  u32 vo[4];
#pragma unroll
  for (int i = 0; i < 4; ++i)
    vo[i] = c * 16 + (AMAP == 0 ? w * 4096 + i * 1024 + g * 256
                    : AMAP == 1 ? g * 4096 + (i & 1) * 16384 + (i >> 1) * 256 + w * 512
                    : AMAP == 2 ? g * 8192 + (i & 1) * 4096 + (i >> 1) * 256 + w * 512
                    : AMAP == 3 ? g * 256 + i * 8192 + w * 1024
                    :             g * 256 + (i & 1) * 4096 + (i >> 1) * 16384 + (w & 3) * 1024 + (w >> 2) * 8192);
  u32x4 KR[SIF][4], VR[SIF][4];
  auto fillk = [&](int s, int step) __attribute__((always_inline)) {
    const u32 so = kvbase + step * 32768u;
#pragma unroll
    for (int i = 0; i < 4; ++i) KR[s][i] = ld128<KAUX>(rk, vo[i], so);
    SB();
  };
  auto fillv = [&](int s, int step) __attribute__((always_inline)) {
    const u32 so = kvbase + step * 32768u;
#pragma unroll
    for (int i = 0; i < 4; ++i) VR[s][i] = ld128<VAUX>(rv, vo[i], so);
    SB();
  };
  auto fill = [&](int s, int step) __attribute__((always_inline)) { fillk(s, step); fillv(s, step); };
  u32x4 wor[4], wdr[2];
  auto fillw = [&]() __attribute__((always_inline)) {
    const rsrc_t ro = mk(p.wo, H * DH * D * 2), rd = mk(p.wd, DF * D * 2);
    const u32 vo = (c < 10 && !(ABL & 2)) ? (128 * h + 16 * w + 4 * g) * (D * 2) + j * 160 + c * 16 : OOB;
    const u32 vd = (ffn && c < 10 && !(ABL & 2)) ? (64 * h + 8 * w + 2 * g) * (D * 2) + j * 160 + c * 16 : OOB;
#pragma unroll
    for (int i = 0; i < 4; ++i) wor[i] = ld128<AUX_NT>(ro, vo, i * (D * 2));
#pragma unroll
    for (int i = 0; i < 2; ++i) wdr[i] = ld128<AUX_NT>(rd, vd, i * (D * 2));
    SB();
  };
  Sel sl;
  {
    const int mm = lane & 7;
    const u32 hm = (mm & 1) ? 0xffff0000u : 0x0000ffffu;
    sl.k0 = (mm >> 1) == 0 ? hm : 0u; sl.k1 = (mm >> 1) == 1 ? hm : 0u; sl.k2 = (mm >> 1) == 2 ? hm : 0u; sl.k3 = (mm >> 1) == 3 ? hm : 0u;
  }

  if (!(ABL & 4) && (w == 0 || (w == 1 && ffn))) {
    u32x2 xs[5], xo[5];
    u32x4 wr[20];
    const rsrc_t rx = mk(p.x, 2 * D * 2);
#pragma unroll
    for (int i = 0; i < 5; ++i) xs[i] = ld64<0>(rx, (bb * D + 20 * (8 * g + (c & 7))) * 2 + i * 8, 0);
#pragma unroll
    for (int i = 0; i < 5; ++i) xo[i] = ld64<0>(rx, (bb * D + 80 * j + 20 * g) * 2 + i * 8, 0);
    const int ld = w == 0 ? H * DH : DF;
    const GAS u16* src = w == 0 ? (const GAS u16*)p.wq + h * DH + c * 8 : (const GAS u16*)(c < 8 ? p.wg : p.wu) + h * 64 + (c & 7) * 8;
    src += (80 * j + 20 * g) * ld;
#pragma unroll
    for (int i = 0; i < 20; ++i) wr[i] = __builtin_nontemporal_load((const GAS u32x4*)(src + i * ld));
    SB();
    __builtin_amdgcn_s_barrier();
    SB();
    float xv[20];
#pragma unroll
    for (int i = 0; i < 5; ++i) { xv[4 * i] = lo16(xs[i][0]); xv[4 * i + 1] = hi16(xs[i][0]); xv[4 * i + 2] = lo16(xs[i][1]); xv[4 * i + 3] = hi16(xs[i][1]); }
    float s = 0.f;
#pragma unroll
    for (int i = 0; i < 20; ++i) s += xv[i];
    s += dppf<XOR1>(s); s += dppf<XOR2>(s); s += dppf<HMIR>(s); s = xrow_sum(s);
    const float mean = s * (1.f / D);
    float q2 = 0.f;
#pragma unroll
    for (int i = 0; i < 20; ++i) { const float d = xv[i] - mean; q2 = fmaf(d, d, q2); }
    q2 += dppf<XOR1>(q2); q2 += dppf<XOR2>(q2); q2 += dppf<HMIR>(q2); q2 = xrow_sum(q2);
    const float mean_b = rbf(mean);
    const float rden = 1.f / rbf(__builtin_sqrtf(rbf(rbf(q2 * (1.f / D)) + 1e-5f)));
    STAMP(8);
    u32 xn[20];
#pragma unroll
    for (int i = 0; i < 5; ++i) {
      const float t0 = rbf(lo16(xo[i][0]) - mean_b) * rden, t1 = rbf(hi16(xo[i][0]) - mean_b) * rden, t2 = rbf(lo16(xo[i][1]) - mean_b) * rden, t3 = rbf(hi16(xo[i][1]) - mean_b) * rden;
      xn[4 * i] = pk2(t0, t0); xn[4 * i + 1] = pk2(t1, t1); xn[4 * i + 2] = pk2(t2, t2); xn[4 * i + 3] = pk2(t3, t3);
    }
    f32x4 a = {0.f, 0.f, 0.f, 0.f}, a2 = a;
#pragma unroll
    for (int i = 0; i < 20; i += 2) { a = mfma(sel(xn[i], sl), wr[i], a); a2 = mfma(sel(xn[i + 1], sl), wr[i + 1], a2); }
    a += a2;
    const u32 o = (w == 0 ? WS_QP : WS_F1) + pid * 2048 + lane * 16;
    st_granule2(rws, o, a[0], a[1], tag);
    st_granule2(rws, o + 1024, a[2], a[3], tag);
    STAMP(10);
    if (w == 0) {
      const u32 oq = WS_QP + h * 8 * 2048 + (lane >> 5) * 1024 + (32 * b + (lane & 31)) * 16;
      u32x4 qa[8];
      if (QSLEEP) __builtin_amdgcn_s_sleep(QSLEEP);
      for (u32 spins = 0; spins < SPIN_CAP; ++spins) {
        asm volatile("" ::: "memory");
#pragma unroll
        for (int pp = 0; pp < 8; ++pp) qa[pp] = ld128<AUX_SC1>(rws, oq + pp * 2048, 0);
        bool ok = true;
#pragma unroll
        for (int pp = 0; pp < 8; ++pp) ok = ok && qa[pp][1] == tag && qa[pp][3] == tag;
        if (__all(ok)) break;
        if (POLL0) __builtin_amdgcn_s_sleep(POLL0);
      }
      f32x2 q = {0.f, 0.f};
#pragma unroll
      for (int pp = 0; pp < 8; ++pp) { q[0] += __uint_as_float(qa[pp][0]); q[1] += __uint_as_float(qa[pp][2]); }
      ((u32*)lds)[L_Q + 4 * (lane & 15) + 2 * ((lane >> 4) & 1) + (lane >> 5)] = pk2(q[0], q[1]);
      STAMP(9);
      __syncthreads();
#pragma unroll
      for (int s = 0; s < SIF; ++s) fill(s, s);
    } else {
#pragma unroll
      for (int s = 0; s < SIF; ++s) fill(s, s);
      __syncthreads();
    }
  } else {
    __builtin_amdgcn_s_barrier();
    SB();
    fillk(0, 0); fillv(0, 0);
#pragma unroll
    for (int s = 1; s < SIF; ++s) {
      asm volatile("s_waitcnt vmcnt(%0)" ::"n"(PACE) : "memory"); fillk(s, s);
      asm volatile("s_waitcnt vmcnt(%0)" ::"n"(PACE) : "memory"); fillv(s, s);
    }
    __syncthreads();
  }
  STAMP(1); CSTAMP(14);
  const u32x4 qv = *(const u32x4*)&lds[L_Q + 4 * c];

  float m = -INFINITY, l = 0.f;
  f32x4 acc = {0.f, 0.f, 0.f, 0.f};
  const bool c3 = (lane & 8) != 0, c2 = (lane & 4) != 0;
  auto ldk = [&](int s, int i, int step) __attribute__((always_inline)) { KR[s][i] = ld128<KAUX>(rk, vo[i], kvbase + step * 32768u); };
  auto ldv = [&](int s, int i, int step) __attribute__((always_inline)) { VR[s][i] = ld128<VAUX>(rv, vo[i], kvbase + step * 32768u); };
  auto consume = [&](int s, int next, int cur = -1) __attribute__((always_inline)) {
    float xd[4];
#pragma unroll
    for (int t = 0; t < 4; ++t) {
      float d = dot2(KR[s][t][0], qv[0], 0.f);
      d = dot2(KR[s][t][1], qv[1], d); d = dot2(KR[s][t][2], qv[2], d); xd[t] = dot2(KR[s][t][3], qv[3], d);
    }
    asm volatile("" : "+v"(xd[0]), "+v"(xd[1]), "+v"(xd[2]), "+v"(xd[3])); SB();
    if (WIN == 8) { if (next >= 0) fillk(s, next); }
    if (WIN == 4) { if (cur >= 0) fillv(s, cur); }
    if (WIN == 6) { if (cur >= 0) { ldv(s, 2, cur); ldv(s, 3, cur); } if (next >= 0) { ldk(s, 0, next); ldk(s, 1, next); } SB(); }
    const float y0 = (c3 ? xd[2] : xd[0]) + dppf<ROR8>(c3 ? xd[0] : xd[2]);
    const float y1 = (c3 ? xd[3] : xd[1]) + dppf<ROR8>(c3 ? xd[1] : xd[3]);
    float sc = (c2 ? y1 : y0) + dppf<HMIR>(c2 ? y0 : y1);
    sc += dppf<XOR1>(sc); sc += dppf<XOR2>(sc);
    sc = rbf(rbf(sc) * SCALE);
    float pm = fmaxf(sc, dppf<ROR4>(sc)); pm = fmaxf(pm, dppf<ROR8>(pm)); pm = xrow_max(pm);
    const float mn = fmaxf(m, pm);
    const float al = __builtin_amdgcn_exp2f((m - mn) * LOG2E), pr = __builtin_amdgcn_exp2f((sc - mn) * LOG2E);
    m = mn; l = fmaf(l, al, pr);
    acc *= al;
    const u32 pp = pk2(pr, pr);
    acc = mfma(sel(dppu<NEWB + 0>(pp), sl), VR[s][0], acc);
    acc = mfma(sel(dppu<NEWB + 4>(pp), sl), VR[s][1], acc);
    acc = mfma(sel(dppu<NEWB + 8>(pp), sl), VR[s][2], acc);
    acc = mfma(sel(dppu<NEWB + 12>(pp), sl), VR[s][3], acc);
    SB();
    if (next >= 0) {
      if (WIN == 8) fillv(s, next);
      if (WIN == 4) fillk(s, next);
      if (WIN == 6) { ldk(s, 2, next); ldk(s, 3, next); ldv(s, 0, next); ldv(s, 1, next); SB(); }
    }
  };
  if (PRIO == 2 && w >= 4) __builtin_amdgcn_s_setprio(1);
  if (PRIO == 3) { if (w >= 6) __builtin_amdgcn_s_setprio(3); else if (w >= 4) __builtin_amdgcn_s_setprio(2); else if (w >= 2) __builtin_amdgcn_s_setprio(1); }
  consume(0, SIF);
#pragma unroll
  for (int s = 1; s < SIF; ++s) consume(s, -1);
  STAMP(13);
  fillw();
#pragma unroll 1
  for (int t = SIF; t < nst - 1; ++t) {
    if (PRIO == 1) { if ((t + (w >> 2)) & 1) __builtin_amdgcn_s_setprio(1); else __builtin_amdgcn_s_setprio(0); }
    consume(0, t + 1, t);
  }
  const u32 of1 = ffn ? WS_F1 + (h * 8 + w) * 2048 + lane * 16 : OOB;
  u32x4 f0 = ld128<AUX_SC1>(rws, of1, 0), f1 = ld128<AUX_SC1>(rws, of1, 1024);
  const int fk0 = 32 * (h < 5 ? h : 0) + 2 * c, fbo = fk0 >= 80, fcol = 80 * j + fk0 - 80 * fbo;
  const u32 xraw = __builtin_amdgcn_raw_buffer_load_b32(mk(p.x, 2 * D * 2), (fbo * D + fcol) * 2, 0, 0);
  SB();
  consume(0, -1, nst - 1);
  STAMP(3); CSTAMP(15);
  if (ffn && !(ABL & 4) && !__all(f0[1] == tag && f0[3] == tag && f1[1] == tag && f1[3] == tag)) {
    for (u32 spins = 0; spins < SPIN_CAP; ++spins) {
      __builtin_amdgcn_s_sleep(8);
      asm volatile("" ::: "memory");
      f0 = ld128<AUX_SC1>(rws, of1, 0); f1 = ld128<AUX_SC1>(rws, of1, 1024);
      if (__all(f0[1] == tag && f0[3] == tag && f1[1] == tag && f1[3] == tag)) break;
    }
  }

  {
    float ls = l; ls += dppf<XOR1>(ls); ls += dppf<XOR2>(ls); ls += dppf<ROR4>(ls); ls += dppf<ROR8>(ls); ls = xrow_sum(ls) * 0.25f;
    float* rec = &lds[L_REC + w * 132];
    if (lane < 32) *(f32x4*)&rec[4 + 8 * c + 4 * g] = acc;
    if (lane == 0) { rec[0] = m; rec[1] = ls; }
    u32* fb = (u32*)&lds[L_F1 + w * 256 + (g >> 1) * 128 + 8 * c + 4 * (g & 1)];
    *(u32x4*)fb = u32x4{f0[0], f0[2], f1[0], f1[2]};
  }
  __syncthreads();
  STAMP(4);
  if (!(ABL & 8)) {
  auto ffn2 = [&]() __attribute__((always_inline)) {
    f32x4 af = {0.f, 0.f, 0.f, 0.f};
#pragma unroll
    for (int i = 0; i < 2; ++i) {
      float gs = 0.f, us = 0.f;
#pragma unroll
      for (int pp = 0; pp < 8; ++pp) { gs += lds[L_F1 + pp * 256 + bb * 128 + 8 * w + 2 * g + i]; us += lds[L_F1 + pp * 256 + bb * 128 + 64 + 8 * w + 2 * g + i]; }
      const float gg = rbf(gs), uu = rbf(us);
      const float z = 0.7978845608028654f * (gg + 0.044715f * gg * gg * gg);
      const float th = 1.f - 2.f / (__builtin_amdgcn_exp2f(z * (2.f * LOG2E)) + 1.f);
      const float hv = rbf(0.5f * gg * (1.f + th)) * uu;
      af = mfma(sel(pk2(hv, hv), sl), wdr[i], af);
    }
    *(f32x4*)&lds[L_FR + w * 256 + (g >> 1) * 128 + 8 * c + 4 * (g & 1)] = af;
    asm volatile("s_waitcnt lgkmcnt(0)" ::: "memory");
    __builtin_amdgcn_s_barrier();
    asm volatile("" ::: "memory");
  };
  if (w == 0) {
    float mw[8], M = -INFINITY;
#pragma unroll
    for (int ww = 0; ww < 8; ++ww) { mw[ww] = lds[L_REC + ww * 132]; M = fmaxf(M, mw[ww]); }
    f32x2 o = {0.f, 0.f}; float L = 0.f;
#pragma unroll
    for (int ww = 0; ww < 8; ++ww) {
      const float e = __builtin_amdgcn_exp2f((mw[ww] - M) * LOG2E);
      o += *(const f32x2*)&lds[L_REC + ww * 132 + 4 + 2 * lane] * e; L = fmaf(e, lds[L_REC + ww * 132 + 1], L);
    }
    const u32 o1 = WS_H1 + pid * H1_STRIDE;
    st_granule2(rws, o1 + lane * 16, o[0], o[1], tag);
    st_granule2(rws, lane == 0 ? o1 + 1024 : OOB, M, L, tag);
    float* r1 = &lds[L_R1 + j * 132];
    *(f32x2*)&r1[4 + 2 * lane] = o;
    if (lane == 0) *(f32x2*)&r1[0] = f32x2{M, L};
    SB();
    ffn2();
  } else {
    const int mj = j ^ w;
    const u32 o1 = WS_H1 + (h * 8 + mj) * H1_STRIDE;
    const u32 oml = lane == 0 ? o1 + 1024 : OOB;
    u32x4 ra = ld128<AUX_SC1>(rws, o1 + lane * 16, 0), rml = ld128<AUX_SC1>(rws, oml, 0);
    SB();
    ffn2();
    const bool have = __all((ra[1] == tag) & (ra[3] == tag) & ((lane != 0) | ((rml[1] == tag) & (rml[3] == tag))));
    SB();
    if (w < 4 && tid - 64 < 160) {
      const int k = tid - 64, bo = k >= 80, col = k - 80 * bo;
      float v = 0.f;
#pragma unroll
      for (int ww = 0; ww < 8; ++ww) v += lds[L_FR + ww * 256 + bo * 128 + col];
      st_granule(rws, WS_H2 + pid * H2_STRIDE + (160 + k) * 8, v, tag);
    }
    if (!have && !(ABL & 16)) {
      for (u32 spins = 0; spins < SPIN_CAP; ++spins) {
        if (POLL1) __builtin_amdgcn_s_sleep(POLL1);
        asm volatile("" ::: "memory");
        rml = ld128<AUX_SC1>(rws, oml, 0);
        if (__builtin_amdgcn_readfirstlane(rml[1]) == tag && __builtin_amdgcn_readfirstlane(rml[3]) == tag) {
          asm volatile("" ::: "memory");
          ra = ld128<AUX_SC1>(rws, o1 + lane * 16, 0);
          if (__all(ra[1] == tag && ra[3] == tag)) break;
        }
      }
    }
    float* r1 = &lds[L_R1 + mj * 132];
    *(u32x2*)&r1[4 + 2 * lane] = u32x2{ra[0], ra[2]};
    if (lane == 0) *(u32x2*)&r1[0] = u32x2{rml[0], rml[2]};
  }
  __syncthreads();
  STAMP(5);
  {
    const float* r1 = &lds[L_R1 + bb * 4 * 132];
    const float m0 = r1[0], m1 = r1[132], m2 = r1[264], m3 = r1[396];
    const float M = fmaxf(fmaxf(m0, m1), fmaxf(m2, m3));
    const float e0 = __builtin_amdgcn_exp2f((m0 - M) * LOG2E), e1 = __builtin_amdgcn_exp2f((m1 - M) * LOG2E), e2 = __builtin_amdgcn_exp2f((m2 - M) * LOG2E), e3 = __builtin_amdgcn_exp2f((m3 - M) * LOG2E);
    const float L = e0 * r1[1] + e1 * r1[133] + e2 * r1[265] + e3 * r1[397];
    const int d0 = 4 + 16 * w + 4 * g;
    f32x4 o = *(const f32x4*)&r1[d0] * e0 + *(const f32x4*)&r1[132 + d0] * e1 + *(const f32x4*)&r1[264 + d0] * e2 + *(const f32x4*)&r1[396 + d0] * e3;
    o *= 1.f / L;
    f32x4 as = {0.f, 0.f, 0.f, 0.f};
#pragma unroll
    for (int i = 0; i < 4; ++i) as = mfma(sel(pk2(o[i], o[i]), sl), wor[i], as);
    *(f32x4*)&lds[L_SR + w * 256 + (g >> 1) * 128 + 8 * c + 4 * (g & 1)] = as;
  }
  __syncthreads();
  if (tid < 160) {
    const int bo = tid >= 80, col = tid - 80 * bo;
    float v = 0.f;
#pragma unroll
    for (int ww = 0; ww < 8; ++ww) v += lds[L_SR + ww * 256 + bo * 128 + col];
    st_granule(rws, WS_H2 + pid * H2_STRIDE + tid * 8, v, tag);
  }
  STAMP(6);
  if (h < 5 && !(ABL & 32)) {
    const u32 o2 = WS_H2 + ((4 * w + g) * 8 + j) * H2_STRIDE + fk0 * 8;
    u32x4 aS = {0u, 0u, 0u, 0u}, aF = aS;
    bool nS = true, nF = true;
    for (u32 spins = 0; spins < SPIN_CAP; ++spins) {
      asm volatile("" ::: "memory");
      if (nS) aS = ld128<AUX_SC1>(rws, o2, 0);
      if (nF) aF = ld128<AUX_SC1>(rws, o2 + 1280, 0);
      nS = !(aS[1] == tag && aS[3] == tag); nF = !(aF[1] == tag && aF[3] == tag);
      if (!__any(nS || nF)) break;
      if (POLL2) __builtin_amdgcn_s_sleep(POLL2);
    }
    const f32x4 part = {xrow_sum(__uint_as_float(aS[0])), xrow_sum(__uint_as_float(aS[2])), xrow_sum(__uint_as_float(aF[0])), xrow_sum(__uint_as_float(aF[2]))};
    if (g == 0) *(f32x4*)&lds[L_FIN + w * 64 + 4 * c] = part;
    __syncthreads();
    if (w == 7) {
      f32x4 t = {0.f, 0.f, 0.f, 0.f};
#pragma unroll
      for (int ww = 0; ww < 8; ++ww) t += *(const f32x4*)&lds[L_FIN + ww * 64 + 4 * c];
      const float s0 = rbf(t[0]), s1 = rbf(t[1]), f0 = rbf(t[2]), f1 = rbf(t[3]);
      const float g0 = rbf(f0 / (1.f + __builtin_amdgcn_exp2f(-f0 * LOG2E))), g1 = rbf(f1 / (1.f + __builtin_amdgcn_exp2f(-f1 * LOG2E)));
      if (g == 0) *(u32*)(p.out + fbo * D + fcol) = pk2(lo16(xraw) + rbf(s0 * g0), hi16(xraw) + rbf(s1 * g1));
      if (bid == 0 && lane == 0) __builtin_amdgcn_raw_buffer_store_b32(tag, rws, WS_EPOCH, 0, AUX_SC1);
      STAMP(7);
    }
  }
  }
}
}

extern "C" void kernel_launch(void* const* d_in, const int* in_sizes, int n_in, void* d_out, int out_size, void* d_ws, size_t ws_size, hipStream_t stream) {
  Params p;
  p.x = (const u16*)d_in[0]; p.kc = (const u16*)d_in[1]; p.vc = (const u16*)d_in[2]; p.wq = (const u16*)d_in[3]; p.wo = (const u16*)d_in[4];
  p.wg = (const u16*)d_in[6]; p.wu = (const u16*)d_in[7]; p.wd = (const u16*)d_in[8];
  p.out = (u16*)d_out; p.ws = (unsigned char*)d_ws;
  fused_decode<<<NWG, NT, 0, stream>>>(p);
}
```

```cpp
#include <hip/hip_runtime.h>
#include <stdint.h>

#define SIF 3
#define NE 266
#define LOCK 0
#define POLL0 1
#define POLL1 1
#define POLL2 1
#define KAUX 2
#define VAUX 2
#define PACE 63
#define QSLEEP 0
#define ABL 0
#define MAPV 0
#define FINE 1
#define AMAP 3
#define LOCKN 0
#define PRIO 0
#define EXTRA 0
#define WIN 4
#define STAG 0
#define ALIGN 0
#define ONELANE 1
#define DIAG 7
#define DELAYK 1
#define STAMPS 0

namespace {
constexpr int D = 640, H = 32, SK = 16384, DH = 128, DF = 1728;
constexpr int NWG = 256, NT = 512;

typedef unsigned u32;
typedef unsigned long long u64;
typedef unsigned short u16;
typedef u32 u32x2 __attribute__((ext_vector_type(2)));
typedef u32 u32x4 __attribute__((ext_vector_type(4)));
typedef float f32x2 __attribute__((ext_vector_type(2)));
typedef float f32x4 __attribute__((ext_vector_type(4)));
typedef __bf16 bf16x2 __attribute__((ext_vector_type(2)));
typedef __bf16 bf16x8 __attribute__((ext_vector_type(8)));
typedef __amdgpu_buffer_rsrc_t rsrc_t;
#define GAS __attribute__((address_space(1)))

constexpr int AUX_NT = 2, AUX_SC1 = 16;
constexpr u32 OOB = 0x80000000u;

constexpr u32 WS_EPOCH = 0;
constexpr u32 WS_QP = 4096;
constexpr u32 WS_F1 = WS_QP + NWG * 2048;
constexpr u32 WS_H1 = WS_F1 + NWG * 2048;
constexpr u32 H1_STRIDE = 144 * 8;
constexpr u32 WS_H2 = WS_H1 + NWG * H1_STRIDE;
constexpr u32 H2_STRIDE = 320 * 8;
constexpr u32 WS_STAMP = WS_H2 + NWG * H2_STRIDE;
constexpr u32 WS_DIAG = WS_STAMP + NWG * 8 * 16 * 4;
constexpr u32 WS_BYTES = WS_DIAG + NWG * 32;

constexpr int L_Q = 0;
constexpr int L_REC = L_Q + 64;
constexpr int L_F1 = L_REC + 8 * 132;
constexpr int L_R1 = L_F1 + 8 * 256;
constexpr int L_SR = L_R1 + 8 * 132;
constexpr int L_FR = L_SR + 8 * 256;
constexpr int L_FIN = L_FR + 8 * 256;
constexpr int L_USED = L_FIN + 8 * 64;
constexpr int L_END = L_USED;

constexpr float LOG2E = 1.4426950408889634f;
constexpr float SCALE = 0.08838834764831845f;
constexpr u32 SPIN_CAP = 1u << 18;

__device__ __forceinline__ float lo16(u32 v) { return __uint_as_float(v << 16); }
__device__ __forceinline__ float hi16(u32 v) { return __uint_as_float(v & 0xffff0000u); }
__device__ __forceinline__ u32 pk2(float a, float b) { bf16x2 r = {(__bf16)a, (__bf16)b}; return __builtin_bit_cast(u32, r); }
__device__ __forceinline__ float rbf(float a) { return lo16(pk2(a, a)); }
__device__ __forceinline__ float dot2(u32 a, u32 b, float c) { return __builtin_amdgcn_fdot2_f32_bf16(__builtin_bit_cast(bf16x2, a), __builtin_bit_cast(bf16x2, b), c, false); }
template <int CTRL> __device__ __forceinline__ float dppf(float x) { return __builtin_bit_cast(float, __builtin_amdgcn_mov_dpp(__builtin_bit_cast(int, x), CTRL, 0xf, 0xf, true)); }
template <int CTRL> __device__ __forceinline__ u32 dppu(u32 x) { return (u32)__builtin_amdgcn_mov_dpp((int)x, CTRL, 0xf, 0xf, true); }
constexpr int XOR1 = 0xB1, XOR2 = 0x4E, HMIR = 0x141, ROR4 = 0x124, ROR8 = 0x128, NEWB = 0x150;
__device__ __forceinline__ float xrow_max(float x) {
  auto s = __builtin_amdgcn_permlane16_swap(__float_as_uint(x), __float_as_uint(x), false, false);
  x = fmaxf(__uint_as_float(s[0]), __uint_as_float(s[1]));
  auto t = __builtin_amdgcn_permlane32_swap(__float_as_uint(x), __float_as_uint(x), false, false);
  return fmaxf(__uint_as_float(t[0]), __uint_as_float(t[1]));
}
__device__ __forceinline__ float xrow_sum(float x) {
  auto s = __builtin_amdgcn_permlane16_swap(__float_as_uint(x), __float_as_uint(x), false, false);
  x = __uint_as_float(s[0]) + __uint_as_float(s[1]);
  auto t = __builtin_amdgcn_permlane32_swap(__float_as_uint(x), __float_as_uint(x), false, false);
  return __uint_as_float(t[0]) + __uint_as_float(t[1]);
}
__device__ __forceinline__ f32x4 mfma(u32x4 a, u32x4 b, f32x4 c) {
  return __builtin_amdgcn_mfma_f32_16x16x32_bf16(__builtin_bit_cast(bf16x8, a), __builtin_bit_cast(bf16x8, b), c, 0, 0, 0);
}
__device__ __forceinline__ rsrc_t mk(const void* p, u32 bytes) { return __builtin_amdgcn_make_buffer_rsrc((void*)p, 0, bytes, 0x00020000); }
template <int AUX> __device__ __forceinline__ u32x4 ld128(rsrc_t r, u32 voff, u32 soff) { return __builtin_amdgcn_raw_buffer_load_b128(r, voff, soff, AUX); }
template <int AUX> __device__ __forceinline__ u32x2 ld64(rsrc_t r, u32 voff, u32 soff) { return __builtin_amdgcn_raw_buffer_load_b64(r, voff, soff, AUX); }
__device__ __forceinline__ void st_granule(rsrc_t r, u32 off, float v, u32 tag) { __builtin_amdgcn_raw_buffer_store_b64(u32x2{__float_as_uint(v), tag}, r, off, 0, AUX_SC1); }
__device__ __forceinline__ void st_granule2(rsrc_t r, u32 off, float v0, float v1, u32 tag) { __builtin_amdgcn_raw_buffer_store_b128(u32x4{__float_as_uint(v0), tag, __float_as_uint(v1), tag}, r, off, 0, AUX_SC1); }
#define SB() __builtin_amdgcn_sched_barrier(0)
#define STAMP(k) do { } while (0)
#define CSTAMP(k) do { } while (0)

struct Params {
  const u16 *x, *kc, *vc, *wq, *wo, *wg, *wu, *wd;
  u16* out;
  unsigned char* ws;
};

struct Sel { u32 k0, k1, k2, k3; };
__device__ __forceinline__ u32x4 sel(u32 pp, const Sel& s) { return u32x4{pp & s.k0, pp & s.k1, pp & s.k2, pp & s.k3}; }

__global__ void __launch_bounds__(NT, 2) fused_decode(Params p) {
  __shared__ __attribute__((aligned(16))) float lds[L_END];
  const int tid = threadIdx.x, lane = tid & 63, w = __builtin_amdgcn_readfirstlane(tid >> 6);
  const int g = lane >> 4, c = lane & 15, bb = c >> 3;
  const int bid = blockIdx.x, xc = bid & 7, jj = bid >> 3;
  const int xp = xc >> 1;
  const int par = xc & 1, b = (jj >> 1) & 1, half = jj & 1;
  const int h = MAPV == 0 ? xp + 4 * (jj >> 2) : MAPV == 1 ? (xp == 1 ? 3 : xp == 3 ? 1 : xp) + 4 * (jj >> 2) : MAPV == 2 ? ((xp + (jj >> 2)) & 3) + 4 * (jj >> 2) : ((xp ^ (jj >> 2)) & 3) + 4 * (jj >> 2);
  const int j = par + 2 * half + 4 * b;
  const int pid = h * 8 + j;
  const bool ffn = h < DF / 64;
  asm volatile("" ::: "v140");
  STAMP(0);
  const rsrc_t rws = mk(p.ws, WS_BYTES);
  const u32 dgs = (u32)__builtin_amdgcn_s_memrealtime();
  u32 dgq = 0, dg13 = 0;
  const u32 tag = *(const __attribute__((address_space(4))) u32*)(p.ws + WS_EPOCH) + 0x9E3779B9u;

  const size_t slab = ((size_t)(b * H + h) * SK + half * (SK / 2)) * DH;
  const rsrc_t rk = mk(p.kc + slab, SK / 2 * DH * 2), rv = mk(p.vc + slab, SK / 2 * DH * 2);
  const int split = NE / 8 + (w < NE % 8 ? 1 : 0);
  const int nst = par ? 64 - split : split;
  const u32 kvbase = (par ? split : 0) * 32768u;
  u32 vo[4];
#pragma unroll
  for (int i = 0; i < 4; ++i)
    vo[i] = c * 16 + (AMAP == 0 ? w * 4096 + i * 1024 + g * 256
                    : AMAP == 1 ? g * 4096 + (i & 1) * 16384 + (i >> 1) * 256 + w * 512
                    : AMAP == 2 ? g * 8192 + (i & 1) * 4096 + (i >> 1) * 256 + w * 512
                    : AMAP == 3 ? g * 256 + i * 8192 + w * 1024
                    :             g * 256 + (i & 1) * 4096 + (i >> 1) * 16384 + (w & 3) * 1024 + (w >> 2) * 8192);
  u32x4 KR[SIF][4], VR[SIF][4];
  auto fillk = [&](int s, int step) __attribute__((always_inline)) {
    const u32 so = kvbase + step * 32768u;
#pragma unroll
    for (int i = 0; i < 4; ++i) KR[s][i] = ld128<KAUX>(rk, vo[i], so);
    SB();
  };
  auto fillv = [&](int s, int step) __attribute__((always_inline)) {
    const u32 so = kvbase + step * 32768u;
#pragma unroll
    for (int i = 0; i < 4; ++i) VR[s][i] = ld128<VAUX>(rv, vo[i], so);
    SB();
  };
  auto fill = [&](int s, int step) __attribute__((always_inline)) { fillk(s, step); fillv(s, step); };
  u32x4 wor[4], wdr[2];
  auto fillw = [&]() __attribute__((always_inline)) {
    const rsrc_t ro = mk(p.wo, H * DH * D * 2), rd = mk(p.wd, DF * D * 2);
    const u32 vo = (c < 10 && !(ABL & 2)) ? (128 * h + 16 * w + 4 * g) * (D * 2) + j * 160 + c * 16 : OOB;
    const u32 vd = (ffn && c < 10 && !(ABL & 2)) ? (64 * h + 8 * w + 2 * g) * (D * 2) + j * 160 + c * 16 : OOB;
#pragma unroll
    for (int i = 0; i < 4; ++i) wor[i] = ld128<AUX_NT>(ro, vo, i * (D * 2));
#pragma unroll
    for (int i = 0; i < 2; ++i) wdr[i] = ld128<AUX_NT>(rd, vd, i * (D * 2));
    SB();
  };
  Sel sl;
  {
    const int mm = lane & 7;
    const u32 hm = (mm & 1) ? 0xffff0000u : 0x0000ffffu;
    sl.k0 = (mm >> 1) == 0 ? hm : 0u; sl.k1 = (mm >> 1) == 1 ? hm : 0u; sl.k2 = (mm >> 1) == 2 ? hm : 0u; sl.k3 = (mm >> 1) == 3 ? hm : 0u;
  }

  if (!(ABL & 4) && (w == 0 || (w == 1 && ffn))) {
    u32x2 xs[5], xo[5];
    u32x4 wr[20];
    const rsrc_t rx = mk(p.x, 2 * D * 2);
#pragma unroll
    for (int i = 0; i < 5; ++i) xs[i] = ld64<0>(rx, (bb * D + 20 * (8 * g + (c & 7))) * 2 + i * 8, 0);
#pragma unroll
    for (int i = 0; i < 5; ++i) xo[i] = ld64<0>(rx, (bb * D + 80 * j + 20 * g) * 2 + i * 8, 0);
    const int ld = w == 0 ? H * DH : DF;
    const GAS u16* src = w == 0 ? (const GAS u16*)p.wq + h * DH + c * 8 : (const GAS u16*)(c < 8 ? p.wg : p.wu) + h * 64 + (c & 7) * 8;
    src += (80 * j + 20 * g) * ld;
#pragma unroll
    for (int i = 0; i < 20; ++i) wr[i] = __builtin_nontemporal_load((const GAS u32x4*)(src + i * ld));
    SB();
    __builtin_amdgcn_s_barrier();
    SB();
    float xv[20];
#pragma unroll
    for (int i = 0; i < 5; ++i) { xv[4 * i] = lo16(xs[i][0]); xv[4 * i + 1] = hi16(xs[i][0]); xv[4 * i + 2] = lo16(xs[i][1]); xv[4 * i + 3] = hi16(xs[i][1]); }
    float s = 0.f;
#pragma unroll
    for (int i = 0; i < 20; ++i) s += xv[i];
    s += dppf<XOR1>(s); s += dppf<XOR2>(s); s += dppf<HMIR>(s); s = xrow_sum(s);
    const float mean = s * (1.f / D);
    float q2 = 0.f;
#pragma unroll
    for (int i = 0; i < 20; ++i) { const float d = xv[i] - mean; q2 = fmaf(d, d, q2); }
    q2 += dppf<XOR1>(q2); q2 += dppf<XOR2>(q2); q2 += dppf<HMIR>(q2); q2 = xrow_sum(q2);
    const float mean_b = rbf(mean);
    const float rden = 1.f / rbf(__builtin_sqrtf(rbf(rbf(q2 * (1.f / D)) + 1e-5f)));
    STAMP(8);
    u32 xn[20];
#pragma unroll
    for (int i = 0; i < 5; ++i) {
      const float t0 = rbf(lo16(xo[i][0]) - mean_b) * rden, t1 = rbf(hi16(xo[i][0]) - mean_b) * rden, t2 = rbf(lo16(xo[i][1]) - mean_b) * rden, t3 = rbf(hi16(xo[i][1]) - mean_b) * rden;
      xn[4 * i] = pk2(t0, t0); xn[4 * i + 1] = pk2(t1, t1); xn[4 * i + 2] = pk2(t2, t2); xn[4 * i + 3] = pk2(t3, t3);
    }
    f32x4 a = {0.f, 0.f, 0.f, 0.f}, a2 = a;
#pragma unroll
    for (int i = 0; i < 20; i += 2) { a = mfma(sel(xn[i], sl), wr[i], a); a2 = mfma(sel(xn[i + 1], sl), wr[i + 1], a2); }
    a += a2;
    const u32 o = (w == 0 ? WS_QP : WS_F1) + pid * 2048 + lane * 16;
    st_granule2(rws, o, a[0], a[1], tag);
    st_granule2(rws, o + 1024, a[2], a[3], tag);
    STAMP(10);
    if (w == 0) {
      const u32 oq = WS_QP + h * 8 * 2048 + (lane >> 5) * 1024 + (32 * b + (lane & 31)) * 16;
      u32x4 qa[8];
      if (QSLEEP) __builtin_amdgcn_s_sleep(QSLEEP);
      for (u32 spins = 0; spins < SPIN_CAP; ++spins) {
        asm volatile("" ::: "memory");
#pragma unroll
        for (int pp = 0; pp < 8; ++pp) qa[pp] = ld128<AUX_SC1>(rws, oq + pp * 2048, 0);
        bool ok = true;
#pragma unroll
        for (int pp = 0; pp < 8; ++pp) ok = ok && qa[pp][1] == tag && qa[pp][3] == tag;
        if (__all(ok)) break;
        if (POLL0) __builtin_amdgcn_s_sleep(POLL0);
      }
      f32x2 q = {0.f, 0.f};
#pragma unroll
      for (int pp = 0; pp < 8; ++pp) { q[0] += __uint_as_float(qa[pp][0]); q[1] += __uint_as_float(qa[pp][2]); }
      ((u32*)lds)[L_Q + 4 * (lane & 15) + 2 * ((lane >> 4) & 1) + (lane >> 5)] = pk2(q[0], q[1]);
      STAMP(9);
      __syncthreads();
#pragma unroll
      for (int s = 0; s < SIF; ++s) fill(s, s);
    } else {
#pragma unroll
      for (int s = 0; s < SIF; ++s) fill(s, s);
      __syncthreads();
    }
  } else {
    __builtin_amdgcn_s_barrier();
    SB();
    fillk(0, 0); fillv(0, 0);
#pragma unroll
    for (int s = 1; s < SIF; ++s) {
      asm volatile("s_waitcnt vmcnt(%0)" ::"n"(PACE) : "memory"); fillk(s, s);
      asm volatile("s_waitcnt vmcnt(%0)" ::"n"(PACE) : "memory"); fillv(s, s);
    }
    __syncthreads();
  }
  STAMP(1); CSTAMP(14);
  dgq = (u32)__builtin_amdgcn_s_memrealtime();
  const u32x4 qv = *(const u32x4*)&lds[L_Q + 4 * c];

  float m = -INFINITY, l = 0.f;
  f32x4 acc = {0.f, 0.f, 0.f, 0.f};
  const bool c3 = (lane & 8) != 0, c2 = (lane & 4) != 0;
  auto ldk = [&](int s, int i, int step) __attribute__((always_inline)) { KR[s][i] = ld128<KAUX>(rk, vo[i], kvbase + step * 32768u); };
  auto ldv = [&](int s, int i, int step) __attribute__((always_inline)) { VR[s][i] = ld128<VAUX>(rv, vo[i], kvbase + step * 32768u); };
  auto consume = [&](int s, int next, int cur = -1) __attribute__((always_inline)) {
    float xd[4];
#pragma unroll
    for (int t = 0; t < 4; ++t) {
      float d = dot2(KR[s][t][0], qv[0], 0.f);
      d = dot2(KR[s][t][1], qv[1], d); d = dot2(KR[s][t][2], qv[2], d); xd[t] = dot2(KR[s][t][3], qv[3], d);
    }
    asm volatile("" : "+v"(xd[0]), "+v"(xd[1]), "+v"(xd[2]), "+v"(xd[3])); SB();
    if (WIN == 8) { if (next >= 0) fillk(s, next); }
    if (WIN == 4) { if (cur >= 0) fillv(s, cur); }
    if (WIN == 6) { if (cur >= 0) { ldv(s, 2, cur); ldv(s, 3, cur); } if (next >= 0) { ldk(s, 0, next); ldk(s, 1, next); } SB(); }
    const float y0 = (c3 ? xd[2] : xd[0]) + dppf<ROR8>(c3 ? xd[0] : xd[2]);
    const float y1 = (c3 ? xd[3] : xd[1]) + dppf<ROR8>(c3 ? xd[1] : xd[3]);
    float sc = (c2 ? y1 : y0) + dppf<HMIR>(c2 ? y0 : y1);
    sc += dppf<XOR1>(sc); sc += dppf<XOR2>(sc);
    sc = rbf(rbf(sc) * SCALE);
    float pm = fmaxf(sc, dppf<ROR4>(sc)); pm = fmaxf(pm, dppf<ROR8>(pm)); pm = xrow_max(pm);
    const float mn = fmaxf(m, pm);
    const float al = __builtin_amdgcn_exp2f((m - mn) * LOG2E), pr = __builtin_amdgcn_exp2f((sc - mn) * LOG2E);
    m = mn; l = fmaf(l, al, pr);
    acc *= al;
    const u32 pp = pk2(pr, pr);
    acc = mfma(sel(dppu<NEWB + 0>(pp), sl), VR[s][0], acc);
    acc = mfma(sel(dppu<NEWB + 4>(pp), sl), VR[s][1], acc);
    acc = mfma(sel(dppu<NEWB + 8>(pp), sl), VR[s][2], acc);
    acc = mfma(sel(dppu<NEWB + 12>(pp), sl), VR[s][3], acc);
    SB();
    if (next >= 0) {
      if (WIN == 8) fillv(s, next);
      if (WIN == 4) fillk(s, next);
      if (WIN == 6) { ldk(s, 2, next); ldk(s, 3, next); ldv(s, 0, next); ldv(s, 1, next); SB(); }
    }
  };
  if (PRIO == 2 && w >= 4) __builtin_amdgcn_s_setprio(1);
  if (PRIO == 3) { if (w >= 6) __builtin_amdgcn_s_setprio(3); else if (w >= 4) __builtin_amdgcn_s_setprio(2); else if (w >= 2) __builtin_amdgcn_s_setprio(1); }
  consume(0, SIF);
#pragma unroll
  for (int s = 1; s < SIF; ++s) consume(s, -1);
  STAMP(13);
  dg13 = (u32)__builtin_amdgcn_s_memrealtime();
  fillw();
#pragma unroll 1
  for (int t = SIF; t < nst - 1; ++t) {
    if (PRIO == 1) { if ((t + (w >> 2)) & 1) __builtin_amdgcn_s_setprio(1); else __builtin_amdgcn_s_setprio(0); }
    consume(0, t + 1, t);
  }
  const u32 of1 = ffn ? WS_F1 + (h * 8 + w) * 2048 + lane * 16 : OOB;
  u32x4 f0 = ld128<AUX_SC1>(rws, of1, 0), f1 = ld128<AUX_SC1>(rws, of1, 1024);
  const int fk0 = 32 * (h < 5 ? h : 0) + 2 * c, fbo = fk0 >= 80, fcol = 80 * j + fk0 - 80 * fbo;
  const u32 xraw = __builtin_amdgcn_raw_buffer_load_b32(mk(p.x, 2 * D * 2), (fbo * D + fcol) * 2, 0, 0);
  SB();
  consume(0, -1, nst - 1);
  STAMP(3); CSTAMP(15);
  if (ffn && !(ABL & 4) && !__all(f0[1] == tag && f0[3] == tag && f1[1] == tag && f1[3] == tag)) {
    for (u32 spins = 0; spins < SPIN_CAP; ++spins) {
      __builtin_amdgcn_s_sleep(8);
      asm volatile("" ::: "memory");
      f0 = ld128<AUX_SC1>(rws, of1, 0); f1 = ld128<AUX_SC1>(rws, of1, 1024);
      if (__all(f0[1] == tag && f0[3] == tag && f1[1] == tag && f1[3] == tag)) break;
    }
  }

  {
    float ls = l; ls += dppf<XOR1>(ls); ls += dppf<XOR2>(ls); ls += dppf<ROR4>(ls); ls += dppf<ROR8>(ls); ls = xrow_sum(ls) * 0.25f;
    float* rec = &lds[L_REC + w * 132];
    if (lane < 32) *(f32x4*)&rec[4 + 8 * c + 4 * g] = acc;
    if (lane == 0) { rec[0] = m; rec[1] = ls; }
    u32* fb = (u32*)&lds[L_F1 + w * 256 + (g >> 1) * 128 + 8 * c + 4 * (g & 1)];
    *(u32x4*)fb = u32x4{f0[0], f0[2], f1[0], f1[2]};
  }
  __syncthreads();
  STAMP(4);
  if (tid == 320) { __builtin_amdgcn_raw_buffer_store_b128(u32x4{dgs, dgq, (u32)__builtin_amdgcn_s_memrealtime(), dg13}, rws, WS_DIAG + bid * 16, 0, AUX_SC1); }
  if (!(ABL & 8)) {
  auto ffn2 = [&]() __attribute__((always_inline)) {
    f32x4 af = {0.f, 0.f, 0.f, 0.f};
#pragma unroll
    for (int i = 0; i < 2; ++i) {
      float gs = 0.f, us = 0.f;
#pragma unroll
      for (int pp = 0; pp < 8; ++pp) { gs += lds[L_F1 + pp * 256 + bb * 128 + 8 * w + 2 * g + i]; us += lds[L_F1 + pp * 256 + bb * 128 + 64 + 8 * w + 2 * g + i]; }
      const float gg = rbf(gs), uu = rbf(us);
      const float z = 0.7978845608028654f * (gg + 0.044715f * gg * gg * gg);
      const float th = 1.f - 2.f / (__builtin_amdgcn_exp2f(z * (2.f * LOG2E)) + 1.f);
      const float hv = rbf(0.5f * gg * (1.f + th)) * uu;
      af = mfma(sel(pk2(hv, hv), sl), wdr[i], af);
    }
    *(f32x4*)&lds[L_FR + w * 256 + (g >> 1) * 128 + 8 * c + 4 * (g & 1)] = af;
    asm volatile("s_waitcnt lgkmcnt(0)" ::: "memory");
    __builtin_amdgcn_s_barrier();
    asm volatile("" ::: "memory");
  };
  if (w == 0) {
    float mw[8], M = -INFINITY;
#pragma unroll
    for (int ww = 0; ww < 8; ++ww) { mw[ww] = lds[L_REC + ww * 132]; M = fmaxf(M, mw[ww]); }
    f32x2 o = {0.f, 0.f}; float L = 0.f;
#pragma unroll
    for (int ww = 0; ww < 8; ++ww) {
      const float e = __builtin_amdgcn_exp2f((mw[ww] - M) * LOG2E);
      o += *(const f32x2*)&lds[L_REC + ww * 132 + 4 + 2 * lane] * e; L = fmaf(e, lds[L_REC + ww * 132 + 1], L);
    }
    const u32 o1 = WS_H1 + pid * H1_STRIDE;
    st_granule2(rws, o1 + lane * 16, o[0], o[1], tag);
    st_granule2(rws, lane == 0 ? o1 + 1024 : OOB, M, L, tag);
    float* r1 = &lds[L_R1 + j * 132];
    *(f32x2*)&r1[4 + 2 * lane] = o;
    if (lane == 0) *(f32x2*)&r1[0] = f32x2{M, L};
    SB();
    ffn2();
  } else {
    const int mj = j ^ w;
    const u32 o1 = WS_H1 + (h * 8 + mj) * H1_STRIDE;
    const u32 oml = lane == 0 ? o1 + 1024 : OOB;
    u32x4 ra = ld128<AUX_SC1>(rws, o1 + lane * 16, 0), rml = ld128<AUX_SC1>(rws, oml, 0);
    SB();
    ffn2();
    const bool have = __all((ra[1] == tag) & (ra[3] == tag) & ((lane != 0) | ((rml[1] == tag) & (rml[3] == tag))));
    SB();
    if (w < 4 && tid - 64 < 160) {
      const int k = tid - 64, bo = k >= 80, col = k - 80 * bo;
      float v = 0.f;
#pragma unroll
      for (int ww = 0; ww < 8; ++ww) v += lds[L_FR + ww * 256 + bo * 128 + col];
      st_granule(rws, WS_H2 + pid * H2_STRIDE + (160 + k) * 8, v, tag);
    }
    if (!have && !(ABL & 16)) {
      for (u32 spins = 0; spins < SPIN_CAP; ++spins) {
        if (POLL1) __builtin_amdgcn_s_sleep(POLL1);
        asm volatile("" ::: "memory");
        rml = ld128<AUX_SC1>(rws, oml, 0);
        if (__builtin_amdgcn_readfirstlane(rml[1]) == tag && __builtin_amdgcn_readfirstlane(rml[3]) == tag) {
          asm volatile("" ::: "memory");
          ra = ld128<AUX_SC1>(rws, o1 + lane * 16, 0);
          if (__all(ra[1] == tag && ra[3] == tag)) break;
        }
      }
    }
    float* r1 = &lds[L_R1 + mj * 132];
    *(u32x2*)&r1[4 + 2 * lane] = u32x2{ra[0], ra[2]};
    if (lane == 0) *(u32x2*)&r1[0] = u32x2{rml[0], rml[2]};
  }
  __syncthreads();
  STAMP(5);
  {
    const float* r1 = &lds[L_R1 + bb * 4 * 132];
    const float m0 = r1[0], m1 = r1[132], m2 = r1[264], m3 = r1[396];
    const float M = fmaxf(fmaxf(m0, m1), fmaxf(m2, m3));
    const float e0 = __builtin_amdgcn_exp2f((m0 - M) * LOG2E), e1 = __builtin_amdgcn_exp2f((m1 - M) * LOG2E), e2 = __builtin_amdgcn_exp2f((m2 - M) * LOG2E), e3 = __builtin_amdgcn_exp2f((m3 - M) * LOG2E);
    const float L = e0 * r1[1] + e1 * r1[133] + e2 * r1[265] + e3 * r1[397];
    const int d0 = 4 + 16 * w + 4 * g;
    f32x4 o = *(const f32x4*)&r1[d0] * e0 + *(const f32x4*)&r1[132 + d0] * e1 + *(const f32x4*)&r1[264 + d0] * e2 + *(const f32x4*)&r1[396 + d0] * e3;
    o *= 1.f / L;
    f32x4 as = {0.f, 0.f, 0.f, 0.f};
#pragma unroll
    for (int i = 0; i < 4; ++i) as = mfma(sel(pk2(o[i], o[i]), sl), wor[i], as);
    *(f32x4*)&lds[L_SR + w * 256 + (g >> 1) * 128 + 8 * c + 4 * (g & 1)] = as;
  }
  __syncthreads();
  if (tid < 160) {
    const int bo = tid >= 80, col = tid - 80 * bo;
    float v = 0.f;
#pragma unroll
    for (int ww = 0; ww < 8; ++ww) v += lds[L_SR + ww * 256 + bo * 128 + col];
    st_granule(rws, WS_H2 + pid * H2_STRIDE + tid * 8, v, tag);
  }
  STAMP(6);
  if (h < 5 && !(ABL & 32)) {
    const u32 o2 = WS_H2 + ((4 * w + g) * 8 + j) * H2_STRIDE + fk0 * 8;
    u32x4 aS = {0u, 0u, 0u, 0u}, aF = aS;
    bool nS = true, nF = true;
    for (u32 spins = 0; spins < SPIN_CAP; ++spins) {
      asm volatile("" ::: "memory");
      if (nS) aS = ld128<AUX_SC1>(rws, o2, 0);
      if (nF) aF = ld128<AUX_SC1>(rws, o2 + 1280, 0);
      nS = !(aS[1] == tag && aS[3] == tag); nF = !(aF[1] == tag && aF[3] == tag);
      if (!__any(nS || nF)) break;
      if (POLL2) __builtin_amdgcn_s_sleep(POLL2);
    }
    const f32x4 part = {xrow_sum(__uint_as_float(aS[0])), xrow_sum(__uint_as_float(aS[2])), xrow_sum(__uint_as_float(aF[0])), xrow_sum(__uint_as_float(aF[2]))};
    if (g == 0) *(f32x4*)&lds[L_FIN + w * 64 + 4 * c] = part;
    __syncthreads();
    if (w == 7) {
      f32x4 t = {0.f, 0.f, 0.f, 0.f};
#pragma unroll
      for (int ww = 0; ww < 8; ++ww) t += *(const f32x4*)&lds[L_FIN + ww * 64 + 4 * c];
      const float s0 = rbf(t[0]), s1 = rbf(t[1]), f0 = rbf(t[2]), f1 = rbf(t[3]);
      const float g0 = rbf(f0 / (1.f + __builtin_amdgcn_exp2f(-f0 * LOG2E))), g1 = rbf(f1 / (1.f + __builtin_amdgcn_exp2f(-f1 * LOG2E)));
      if (g == 0) *(u32*)(p.out + fbo * D + fcol) = pk2(lo16(xraw) + rbf(s0 * g0), hi16(xraw) + rbf(s1 * g1));
      if (bid == 0 && lane == 0) __builtin_amdgcn_raw_buffer_store_b32(tag, rws, WS_EPOCH, 0, AUX_SC1);
      STAMP(7);
    if (bid == 0) {
      const u32 tend = (u32)__builtin_amdgcn_s_memrealtime();
      int mnS = 1 << 30, mxQ = -(1 << 30), mnE = 1 << 30, mxE = -(1 << 30), sE = 0, sQ = 0, s13 = 0;
      for (int i = 0; i < 4; ++i) {
        const u32x4 e = ld128<AUX_SC1>(rws, WS_DIAG + (lane + 64 * i) * 16, 0);
        const int ds = (int)(e[0] - dgs), dq = (int)(e[1] - dgs), de = (int)(e[2] - dgs), d13 = (int)(e[3] - dgs);
        mnS = min(mnS, ds); mxQ = max(mxQ, dq); mnE = min(mnE, de); mxE = max(mxE, de); sE += de; sQ += dq; s13 += d13;
      }
      for (int o = 32; o >= 1; o >>= 1) { mnS = min(mnS, __shfl_xor(mnS, o)); mxQ = max(mxQ, __shfl_xor(mxQ, o)); mnE = min(mnE, __shfl_xor(mnE, o)); mxE = max(mxE, __shfl_xor(mxE, o)); sE += __shfl_xor(sE, o); sQ += __shfl_xor(sQ, o); s13 += __shfl_xor(s13, o); }
      int meas = 0;
      if (DIAG == 1) meas = sQ / 256 - mnS;
      if (DIAG == 2) meas = s13 / 256 - mnS;
      if (DIAG == 3) meas = sE / 256 - mnS;
      if (DIAG == 4) meas = mxE - mnS;
      if (DIAG == 6) meas = (int)(tend - dgs) - mnS;
      const u32 t1 = (u32)__builtin_amdgcn_s_memrealtime();
      const u32 dl = (u32)max(meas, 0) * DELAYK;
      while ((u32)__builtin_amdgcn_s_memrealtime() - t1 < dl) __builtin_amdgcn_s_sleep(1);
    }
    }
  }
  }
}
}

extern "C" void kernel_launch(void* const* d_in, const int* in_sizes, int n_in, void* d_out, int out_size, void* d_ws, size_t ws_size, hipStream_t stream) {
  Params p;
  p.x = (const u16*)d_in[0]; p.kc = (const u16*)d_in[1]; p.vc = (const u16*)d_in[2]; p.wq = (const u16*)d_in[3]; p.wo = (const u16*)d_in[4];
  p.wg = (const u16*)d_in[6]; p.wu = (const u16*)d_in[7]; p.wd = (const u16*)d_in[8];
  p.out = (u16*)d_out; p.ws = (unsigned char*)d_ws;
  fused_decode<<<NWG, NT, 0, stream>>>(p);
}
```

```cpp
#include <hip/hip_runtime.h>
#include <stdint.h>

#define SIF 3
#define NE 266
#define LOCK 0
#define POLL0 1
#define POLL1 1
#define POLL2 1
#define KAUX 2
#define VAUX 2
#define PACE 63
#define QSLEEP 0
#define ABL 0
#define MAPV 0
#define FINE 1
#define AMAP 3
#define LOCKN 0
#define PRIO 0
#define EXTRA 0
#define WIN 4
#define STAG 0
#define ALIGN 0
#define ONELANE 1
#define DIAG 8
#define DELAYK 1
#define STAMPS 0

namespace {
constexpr int D = 640, H = 32, SK = 16384, DH = 128, DF = 1728;
constexpr int NWG = 256, NT = 512;

typedef unsigned u32;
typedef unsigned long long u64;
typedef unsigned short u16;
typedef u32 u32x2 __attribute__((ext_vector_type(2)));
typedef u32 u32x4 __attribute__((ext_vector_type(4)));
typedef float f32x2 __attribute__((ext_vector_type(2)));
typedef float f32x4 __attribute__((ext_vector_type(4)));
typedef __bf16 bf16x2 __attribute__((ext_vector_type(2)));
typedef __bf16 bf16x8 __attribute__((ext_vector_type(8)));
typedef __amdgpu_buffer_rsrc_t rsrc_t;
#define GAS __attribute__((address_space(1)))

constexpr int AUX_NT = 2, AUX_SC1 = 16;
constexpr u32 OOB = 0x80000000u;

constexpr u32 WS_EPOCH = 0;
constexpr u32 WS_QP = 4096;
constexpr u32 WS_F1 = WS_QP + NWG * 2048;
constexpr u32 WS_H1 = WS_F1 + NWG * 2048;
constexpr u32 H1_STRIDE = 144 * 8;
constexpr u32 WS_H2 = WS_H1 + NWG * H1_STRIDE;
constexpr u32 H2_STRIDE = 320 * 8;
constexpr u32 WS_STAMP = WS_H2 + NWG * H2_STRIDE;
constexpr u32 WS_DIAG = WS_STAMP + NWG * 8 * 16 * 4;
constexpr u32 WS_BYTES = WS_DIAG + NWG * 32;

constexpr int L_Q = 0;
constexpr int L_REC = L_Q + 64;
constexpr int L_F1 = L_REC + 8 * 132;
constexpr int L_R1 = L_F1 + 8 * 256;
constexpr int L_SR = L_R1 + 8 * 132;
constexpr int L_FR = L_SR + 8 * 256;
constexpr int L_FIN = L_FR + 8 * 256;
constexpr int L_USED = L_FIN + 8 * 64;
constexpr int L_END = L_USED;

constexpr float LOG2E = 1.4426950408889634f;
constexpr float SCALE = 0.08838834764831845f;
constexpr u32 SPIN_CAP = 1u << 18;

__device__ __forceinline__ float lo16(u32 v) { return __uint_as_float(v << 16); }
__device__ __forceinline__ float hi16(u32 v) { return __uint_as_float(v & 0xffff0000u); }
__device__ __forceinline__ u32 pk2(float a, float b) { bf16x2 r = {(__bf16)a, (__bf16)b}; return __builtin_bit_cast(u32, r); }
__device__ __forceinline__ float rbf(float a) { return lo16(pk2(a, a)); }
__device__ __forceinline__ float dot2(u32 a, u32 b, float c) { return __builtin_amdgcn_fdot2_f32_bf16(__builtin_bit_cast(bf16x2, a), __builtin_bit_cast(bf16x2, b), c, false); }
template <int CTRL> __device__ __forceinline__ float dppf(float x) { return __builtin_bit_cast(float, __builtin_amdgcn_mov_dpp(__builtin_bit_cast(int, x), CTRL, 0xf, 0xf, true)); }
template <int CTRL> __device__ __forceinline__ u32 dppu(u32 x) { return (u32)__builtin_amdgcn_mov_dpp((int)x, CTRL, 0xf, 0xf, true); }
constexpr int XOR1 = 0xB1, XOR2 = 0x4E, HMIR = 0x141, ROR4 = 0x124, ROR8 = 0x128, NEWB = 0x150;
__device__ __forceinline__ float xrow_max(float x) {
  auto s = __builtin_amdgcn_permlane16_swap(__float_as_uint(x), __float_as_uint(x), false, false);
  x = fmaxf(__uint_as_float(s[0]), __uint_as_float(s[1]));
  auto t = __builtin_amdgcn_permlane32_swap(__float_as_uint(x), __float_as_uint(x), false, false);
  return fmaxf(__uint_as_float(t[0]), __uint_as_float(t[1]));
}
__device__ __forceinline__ float xrow_sum(float x) {
  auto s = __builtin_amdgcn_permlane16_swap(__float_as_uint(x), __float_as_uint(x), false, false);
  x = __uint_as_float(s[0]) + __uint_as_float(s[1]);
  auto t = __builtin_amdgcn_permlane32_swap(__float_as_uint(x), __float_as_uint(x), false, false);
  return __uint_as_float(t[0]) + __uint_as_float(t[1]);
}
__device__ __forceinline__ f32x4 mfma(u32x4 a, u32x4 b, f32x4 c) {
  return __builtin_amdgcn_mfma_f32_16x16x32_bf16(__builtin_bit_cast(bf16x8, a), __builtin_bit_cast(bf16x8, b), c, 0, 0, 0);
}
__device__ __forceinline__ rsrc_t mk(const void* p, u32 bytes) { return __builtin_amdgcn_make_buffer_rsrc((void*)p, 0, bytes, 0x00020000); }
template <int AUX> __device__ __forceinline__ u32x4 ld128(rsrc_t r, u32 voff, u32 soff) { return __builtin_amdgcn_raw_buffer_load_b128(r, voff, soff, AUX); }
template <int AUX> __device__ __forceinline__ u32x2 ld64(rsrc_t r, u32 voff, u32 soff) { return __builtin_amdgcn_raw_buffer_load_b64(r, voff, soff, AUX); }
__device__ __forceinline__ void st_granule(rsrc_t r, u32 off, float v, u32 tag) { __builtin_amdgcn_raw_buffer_store_b64(u32x2{__float_as_uint(v), tag}, r, off, 0, AUX_SC1); }
__device__ __forceinline__ void st_granule2(rsrc_t r, u32 off, float v0, float v1, u32 tag) { __builtin_amdgcn_raw_buffer_store_b128(u32x4{__float_as_uint(v0), tag, __float_as_uint(v1), tag}, r, off, 0, AUX_SC1); }
#define SB() __builtin_amdgcn_sched_barrier(0)
#define STAMP(k) do { } while (0)
#define CSTAMP(k) do { } while (0)

struct Params {
  const u16 *x, *kc, *vc, *wq, *wo, *wg, *wu, *wd;
  u16* out;
  unsigned char* ws;
};

struct Sel { u32 k0, k1, k2, k3; };
__device__ __forceinline__ u32x4 sel(u32 pp, const Sel& s) { return u32x4{pp & s.k0, pp & s.k1, pp & s.k2, pp & s.k3}; }

__global__ void __launch_bounds__(NT, 2) fused_decode(Params p) {
  __shared__ __attribute__((aligned(16))) float lds[L_END];
  const int tid = threadIdx.x, lane = tid & 63, w = __builtin_amdgcn_readfirstlane(tid >> 6);
  const int g = lane >> 4, c = lane & 15, bb = c >> 3;
  const int bid = blockIdx.x, xc = bid & 7, jj = bid >> 3;
  const int xp = xc >> 1;
  const int par = xc & 1, b = (jj >> 1) & 1, half = jj & 1;
  const int h = MAPV == 0 ? xp + 4 * (jj >> 2) : MAPV == 1 ? (xp == 1 ? 3 : xp == 3 ? 1 : xp) + 4 * (jj >> 2) : MAPV == 2 ? ((xp + (jj >> 2)) & 3) + 4 * (jj >> 2) : ((xp ^ (jj >> 2)) & 3) + 4 * (jj >> 2);
  const int j = par + 2 * half + 4 * b;
  const int pid = h * 8 + j;
  const bool ffn = h < DF / 64;
  asm volatile("" ::: "v250");
  STAMP(0);
  const rsrc_t rws = mk(p.ws, WS_BYTES);
  const u32 dgs = (u32)__builtin_amdgcn_s_memrealtime();
  u32 dgq = 0, dg13 = 0;
  const u32 tag = *(const __attribute__((address_space(4))) u32*)(p.ws + WS_EPOCH) + 0x9E3779B9u;

  const size_t slab = ((size_t)(b * H + h) * SK + half * (SK / 2)) * DH;
  const rsrc_t rk = mk(p.kc + slab, SK / 2 * DH * 2), rv = mk(p.vc + slab, SK / 2 * DH * 2);
  const int split = NE / 8 + (w < NE % 8 ? 1 : 0);
  const int nst = par ? 64 - split : split;
  const u32 kvbase = (par ? split : 0) * 32768u;
  u32 vo[4];
#pragma unroll
  for (int i = 0; i < 4; ++i)
    vo[i] = c * 16 + (AMAP == 0 ? w * 4096 + i * 1024 + g * 256
                    : AMAP == 1 ? g * 4096 + (i & 1) * 16384 + (i >> 1) * 256 + w * 512
                    : AMAP == 2 ? g * 8192 + (i & 1) * 4096 + (i >> 1) * 256 + w * 512
                    : AMAP == 3 ? g * 256 + i * 8192 + w * 1024
                    :             g * 256 + (i & 1) * 4096 + (i >> 1) * 16384 + (w & 3) * 1024 + (w >> 2) * 8192);
  u32x4 KR[SIF][4], VR[SIF][4];
  auto fillk = [&](int s, int step) __attribute__((always_inline)) {
    const u32 so = kvbase + step * 32768u;
#pragma unroll
    for (int i = 0; i < 4; ++i) KR[s][i] = ld128<KAUX>(rk, vo[i], so);
    SB();
  };
  auto fillv = [&](int s, int step) __attribute__((always_inline)) {
    const u32 so = kvbase + step * 32768u;
#pragma unroll
    for (int i = 0; i < 4; ++i) VR[s][i] = ld128<VAUX>(rv, vo[i], so);
    SB();
  };
  auto fill = [&](int s, int step) __attribute__((always_inline)) { fillk(s, step); fillv(s, step); };
  u32x4 wor[4], wdr[2];
  auto fillw = [&]() __attribute__((always_inline)) {
    const rsrc_t ro = mk(p.wo, H * DH * D * 2), rd = mk(p.wd, DF * D * 2);
    const u32 vo = (c < 10 && !(ABL & 2)) ? (128 * h + 16 * w + 4 * g) * (D * 2) + j * 160 + c * 16 : OOB;
    const u32 vd = (ffn && c < 10 && !(ABL & 2)) ? (64 * h + 8 * w + 2 * g) * (D * 2) + j * 160 + c * 16 : OOB;
#pragma unroll
    for (int i = 0; i < 4; ++i) wor[i] = ld128<AUX_NT>(ro, vo, i * (D * 2));
#pragma unroll
    for (int i = 0; i < 2; ++i) wdr[i] = ld128<AUX_NT>(rd, vd, i * (D * 2));
    SB();
  };
  Sel sl;
  {
    const int mm = lane & 7;
    const u32 hm = (mm & 1) ? 0xffff0000u : 0x0000ffffu;
    sl.k0 = (mm >> 1) == 0 ? hm : 0u; sl.k1 = (mm >> 1) == 1 ? hm : 0u; sl.k2 = (mm >> 1) == 2 ? hm : 0u; sl.k3 = (mm >> 1) == 3 ? hm : 0u;
  }

  if (!(ABL & 4) && (w == 0 || (w == 1 && ffn))) {
    u32x2 xs[5], xo[5];
    u32x4 wr[20];
    const rsrc_t rx = mk(p.x, 2 * D * 2);
#pragma unroll
    for (int i = 0; i < 5; ++i) xs[i] = ld64<0>(rx, (bb * D + 20 * (8 * g + (c & 7))) * 2 + i * 8, 0);
#pragma unroll
    for (int i = 0; i < 5; ++i) xo[i] = ld64<0>(rx, (bb * D + 80 * j + 20 * g) * 2 + i * 8, 0);
    const int ld = w == 0 ? H * DH : DF;
    const GAS u16* src = w == 0 ? (const GAS u16*)p.wq + h * DH + c * 8 : (const GAS u16*)(c < 8 ? p.wg : p.wu) + h * 64 + (c & 7) * 8;
    src += (80 * j + 20 * g) * ld;
#pragma unroll
    for (int i = 0; i < 20; ++i) wr[i] = __builtin_nontemporal_load((const GAS u32x4*)(src + i * ld));
    SB();
    __builtin_amdgcn_s_barrier();
    SB();
    float xv[20];
#pragma unroll
    for (int i = 0; i < 5; ++i) { xv[4 * i] = lo16(xs[i][0]); xv[4 * i + 1] = hi16(xs[i][0]); xv[4 * i + 2] = lo16(xs[i][1]); xv[4 * i + 3] = hi16(xs[i][1]); }
    float s = 0.f;
#pragma unroll
    for (int i = 0; i < 20; ++i) s += xv[i];
    s += dppf<XOR1>(s); s += dppf<XOR2>(s); s += dppf<HMIR>(s); s = xrow_sum(s);
    const float mean = s * (1.f / D);
    float q2 = 0.f;
#pragma unroll
    for (int i = 0; i < 20; ++i) { const float d = xv[i] - mean; q2 = fmaf(d, d, q2); }
    q2 += dppf<XOR1>(q2); q2 += dppf<XOR2>(q2); q2 += dppf<HMIR>(q2); q2 = xrow_sum(q2);
    const float mean_b = rbf(mean);
    const float rden = 1.f / rbf(__builtin_sqrtf(rbf(rbf(q2 * (1.f / D)) + 1e-5f)));
    STAMP(8);
    u32 xn[20];
#pragma unroll
    for (int i = 0; i < 5; ++i) {
      const float t0 = rbf(lo16(xo[i][0]) - mean_b) * rden, t1 = rbf(hi16(xo[i][0]) - mean_b) * rden, t2 = rbf(lo16(xo[i][1]) - mean_b) * rden, t3 = rbf(hi16(xo[i][1]) - mean_b) * rden;
      xn[4 * i] = pk2(t0, t0); xn[4 * i + 1] = pk2(t1, t1); xn[4 * i + 2] = pk2(t2, t2); xn[4 * i + 3] = pk2(t3, t3);
    }
    f32x4 a = {0.f, 0.f, 0.f, 0.f}, a2 = a;
#pragma unroll
    for (int i = 0; i < 20; i += 2) { a = mfma(sel(xn[i], sl), wr[i], a); a2 = mfma(sel(xn[i + 1], sl), wr[i + 1], a2); }
    a += a2;
    const u32 o = (w == 0 ? WS_QP : WS_F1) + pid * 2048 + lane * 16;
    st_granule2(rws, o, a[0], a[1], tag);
    st_granule2(rws, o + 1024, a[2], a[3], tag);
    STAMP(10);
    if (w == 0) {
      const u32 oq = WS_QP + h * 8 * 2048 + (lane >> 5) * 1024 + (32 * b + (lane & 31)) * 16;
      u32x4 qa[8];
      if (QSLEEP) __builtin_amdgcn_s_sleep(QSLEEP);
      for (u32 spins = 0; spins < SPIN_CAP; ++spins) {
        asm volatile("" ::: "memory");
#pragma unroll
        for (int pp = 0; pp < 8; ++pp) qa[pp] = ld128<AUX_SC1>(rws, oq + pp * 2048, 0);
        bool ok = true;
#pragma unroll
        for (int pp = 0; pp < 8; ++pp) ok = ok && qa[pp][1] == tag && qa[pp][3] == tag;
        if (__all(ok)) break;
        if (POLL0) __builtin_amdgcn_s_sleep(POLL0);
      }
      f32x2 q = {0.f, 0.f};
#pragma unroll
      for (int pp = 0; pp < 8; ++pp) { q[0] += __uint_as_float(qa[pp][0]); q[1] += __uint_as_float(qa[pp][2]); }
      ((u32*)lds)[L_Q + 4 * (lane & 15) + 2 * ((lane >> 4) & 1) + (lane >> 5)] = pk2(q[0], q[1]);
      STAMP(9);
      __syncthreads();
#pragma unroll
      for (int s = 0; s < SIF; ++s) fill(s, s);
    } else {
#pragma unroll
      for (int s = 0; s < SIF; ++s) fill(s, s);
      __syncthreads();
    }
  } else {
    __builtin_amdgcn_s_barrier();
    SB();
    fillk(0, 0); fillv(0, 0);
#pragma unroll
    for (int s = 1; s < SIF; ++s) {
      asm volatile("s_waitcnt vmcnt(%0)" ::"n"(PACE) : "memory"); fillk(s, s);
      asm volatile("s_waitcnt vmcnt(%0)" ::"n"(PACE) : "memory"); fillv(s, s);
    }
    __syncthreads();
  }
  STAMP(1); CSTAMP(14);
  dgq = (u32)__builtin_amdgcn_s_memrealtime();
  const u32x4 qv = *(const u32x4*)&lds[L_Q + 4 * c];

  float m = -INFINITY, l = 0.f;
  f32x4 acc = {0.f, 0.f, 0.f, 0.f};
  const bool c3 = (lane & 8) != 0, c2 = (lane & 4) != 0;
  auto ldk = [&](int s, int i, int step) __attribute__((always_inline)) { KR[s][i] = ld128<KAUX>(rk, vo[i], kvbase + step * 32768u); };
  auto ldv = [&](int s, int i, int step) __attribute__((always_inline)) { VR[s][i] = ld128<VAUX>(rv, vo[i], kvbase + step * 32768u); };
  auto consume = [&](int s, int next, int cur = -1) __attribute__((always_inline)) {
    float xd[4];
#pragma unroll
    for (int t = 0; t < 4; ++t) {
      float d = dot2(KR[s][t][0], qv[0], 0.f);
      d = dot2(KR[s][t][1], qv[1], d); d = dot2(KR[s][t][2], qv[2], d); xd[t] = dot2(KR[s][t][3], qv[3], d);
    }
    asm volatile("" : "+v"(xd[0]), "+v"(xd[1]), "+v"(xd[2]), "+v"(xd[3])); SB();
    if (WIN == 8) { if (next >= 0) fillk(s, next); }
    if (WIN == 4) { if (cur >= 0) fillv(s, cur); }
    if (WIN == 6) { if (cur >= 0) { ldv(s, 2, cur); ldv(s, 3, cur); } if (next >= 0) { ldk(s, 0, next); ldk(s, 1, next); } SB(); }
    const float y0 = (c3 ? xd[2] : xd[0]) + dppf<ROR8>(c3 ? xd[0] : xd[2]);
    const float y1 = (c3 ? xd[3] : xd[1]) + dppf<ROR8>(c3 ? xd[1] : xd[3]);
    float sc = (c2 ? y1 : y0) + dppf<HMIR>(c2 ? y0 : y1);
    sc += dppf<XOR1>(sc); sc += dppf<XOR2>(sc);
    sc = rbf(rbf(sc) * SCALE);
    float pm = fmaxf(sc, dppf<ROR4>(sc)); pm = fmaxf(pm, dppf<ROR8>(pm)); pm = xrow_max(pm);
    const float mn = fmaxf(m, pm);
    const float al = __builtin_amdgcn_exp2f((m - mn) * LOG2E), pr = __builtin_amdgcn_exp2f((sc - mn) * LOG2E);
    m = mn; l = fmaf(l, al, pr);
    acc *= al;
    const u32 pp = pk2(pr, pr);
    acc = mfma(sel(dppu<NEWB + 0>(pp), sl), VR[s][0], acc);
    acc = mfma(sel(dppu<NEWB + 4>(pp), sl), VR[s][1], acc);
    acc = mfma(sel(dppu<NEWB + 8>(pp), sl), VR[s][2], acc);
    acc = mfma(sel(dppu<NEWB + 12>(pp), sl), VR[s][3], acc);
    SB();
    if (next >= 0) {
      if (WIN == 8) fillv(s, next);
      if (WIN == 4) fillk(s, next);
      if (WIN == 6) { ldk(s, 2, next); ldk(s, 3, next); ldv(s, 0, next); ldv(s, 1, next); SB(); }
    }
  };
  if (PRIO == 2 && w >= 4) __builtin_amdgcn_s_setprio(1);
  if (PRIO == 3) { if (w >= 6) __builtin_amdgcn_s_setprio(3); else if (w >= 4) __builtin_amdgcn_s_setprio(2); else if (w >= 2) __builtin_amdgcn_s_setprio(1); }
  consume(0, SIF);
#pragma unroll
  for (int s = 1; s < SIF; ++s) consume(s, -1);
  STAMP(13);
  dg13 = (u32)__builtin_amdgcn_s_memrealtime();
  fillw();
#pragma unroll 1
  for (int t = SIF; t < nst - 1; ++t) {
    if (PRIO == 1) { if ((t + (w >> 2)) & 1) __builtin_amdgcn_s_setprio(1); else __builtin_amdgcn_s_setprio(0); }
    consume(0, t + 1, t);
  }
  const u32 of1 = ffn ? WS_F1 + (h * 8 + w) * 2048 + lane * 16 : OOB;
  u32x4 f0 = ld128<AUX_SC1>(rws, of1, 0), f1 = ld128<AUX_SC1>(rws, of1, 1024);
  const int fk0 = 32 * (h < 5 ? h : 0) + 2 * c, fbo = fk0 >= 80, fcol = 80 * j + fk0 - 80 * fbo;
  const u32 xraw = __builtin_amdgcn_raw_buffer_load_b32(mk(p.x, 2 * D * 2), (fbo * D + fcol) * 2, 0, 0);
  SB();
  consume(0, -1, nst - 1);
  STAMP(3); CSTAMP(15);
  if (ffn && !(ABL & 4) && !__all(f0[1] == tag && f0[3] == tag && f1[1] == tag && f1[3] == tag)) {
    for (u32 spins = 0; spins < SPIN_CAP; ++spins) {
      __builtin_amdgcn_s_sleep(8);
      asm volatile("" ::: "memory");
      f0 = ld128<AUX_SC1>(rws, of1, 0); f1 = ld128<AUX_SC1>(rws, of1, 1024);
      if (__all(f0[1] == tag && f0[3] == tag && f1[1] == tag && f1[3] == tag)) break;
    }
  }

  {
    float ls = l; ls += dppf<XOR1>(ls); ls += dppf<XOR2>(ls); ls += dppf<ROR4>(ls); ls += dppf<ROR8>(ls); ls = xrow_sum(ls) * 0.25f;
    float* rec = &lds[L_REC + w * 132];
    if (lane < 32) *(f32x4*)&rec[4 + 8 * c + 4 * g] = acc;
    if (lane == 0) { rec[0] = m; rec[1] = ls; }
    u32* fb = (u32*)&lds[L_F1 + w * 256 + (g >> 1) * 128 + 8 * c + 4 * (g & 1)];
    *(u32x4*)fb = u32x4{f0[0], f0[2], f1[0], f1[2]};
  }
  __syncthreads();
  STAMP(4);
  if (tid == 320) { __builtin_amdgcn_raw_buffer_store_b128(u32x4{dgs, dgq, (u32)__builtin_amdgcn_s_memrealtime(), dg13}, rws, WS_DIAG + bid * 16, 0, AUX_SC1); }
  if (!(ABL & 8)) {
  auto ffn2 = [&]() __attribute__((always_inline)) {
    f32x4 af = {0.f, 0.f, 0.f, 0.f};
#pragma unroll
    for (int i = 0; i < 2; ++i) {
      float gs = 0.f, us = 0.f;
#pragma unroll
      for (int pp = 0; pp < 8; ++pp) { gs += lds[L_F1 + pp * 256 + bb * 128 + 8 * w + 2 * g + i]; us += lds[L_F1 + pp * 256 + bb * 128 + 64 + 8 * w + 2 * g + i]; }
      const float gg = rbf(gs), uu = rbf(us);
      const float z = 0.7978845608028654f * (gg + 0.044715f * gg * gg * gg);
      const float th = 1.f - 2.f / (__builtin_amdgcn_exp2f(z * (2.f * LOG2E)) + 1.f);
      const float hv = rbf(0.5f * gg * (1.f + th)) * uu;
      af = mfma(sel(pk2(hv, hv), sl), wdr[i], af);
    }
    *(f32x4*)&lds[L_FR + w * 256 + (g >> 1) * 128 + 8 * c + 4 * (g & 1)] = af;
    asm volatile("s_waitcnt lgkmcnt(0)" ::: "memory");
    __builtin_amdgcn_s_barrier();
    asm volatile("" ::: "memory");
  };
  if (w == 0) {
    float mw[8], M = -INFINITY;
#pragma unroll
    for (int ww = 0; ww < 8; ++ww) { mw[ww] = lds[L_REC + ww * 132]; M = fmaxf(M, mw[ww]); }
    f32x2 o = {0.f, 0.f}; float L = 0.f;
#pragma unroll
    for (int ww = 0; ww < 8; ++ww) {
      const float e = __builtin_amdgcn_exp2f((mw[ww] - M) * LOG2E);
      o += *(const f32x2*)&lds[L_REC + ww * 132 + 4 + 2 * lane] * e; L = fmaf(e, lds[L_REC + ww * 132 + 1], L);
    }
    const u32 o1 = WS_H1 + pid * H1_STRIDE;
    st_granule2(rws, o1 + lane * 16, o[0], o[1], tag);
    st_granule2(rws, lane == 0 ? o1 + 1024 : OOB, M, L, tag);
    float* r1 = &lds[L_R1 + j * 132];
    *(f32x2*)&r1[4 + 2 * lane] = o;
    if (lane == 0) *(f32x2*)&r1[0] = f32x2{M, L};
    SB();
    ffn2();
  } else {
    const int mj = j ^ w;
    const u32 o1 = WS_H1 + (h * 8 + mj) * H1_STRIDE;
    const u32 oml = lane == 0 ? o1 + 1024 : OOB;
    u32x4 ra = ld128<AUX_SC1>(rws, o1 + lane * 16, 0), rml = ld128<AUX_SC1>(rws, oml, 0);
    SB();
    ffn2();
    const bool have = __all((ra[1] == tag) & (ra[3] == tag) & ((lane != 0) | ((rml[1] == tag) & (rml[3] == tag))));
    SB();
    if (w < 4 && tid - 64 < 160) {
      const int k = tid - 64, bo = k >= 80, col = k - 80 * bo;
      float v = 0.f;
#pragma unroll
      for (int ww = 0; ww < 8; ++ww) v += lds[L_FR + ww * 256 + bo * 128 + col];
      st_granule(rws, WS_H2 + pid * H2_STRIDE + (160 + k) * 8, v, tag);
    }
    if (!have && !(ABL & 16)) {
      for (u32 spins = 0; spins < SPIN_CAP; ++spins) {
        if (POLL1) __builtin_amdgcn_s_sleep(POLL1);
        asm volatile("" ::: "memory");
        rml = ld128<AUX_SC1>(rws, oml, 0);
        if (__builtin_amdgcn_readfirstlane(rml[1]) == tag && __builtin_amdgcn_readfirstlane(rml[3]) == tag) {
          asm volatile("" ::: "memory");
          ra = ld128<AUX_SC1>(rws, o1 + lane * 16, 0);
          if (__all(ra[1] == tag && ra[3] == tag)) break;
        }
      }
    }
    float* r1 = &lds[L_R1 + mj * 132];
    *(u32x2*)&r1[4 + 2 * lane] = u32x2{ra[0], ra[2]};
    if (lane == 0) *(u32x2*)&r1[0] = u32x2{rml[0], rml[2]};
  }
  __syncthreads();
  STAMP(5);
  {
    const float* r1 = &lds[L_R1 + bb * 4 * 132];
    const float m0 = r1[0], m1 = r1[132], m2 = r1[264], m3 = r1[396];
    const float M = fmaxf(fmaxf(m0, m1), fmaxf(m2, m3));
    const float e0 = __builtin_amdgcn_exp2f((m0 - M) * LOG2E), e1 = __builtin_amdgcn_exp2f((m1 - M) * LOG2E), e2 = __builtin_amdgcn_exp2f((m2 - M) * LOG2E), e3 = __builtin_amdgcn_exp2f((m3 - M) * LOG2E);
    const float L = e0 * r1[1] + e1 * r1[133] + e2 * r1[265] + e3 * r1[397];
    const int d0 = 4 + 16 * w + 4 * g;
    f32x4 o = *(const f32x4*)&r1[d0] * e0 + *(const f32x4*)&r1[132 + d0] * e1 + *(const f32x4*)&r1[264 + d0] * e2 + *(const f32x4*)&r1[396 + d0] * e3;
    o *= 1.f / L;
    f32x4 as = {0.f, 0.f, 0.f, 0.f};
#pragma unroll
    for (int i = 0; i < 4; ++i) as = mfma(sel(pk2(o[i], o[i]), sl), wor[i], as);
    *(f32x4*)&lds[L_SR + w * 256 + (g >> 1) * 128 + 8 * c + 4 * (g & 1)] = as;
  }
  __syncthreads();
  if (tid < 160) {
    const int bo = tid >= 80, col = tid - 80 * bo;
    float v = 0.f;
#pragma unroll
    for (int ww = 0; ww < 8; ++ww) v += lds[L_SR + ww * 256 + bo * 128 + col];
    st_granule(rws, WS_H2 + pid * H2_STRIDE + tid * 8, v, tag);
  }
  STAMP(6);
  if (h < 5 && !(ABL & 32)) {
    const u32 o2 = WS_H2 + ((4 * w + g) * 8 + j) * H2_STRIDE + fk0 * 8;
    u32x4 aS = {0u, 0u, 0u, 0u}, aF = aS;
    bool nS = true, nF = true;
    for (u32 spins = 0; spins < SPIN_CAP; ++spins) {
      asm volatile("" ::: "memory");
      if (nS) aS = ld128<AUX_SC1>(rws, o2, 0);
      if (nF) aF = ld128<AUX_SC1>(rws, o2 + 1280, 0);
      nS = !(aS[1] == tag && aS[3] == tag); nF = !(aF[1] == tag && aF[3] == tag);
      if (!__any(nS || nF)) break;
      if (POLL2) __builtin_amdgcn_s_sleep(POLL2);
    }
    const f32x4 part = {xrow_sum(__uint_as_float(aS[0])), xrow_sum(__uint_as_float(aS[2])), xrow_sum(__uint_as_float(aF[0])), xrow_sum(__uint_as_float(aF[2]))};
    if (g == 0) *(f32x4*)&lds[L_FIN + w * 64 + 4 * c] = part;
    __syncthreads();
    if (w == 7) {
      f32x4 t = {0.f, 0.f, 0.f, 0.f};
#pragma unroll
      for (int ww = 0; ww < 8; ++ww) t += *(const f32x4*)&lds[L_FIN + ww * 64 + 4 * c];
      const float s0 = rbf(t[0]), s1 = rbf(t[1]), f0 = rbf(t[2]), f1 = rbf(t[3]);
      const float g0 = rbf(f0 / (1.f + __builtin_amdgcn_exp2f(-f0 * LOG2E))), g1 = rbf(f1 / (1.f + __builtin_amdgcn_exp2f(-f1 * LOG2E)));
      if (g == 0) *(u32*)(p.out + fbo * D + fcol) = pk2(lo16(xraw) + rbf(s0 * g0), hi16(xraw) + rbf(s1 * g1));
      if (bid == 0 && lane == 0) __builtin_amdgcn_raw_buffer_store_b32(tag, rws, WS_EPOCH, 0, AUX_SC1);
      STAMP(7);
    if (bid == 0) {
      const u32 tend = (u32)__builtin_amdgcn_s_memrealtime();
      int mnS = 1 << 30, mxQ = -(1 << 30), mnE = 1 << 30, mxE = -(1 << 30), sE = 0, sQ = 0, s13 = 0;
      for (int i = 0; i < 4; ++i) {
        const u32x4 e = ld128<AUX_SC1>(rws, WS_DIAG + (lane + 64 * i) * 16, 0);
        const int ds = (int)(e[0] - dgs), dq = (int)(e[1] - dgs), de = (int)(e[2] - dgs), d13 = (int)(e[3] - dgs);
        mnS = min(mnS, ds); mxQ = max(mxQ, dq); mnE = min(mnE, de); mxE = max(mxE, de); sE += de; sQ += dq; s13 += d13;
      }
      for (int o = 32; o >= 1; o >>= 1) { mnS = min(mnS, __shfl_xor(mnS, o)); mxQ = max(mxQ, __shfl_xor(mxQ, o)); mnE = min(mnE, __shfl_xor(mnE, o)); mxE = max(mxE, __shfl_xor(mxE, o)); sE += __shfl_xor(sE, o); sQ += __shfl_xor(sQ, o); s13 += __shfl_xor(s13, o); }
      int meas = 0;
      if (DIAG == 1) meas = sQ / 256 - mnS;
      if (DIAG == 2) meas = s13 / 256 - mnS;
      if (DIAG == 3) meas = sE / 256 - mnS;
      if (DIAG == 4) meas = mxE - mnS;
      if (DIAG == 6) meas = (int)(tend - dgs) - mnS;
      const u32 t1 = (u32)__builtin_amdgcn_s_memrealtime();
      u32 dl = (u32)max(meas, 0) * DELAYK;
      if (DIAG == 8) dl = 15000u - (t1 - (dgs + mnS));
      if (DIAG >= 9) { const int m2 = DIAG == 9 ? (int)(tend - dgs) - mnS : DIAG == 10 ? mxE - mnS : DIAG == 11 ? sE / 256 - mnS : DIAG == 12 ? sQ / 256 - mnS : s13 / 256 - mnS;
        dl = 15000u + (u32)m2 * DELAYK - (t1 - (dgs + mnS)); }
      while ((u32)__builtin_amdgcn_s_memrealtime() - t1 < dl) __builtin_amdgcn_s_sleep(1);
    }
    }
  }
  }
}
}

extern "C" void kernel_launch(void* const* d_in, const int* in_sizes, int n_in, void* d_out, int out_size, void* d_ws, size_t ws_size, hipStream_t stream) {
  Params p;
  p.x = (const u16*)d_in[0]; p.kc = (const u16*)d_in[1]; p.vc = (const u16*)d_in[2]; p.wq = (const u16*)d_in[3]; p.wo = (const u16*)d_in[4];
  p.wg = (const u16*)d_in[6]; p.wu = (const u16*)d_in[7]; p.wd = (const u16*)d_in[8];
  p.out = (u16*)d_out; p.ws = (unsigned char*)d_ws;
  fused_decode<<<NWG, NT, 0, stream>>>(p);
}
```

```cpp
#include <hip/hip_runtime.h>
#include <stdint.h>

#define SIF 3
#define NE 264
#define LOCK 0
#define POLL0 1
#define POLL1 1
#define POLL2 1
#define KAUX 2
#define VAUX 2
#define PACE 63
#define QSLEEP 0
#define ABL 0
#define MAPV 0
#define FINE 1
#define AMAP 3
#define LOCKN 0
#define PRIO 0
#define EXTRA 0
#define WIN 4
#define STAG 0
#define ALIGN 0
#define ONELANE 1
#define TSCHED 0
#define EARLY 1
#define NOBAR1 0
#define QMASK 0
#define BOOSTM 0
#define FULLPOLL 1
#define JCMAP 1
#define WAUX 0
#define STAMPS 0

namespace {
constexpr int D = 640, H = 32, SK = 16384, DH = 128, DF = 1728;
constexpr int NWG = 256, NT = 512;

typedef unsigned u32;
typedef unsigned long long u64;
typedef unsigned short u16;
typedef u32 u32x2 __attribute__((ext_vector_type(2)));
typedef u32 u32x4 __attribute__((ext_vector_type(4)));
typedef float f32x2 __attribute__((ext_vector_type(2)));
typedef float f32x4 __attribute__((ext_vector_type(4)));
typedef __bf16 bf16x2 __attribute__((ext_vector_type(2)));
typedef __bf16 bf16x8 __attribute__((ext_vector_type(8)));
typedef __amdgpu_buffer_rsrc_t rsrc_t;
#define GAS __attribute__((address_space(1)))

constexpr int AUX_NT = 2, AUX_SC1 = 16;
constexpr u32 OOB = 0x80000000u;

constexpr u32 WS_EPOCH = 0;
constexpr u32 WS_QP = 4096;
constexpr u32 WS_F1 = WS_QP + NWG * 2048;
constexpr u32 WS_H1 = WS_F1 + NWG * 2048;
constexpr u32 H1_STRIDE = 144 * 8;
constexpr u32 WS_H2 = WS_H1 + NWG * H1_STRIDE;
constexpr u32 H2_STRIDE = 320 * 8;
constexpr u32 WS_STAMP = WS_H2 + NWG * H2_STRIDE;
constexpr u32 WS_BYTES = WS_STAMP + NWG * 8 * 16 * 4;

constexpr int L_Q = 0;
constexpr int L_REC = L_Q + 64;
constexpr int L_F1 = L_REC + 8 * 132;
constexpr int L_R1 = L_F1 + 8 * 256;
constexpr int L_SR = L_R1 + 8 * 132;
constexpr int L_FR = L_SR + 8 * 256;
constexpr int L_FIN = L_FR + 8 * 256;
constexpr int L_USED = L_FIN + 8 * 64;
constexpr int L_END = L_USED;

constexpr float LOG2E = 1.4426950408889634f;
constexpr float SCALE = 0.08838834764831845f;
constexpr u32 SPIN_CAP = 1u << 18;

__device__ __forceinline__ float lo16(u32 v) { return __uint_as_float(v << 16); }
__device__ __forceinline__ float hi16(u32 v) { return __uint_as_float(v & 0xffff0000u); }
__device__ __forceinline__ u32 pk2(float a, float b) { bf16x2 r = {(__bf16)a, (__bf16)b}; return __builtin_bit_cast(u32, r); }
__device__ __forceinline__ float rbf(float a) { return lo16(pk2(a, a)); }
__device__ __forceinline__ float dot2(u32 a, u32 b, float c) { return __builtin_amdgcn_fdot2_f32_bf16(__builtin_bit_cast(bf16x2, a), __builtin_bit_cast(bf16x2, b), c, false); }
template <int CTRL> __device__ __forceinline__ float dppf(float x) { return __builtin_bit_cast(float, __builtin_amdgcn_mov_dpp(__builtin_bit_cast(int, x), CTRL, 0xf, 0xf, true)); }
template <int CTRL> __device__ __forceinline__ u32 dppu(u32 x) { return (u32)__builtin_amdgcn_mov_dpp((int)x, CTRL, 0xf, 0xf, true); }
constexpr int XOR1 = 0xB1, XOR2 = 0x4E, HMIR = 0x141, ROR4 = 0x124, ROR8 = 0x128, NEWB = 0x150;
__device__ __forceinline__ float xrow_max(float x) {
  auto s = __builtin_amdgcn_permlane16_swap(__float_as_uint(x), __float_as_uint(x), false, false);
  x = fmaxf(__uint_as_float(s[0]), __uint_as_float(s[1]));
  auto t = __builtin_amdgcn_permlane32_swap(__float_as_uint(x), __float_as_uint(x), false, false);
  return fmaxf(__uint_as_float(t[0]), __uint_as_float(t[1]));
}
__device__ __forceinline__ float xrow_sum(float x) {
  auto s = __builtin_amdgcn_permlane16_swap(__float_as_uint(x), __float_as_uint(x), false, false);
  x = __uint_as_float(s[0]) + __uint_as_float(s[1]);
  auto t = __builtin_amdgcn_permlane32_swap(__float_as_uint(x), __float_as_uint(x), false, false);
  return __uint_as_float(t[0]) + __uint_as_float(t[1]);
}
__device__ __forceinline__ f32x4 mfma(u32x4 a, u32x4 b, f32x4 c) {
  return __builtin_amdgcn_mfma_f32_16x16x32_bf16(__builtin_bit_cast(bf16x8, a), __builtin_bit_cast(bf16x8, b), c, 0, 0, 0);
}
__device__ __forceinline__ rsrc_t mk(const void* p, u32 bytes) { return __builtin_amdgcn_make_buffer_rsrc((void*)p, 0, bytes, 0x00020000); }
template <int AUX> __device__ __forceinline__ u32x4 ld128(rsrc_t r, u32 voff, u32 soff) { return __builtin_amdgcn_raw_buffer_load_b128(r, voff, soff, AUX); }
template <int AUX> __device__ __forceinline__ u32x2 ld64(rsrc_t r, u32 voff, u32 soff) { return __builtin_amdgcn_raw_buffer_load_b64(r, voff, soff, AUX); }
__device__ __forceinline__ void st_granule(rsrc_t r, u32 off, float v, u32 tag) { __builtin_amdgcn_raw_buffer_store_b64(u32x2{__float_as_uint(v), tag}, r, off, 0, AUX_SC1); }
__device__ __forceinline__ void st_granule2(rsrc_t r, u32 off, float v0, float v1, u32 tag) { __builtin_amdgcn_raw_buffer_store_b128(u32x4{__float_as_uint(v0), tag, __float_as_uint(v1), tag}, r, off, 0, AUX_SC1); }
#define SB() __builtin_amdgcn_sched_barrier(0)
#define STAMP(k) do { } while (0)
#define CSTAMP(k) do { } while (0)

struct Params {
  const u16 *x, *kc, *vc, *wq, *wo, *wg, *wu, *wd;
  u16* out;
  unsigned char* ws;
};

struct Sel { u32 k0, k1, k2, k3; };
__device__ __forceinline__ u32x4 sel(u32 pp, const Sel& s) { return u32x4{pp & s.k0, pp & s.k1, pp & s.k2, pp & s.k3}; }

__global__ void __launch_bounds__(NT, 2) fused_decode(Params p) {
  __shared__ __attribute__((aligned(16))) float lds[L_END];
  const int tid = threadIdx.x, lane = tid & 63, w = __builtin_amdgcn_readfirstlane(tid >> 6);
  const int g = lane >> 4, c = lane & 15, bb = c >> 3;
  const int bid = blockIdx.x, xc = bid & 7, jj = bid >> 3;
  const int xp = xc >> 1;
  const int par = xc & 1, b = (jj >> 1) & 1, half = jj & 1;
  const int h = MAPV == 0 ? xp + 4 * (jj >> 2) : MAPV == 1 ? (xp == 1 ? 3 : xp == 3 ? 1 : xp) + 4 * (jj >> 2) : MAPV == 2 ? ((xp + (jj >> 2)) & 3) + 4 * (jj >> 2) : ((xp ^ (jj >> 2)) & 3) + 4 * (jj >> 2);
  const int j = par + 2 * half + 4 * b;
  const int pid = h * 8 + j;
  const int jc = JCMAP ? 4 * par + (j >> 1) : j;
  const bool ffn = h < DF / 64;
  asm volatile("" ::: "v140");
  STAMP(0);
  const rsrc_t rws = mk(p.ws, WS_BYTES);
  const u32 tag = *(const __attribute__((address_space(4))) u32*)(p.ws + WS_EPOCH) + 0x9E3779B9u;

  const size_t slab = ((size_t)(b * H + h) * SK + half * (SK / 2)) * DH;
  const rsrc_t rk = mk(p.kc + slab, SK / 2 * DH * 2), rv = mk(p.vc + slab, SK / 2 * DH * 2);
  const int split = NE / 8 + (w < NE % 8 ? 1 : 0);
  const int nst = par ? 64 - split : split;
  const u32 kvbase = (par ? split : 0) * 32768u;
  u32 vo[4];
#pragma unroll
  for (int i = 0; i < 4; ++i)
    vo[i] = c * 16 + (AMAP == 0 ? w * 4096 + i * 1024 + g * 256
                    : AMAP == 1 ? g * 4096 + (i & 1) * 16384 + (i >> 1) * 256 + w * 512
                    : AMAP == 2 ? g * 8192 + (i & 1) * 4096 + (i >> 1) * 256 + w * 512
                    : AMAP == 3 ? g * 256 + i * 8192 + w * 1024
                    :             g * 256 + (i & 1) * 4096 + (i >> 1) * 16384 + (w & 3) * 1024 + (w >> 2) * 8192);
  u32x4 KR[SIF][4], VR[SIF][4];
  auto fillk = [&](int s, int step) __attribute__((always_inline)) {
    const u32 so = kvbase + step * 32768u;
#pragma unroll
    for (int i = 0; i < 4; ++i) KR[s][i] = ld128<KAUX>(rk, vo[i], so);
    SB();
  };
  auto fillv = [&](int s, int step) __attribute__((always_inline)) {
    const u32 so = kvbase + step * 32768u;
#pragma unroll
    for (int i = 0; i < 4; ++i) VR[s][i] = ld128<VAUX>(rv, vo[i], so);
    SB();
  };
  auto fill = [&](int s, int step) __attribute__((always_inline)) { fillk(s, step); fillv(s, step); };
  u32x4 wor[4], wdr[2];
  auto fillw = [&]() __attribute__((always_inline)) {
    const rsrc_t ro = mk(p.wo, H * DH * D * 2), rd = mk(p.wd, DF * D * 2);
    const u32 vo = (c < 10 && !(ABL & 2)) ? (128 * h + 16 * w + 4 * g) * (D * 2) + jc * 160 + c * 16 : OOB;
    const u32 vd = (ffn && c < 10 && !(ABL & 2)) ? (64 * h + 8 * w + 2 * g) * (D * 2) + jc * 160 + c * 16 : OOB;
#pragma unroll
    for (int i = 0; i < 4; ++i) wor[i] = ld128<WAUX>(ro, vo, i * (D * 2));
#pragma unroll
    for (int i = 0; i < 2; ++i) wdr[i] = ld128<WAUX>(rd, vd, i * (D * 2));
    SB();
  };
  Sel sl;
  {
    const int mm = lane & 7;
    const u32 hm = (mm & 1) ? 0xffff0000u : 0x0000ffffu;
    sl.k0 = (mm >> 1) == 0 ? hm : 0u; sl.k1 = (mm >> 1) == 1 ? hm : 0u; sl.k2 = (mm >> 1) == 2 ? hm : 0u; sl.k3 = (mm >> 1) == 3 ? hm : 0u;
  }

  if (!(ABL & 4) && (w == 0 || (w == 1 && ffn))) {
    u32x2 xs[5], xo[5];
    u32x4 wr[20];
    const rsrc_t rx = mk(p.x, 2 * D * 2);
#pragma unroll
    for (int i = 0; i < 5; ++i) xs[i] = ld64<0>(rx, (bb * D + 20 * (8 * g + (c & 7))) * 2 + i * 8, 0);
#pragma unroll
    for (int i = 0; i < 5; ++i) xo[i] = ld64<0>(rx, (bb * D + 80 * j + 20 * g) * 2 + i * 8, 0);
    const int ld = w == 0 ? H * DH : DF;
    const GAS u16* src = w == 0 ? (const GAS u16*)p.wq + h * DH + c * 8 : (const GAS u16*)(c < 8 ? p.wg : p.wu) + h * 64 + (c & 7) * 8;
    src += (80 * j + 20 * g) * ld;
#pragma unroll
    for (int i = 0; i < 20; ++i) wr[i] = __builtin_nontemporal_load((const GAS u32x4*)(src + i * ld));
    SB();
    if (!NOBAR1) __builtin_amdgcn_s_barrier();
    SB();
    float xv[20];
#pragma unroll
    for (int i = 0; i < 5; ++i) { xv[4 * i] = lo16(xs[i][0]); xv[4 * i + 1] = hi16(xs[i][0]); xv[4 * i + 2] = lo16(xs[i][1]); xv[4 * i + 3] = hi16(xs[i][1]); }
    float s = 0.f;
#pragma unroll
    for (int i = 0; i < 20; ++i) s += xv[i];
    s += dppf<XOR1>(s); s += dppf<XOR2>(s); s += dppf<HMIR>(s); s = xrow_sum(s);
    const float mean = s * (1.f / D);
    float q2 = 0.f;
#pragma unroll
    for (int i = 0; i < 20; ++i) { const float d = xv[i] - mean; q2 = fmaf(d, d, q2); }
    q2 += dppf<XOR1>(q2); q2 += dppf<XOR2>(q2); q2 += dppf<HMIR>(q2); q2 = xrow_sum(q2);
    const float mean_b = rbf(mean);
    const float rden = 1.f / rbf(__builtin_sqrtf(rbf(rbf(q2 * (1.f / D)) + 1e-5f)));
    STAMP(8);
    u32 xn[20];
#pragma unroll
    for (int i = 0; i < 5; ++i) {
      const float t0 = rbf(lo16(xo[i][0]) - mean_b) * rden, t1 = rbf(hi16(xo[i][0]) - mean_b) * rden, t2 = rbf(lo16(xo[i][1]) - mean_b) * rden, t3 = rbf(hi16(xo[i][1]) - mean_b) * rden;
      xn[4 * i] = pk2(t0, t0); xn[4 * i + 1] = pk2(t1, t1); xn[4 * i + 2] = pk2(t2, t2); xn[4 * i + 3] = pk2(t3, t3);
    }
    f32x4 a = {0.f, 0.f, 0.f, 0.f}, a2 = a;
#pragma unroll
    for (int i = 0; i < 20; i += 2) { a = mfma(sel(xn[i], sl), wr[i], a); a2 = mfma(sel(xn[i + 1], sl), wr[i + 1], a2); }
    a += a2;
    const u32 o = (w == 0 ? WS_QP : WS_F1) + pid * 2048 + lane * 16;
    st_granule2(rws, o, a[0], a[1], tag);
    st_granule2(rws, o + 1024, a[2], a[3], tag);
    STAMP(10);
    if (w == 0) {
      const u32 oq = WS_QP + h * 8 * 2048 + (lane >> 5) * 1024 + (32 * b + (lane & 31)) * 16;
      u32x4 qa[8];
      if (QSLEEP) __builtin_amdgcn_s_sleep(QSLEEP);
      u32 need = 0xffu;
      for (u32 spins = 0; spins < SPIN_CAP; ++spins) {
        asm volatile("" ::: "memory");
#pragma unroll
        for (int pp = 0; pp < 8; ++pp) if (!QMASK || (need >> pp & 1)) qa[pp] = ld128<AUX_SC1>(rws, oq + pp * 2048, 0);
#pragma unroll
        for (int pp = 0; pp < 8; ++pp) if (__all((qa[pp][1] == tag) & (qa[pp][3] == tag))) need &= ~(1u << pp);
        if (!need) break;
        if (POLL0) __builtin_amdgcn_s_sleep(POLL0);
      }
      f32x2 q = {0.f, 0.f};
#pragma unroll
      for (int pp = 0; pp < 8; ++pp) { q[0] += __uint_as_float(qa[pp][0]); q[1] += __uint_as_float(qa[pp][2]); }
      ((u32*)lds)[L_Q + 4 * (lane & 15) + 2 * ((lane >> 4) & 1) + (lane >> 5)] = pk2(q[0], q[1]);
      STAMP(9);
      __syncthreads();
#pragma unroll
      for (int s = 0; s < SIF; ++s) fill(s, s);
    } else {
#pragma unroll
      for (int s = 0; s < SIF; ++s) fill(s, s);
      __syncthreads();
    }
  } else {
    if (!NOBAR1) __builtin_amdgcn_s_barrier();
    SB();
    fillk(0, 0); fillv(0, 0);
#pragma unroll
    for (int s = 1; s < SIF; ++s) {
      asm volatile("s_waitcnt vmcnt(%0)" ::"n"(PACE) : "memory"); fillk(s, s);
      asm volatile("s_waitcnt vmcnt(%0)" ::"n"(PACE) : "memory"); fillv(s, s);
    }
    __syncthreads();
  }
  STAMP(1); CSTAMP(14);
  const u32x4 qv = *(const u32x4*)&lds[L_Q + 4 * c];

  float m = -INFINITY, l = 0.f;
  f32x4 acc = {0.f, 0.f, 0.f, 0.f};
  const bool c3 = (lane & 8) != 0, c2 = (lane & 4) != 0;
  float xd[4];
  auto dots = [&](int s) __attribute__((always_inline)) {
#pragma unroll
    for (int t = 0; t < 4; ++t) {
      float d = dot2(KR[s][t][0], qv[0], 0.f);
      d = dot2(KR[s][t][1], qv[1], d); d = dot2(KR[s][t][2], qv[2], d); xd[t] = dot2(KR[s][t][3], qv[3], d);
    }
    asm volatile("" : "+v"(xd[0]), "+v"(xd[1]), "+v"(xd[2]), "+v"(xd[3])); SB();
  };
  auto softmax = [&]() __attribute__((always_inline)) -> u32 {
    const float y0 = (c3 ? xd[2] : xd[0]) + dppf<ROR8>(c3 ? xd[0] : xd[2]);
    const float y1 = (c3 ? xd[3] : xd[1]) + dppf<ROR8>(c3 ? xd[1] : xd[3]);
    float sc = (c2 ? y1 : y0) + dppf<HMIR>(c2 ? y0 : y1);
    sc += dppf<XOR1>(sc); sc += dppf<XOR2>(sc);
    sc = rbf(rbf(sc) * SCALE);
    float pm = fmaxf(sc, dppf<ROR4>(sc)); pm = fmaxf(pm, dppf<ROR8>(pm)); pm = xrow_max(pm);
    const float mn = fmaxf(m, pm);
    const float al = __builtin_amdgcn_exp2f((m - mn) * LOG2E), pr = __builtin_amdgcn_exp2f((sc - mn) * LOG2E);
    m = mn; l = fmaf(l, al, pr);
    acc *= al;
    return pk2(pr, pr);
  };
  auto pv = [&](int s, u32 pp) __attribute__((always_inline)) {
    acc = mfma(sel(dppu<NEWB + 0>(pp), sl), VR[s][0], acc);
    acc = mfma(sel(dppu<NEWB + 4>(pp), sl), VR[s][1], acc);
    acc = mfma(sel(dppu<NEWB + 8>(pp), sl), VR[s][2], acc);
    acc = mfma(sel(dppu<NEWB + 12>(pp), sl), VR[s][3], acc);
    SB();
  };
  auto landedK = [&](int s) __attribute__((always_inline)) { asm volatile("" :: "v"(KR[s][0]), "v"(KR[s][1]), "v"(KR[s][2]), "v"(KR[s][3])); SB(); };
  auto landedV = [&](int s, u32 pp) __attribute__((always_inline)) { asm volatile("" :: "v"(VR[s][0]), "v"(VR[s][1]), "v"(VR[s][2]), "v"(VR[s][3]), "v"(pp)); SB(); };
  auto stepN = [&](int t, bool more) __attribute__((always_inline)) {
    landedK(0); fillv(0, t); dots(0);
    const u32 pp = softmax();
    landedV(0, pp); if (more) fillk(0, t + 1);
    pv(0, pp);
  };
  auto stepB = [&](int t, bool more) __attribute__((always_inline)) {
    dots(0); if (more) fillk(0, t + 1);
    const u32 pp = softmax();
    pv(0, pp);
  };
  { dots(0); const u32 pp = softmax(); landedV(0, pp); fillk(0, SIF); pv(0, pp); }
#pragma unroll
  for (int s = 1; s < SIF; ++s) { dots(s); const u32 pp = softmax(); pv(s, pp); }
  STAMP(13);
  fillw();
  auto ctl = [&](int t) __attribute__((always_inline)) -> bool {
    return BOOSTM == 2 || (BOOSTM == 1 && par) || (BOOSTM == 4 && !par);
  };
  int t = SIF;
  while (t < nst - 1) {
    bool boost = ctl(t);
    if (!boost) {
#pragma unroll 1
      do { stepN(t, true); ++t; boost = ctl(t); } while (!boost && t < nst - 1);
    } else {
      fillv(0, t);
#pragma unroll 1
      while (boost && t < nst - 2) { stepB(t, true); fillv(0, t + 1); ++t; boost = ctl(t); }
      stepB(t, true); ++t;
    }
  }
  const u32 of1 = ffn ? WS_F1 + (h * 8 + w) * 2048 + lane * 16 : OOB;
  u32x4 f0 = ld128<AUX_SC1>(rws, of1, 0), f1 = ld128<AUX_SC1>(rws, of1, 1024);
  const int fk0 = 32 * (h < 5 ? h : 0) + 2 * c, fbo = fk0 >= 80, fcol = 80 * jc + fk0 - 80 * fbo;
  const u32 xraw = __builtin_amdgcn_raw_buffer_load_b32(mk(p.x, 2 * D * 2), (fbo * D + fcol) * 2, 0, 0);
  SB();
  stepN(nst - 1, false);
  STAMP(3); CSTAMP(15);
  if (ffn && !(ABL & 4) && !__all(f0[1] == tag && f0[3] == tag && f1[1] == tag && f1[3] == tag)) {
    for (u32 spins = 0; spins < SPIN_CAP; ++spins) {
      __builtin_amdgcn_s_sleep(8);
      asm volatile("" ::: "memory");
      f0 = ld128<AUX_SC1>(rws, of1, 0); f1 = ld128<AUX_SC1>(rws, of1, 1024);
      if (__all(f0[1] == tag && f0[3] == tag && f1[1] == tag && f1[3] == tag)) break;
    }
  }

  {
    float ls = l; ls += dppf<XOR1>(ls); ls += dppf<XOR2>(ls); ls += dppf<ROR4>(ls); ls += dppf<ROR8>(ls); ls = xrow_sum(ls) * 0.25f;
    float* rec = &lds[L_REC + w * 132];
    if (lane < 32) *(f32x4*)&rec[4 + 8 * c + 4 * g] = acc;
    if (lane == 0) { rec[0] = m; rec[1] = ls; }
    u32* fb = (u32*)&lds[L_F1 + w * 256 + (g >> 1) * 128 + 8 * c + 4 * (g & 1)];
    *(u32x4*)fb = u32x4{f0[0], f0[2], f1[0], f1[2]};
  }
  __syncthreads();
  STAMP(4);
  if (!(ABL & 8)) {
  auto ffn2 = [&]() __attribute__((always_inline)) {
    f32x4 af = {0.f, 0.f, 0.f, 0.f};
#pragma unroll
    for (int i = 0; i < 2; ++i) {
      float gs = 0.f, us = 0.f;
#pragma unroll
      for (int pp = 0; pp < 8; ++pp) { gs += lds[L_F1 + pp * 256 + bb * 128 + 8 * w + 2 * g + i]; us += lds[L_F1 + pp * 256 + bb * 128 + 64 + 8 * w + 2 * g + i]; }
      const float gg = rbf(gs), uu = rbf(us);
      const float z = 0.7978845608028654f * (gg + 0.044715f * gg * gg * gg);
      const float th = 1.f - 2.f / (__builtin_amdgcn_exp2f(z * (2.f * LOG2E)) + 1.f);
      const float hv = rbf(0.5f * gg * (1.f + th)) * uu;
      af = mfma(sel(pk2(hv, hv), sl), wdr[i], af);
    }
    *(f32x4*)&lds[L_FR + w * 256 + (g >> 1) * 128 + 8 * c + 4 * (g & 1)] = af;
    asm volatile("s_waitcnt lgkmcnt(0)" ::: "memory");
    __builtin_amdgcn_s_barrier();
    asm volatile("" ::: "memory");
  };
  if (w == 0) {
    float mw[8], M = -INFINITY;
#pragma unroll
    for (int ww = 0; ww < 8; ++ww) { mw[ww] = lds[L_REC + ww * 132]; M = fmaxf(M, mw[ww]); }
    f32x2 o = {0.f, 0.f}; float L = 0.f;
#pragma unroll
    for (int ww = 0; ww < 8; ++ww) {
      const float e = __builtin_amdgcn_exp2f((mw[ww] - M) * LOG2E);
      o += *(const f32x2*)&lds[L_REC + ww * 132 + 4 + 2 * lane] * e; L = fmaf(e, lds[L_REC + ww * 132 + 1], L);
    }
    const u32 o1 = WS_H1 + pid * H1_STRIDE;
    st_granule2(rws, o1 + lane * 16, o[0], o[1], tag);
    st_granule2(rws, lane == 0 ? o1 + 1024 : OOB, M, L, tag);
    float* r1 = &lds[L_R1 + j * 132];
    *(f32x2*)&r1[4 + 2 * lane] = o;
    if (lane == 0) *(f32x2*)&r1[0] = f32x2{M, L};
    SB();
    ffn2();
  } else {
    const int mj = j ^ w;
    const u32 o1 = WS_H1 + (h * 8 + mj) * H1_STRIDE;
    const u32 oml = lane == 0 ? o1 + 1024 : OOB;
    u32x4 ra = ld128<AUX_SC1>(rws, o1 + lane * 16, 0), rml = ld128<AUX_SC1>(rws, oml, 0);
    SB();
    ffn2();
    const bool have = __all((ra[1] == tag) & (ra[3] == tag) & ((lane != 0) | ((rml[1] == tag) & (rml[3] == tag))));
    SB();
    if (w < 4 && tid - 64 < 160) {
      const int k = tid - 64, bo = k >= 80, col = k - 80 * bo;
      float v = 0.f;
#pragma unroll
      for (int ww = 0; ww < 8; ++ww) v += lds[L_FR + ww * 256 + bo * 128 + col];
      st_granule(rws, WS_H2 + pid * H2_STRIDE + (160 + k) * 8, v, tag);
    }
    if (!have && !(ABL & 16)) {
      for (u32 spins = 0; spins < SPIN_CAP; ++spins) {
        if (POLL1) __builtin_amdgcn_s_sleep(POLL1);
        asm volatile("" ::: "memory");
        rml = ld128<AUX_SC1>(rws, oml, 0);
        if (FULLPOLL) {
          ra = ld128<AUX_SC1>(rws, o1 + lane * 16, 0);
          if (__all((ra[1] == tag) & (ra[3] == tag) & ((lane != 0) | ((rml[1] == tag) & (rml[3] == tag))))) break;
        } else if (__builtin_amdgcn_readfirstlane(rml[1]) == tag && __builtin_amdgcn_readfirstlane(rml[3]) == tag) {
          asm volatile("" ::: "memory");
          ra = ld128<AUX_SC1>(rws, o1 + lane * 16, 0);
          if (__all(ra[1] == tag && ra[3] == tag)) break;
        }
      }
    }
    float* r1 = &lds[L_R1 + mj * 132];
    *(u32x2*)&r1[4 + 2 * lane] = u32x2{ra[0], ra[2]};
    if (lane == 0) *(u32x2*)&r1[0] = u32x2{rml[0], rml[2]};
  }
  __syncthreads();
  STAMP(5);
  {
    const float* r1 = &lds[L_R1 + bb * 4 * 132];
    const float m0 = r1[0], m1 = r1[132], m2 = r1[264], m3 = r1[396];
    const float M = fmaxf(fmaxf(m0, m1), fmaxf(m2, m3));
    const float e0 = __builtin_amdgcn_exp2f((m0 - M) * LOG2E), e1 = __builtin_amdgcn_exp2f((m1 - M) * LOG2E), e2 = __builtin_amdgcn_exp2f((m2 - M) * LOG2E), e3 = __builtin_amdgcn_exp2f((m3 - M) * LOG2E);
    const float L = e0 * r1[1] + e1 * r1[133] + e2 * r1[265] + e3 * r1[397];
    const int d0 = 4 + 16 * w + 4 * g;
    f32x4 o = *(const f32x4*)&r1[d0] * e0 + *(const f32x4*)&r1[132 + d0] * e1 + *(const f32x4*)&r1[264 + d0] * e2 + *(const f32x4*)&r1[396 + d0] * e3;
    o *= __builtin_amdgcn_rcpf(L);
    const f32x4 z4 = {0.f, 0.f, 0.f, 0.f};
    f32x4 as = mfma(sel(pk2(o[0], o[0]), sl), wor[0], z4), as2 = mfma(sel(pk2(o[1], o[1]), sl), wor[1], z4);
    as = mfma(sel(pk2(o[2], o[2]), sl), wor[2], as); as2 = mfma(sel(pk2(o[3], o[3]), sl), wor[3], as2);
    as += as2;
    *(f32x4*)&lds[L_SR + w * 256 + (g >> 1) * 128 + 8 * c + 4 * (g & 1)] = as;
  }
  __syncthreads();
  if (tid < 160) {
    const int bo = tid >= 80, col = tid - 80 * bo;
    float v = 0.f;
#pragma unroll
    for (int ww = 0; ww < 8; ++ww) v += lds[L_SR + ww * 256 + bo * 128 + col];
    st_granule(rws, WS_H2 + pid * H2_STRIDE + tid * 8, v, tag);
  }
  STAMP(6);
  if (h < 5 && !(ABL & 32)) {
    const u32 o2 = WS_H2 + ((4 * w + g) * 8 + j) * H2_STRIDE + fk0 * 8;
    u32x4 aS = {0u, 0u, 0u, 0u}, aF = aS;
    bool nS = true, nF = true;
    for (u32 spins = 0; spins < SPIN_CAP; ++spins) {
      asm volatile("" ::: "memory");
      if (nS) aS = ld128<AUX_SC1>(rws, o2, 0);
      if (nF) aF = ld128<AUX_SC1>(rws, o2 + 1280, 0);
      nS = !(aS[1] == tag && aS[3] == tag); nF = !(aF[1] == tag && aF[3] == tag);
      if (!__any(nS || nF)) break;
      if (POLL2) __builtin_amdgcn_s_sleep(POLL2);
    }
    const f32x4 part = {xrow_sum(__uint_as_float(aS[0])), xrow_sum(__uint_as_float(aS[2])), xrow_sum(__uint_as_float(aF[0])), xrow_sum(__uint_as_float(aF[2]))};
    if (g == 0) *(f32x4*)&lds[L_FIN + w * 64 + 4 * c] = part;
    __syncthreads();
    if (w == 7) {
      f32x4 t = {0.f, 0.f, 0.f, 0.f};
#pragma unroll
      for (int ww = 0; ww < 8; ++ww) t += *(const f32x4*)&lds[L_FIN + ww * 64 + 4 * c];
      const float s0 = rbf(t[0]), s1 = rbf(t[1]), f0 = rbf(t[2]), f1 = rbf(t[3]);
      const float g0 = rbf(f0 / (1.f + __builtin_amdgcn_exp2f(-f0 * LOG2E))), g1 = rbf(f1 / (1.f + __builtin_amdgcn_exp2f(-f1 * LOG2E)));
      if (g == 0) *(u32*)(p.out + fbo * D + fcol) = pk2(lo16(xraw) + rbf(s0 * g0), hi16(xraw) + rbf(s1 * g1));
      if (bid == 0 && lane == 0) __builtin_amdgcn_raw_buffer_store_b32(tag, rws, WS_EPOCH, 0, AUX_SC1);
      STAMP(7);
    }
  }
  }
}
}

extern "C" void kernel_launch(void* const* d_in, const int* in_sizes, int n_in, void* d_out, int out_size, void* d_ws, size_t ws_size, hipStream_t stream) {
  Params p;
  p.x = (const u16*)d_in[0]; p.kc = (const u16*)d_in[1]; p.vc = (const u16*)d_in[2]; p.wq = (const u16*)d_in[3]; p.wo = (const u16*)d_in[4];
  p.wg = (const u16*)d_in[6]; p.wu = (const u16*)d_in[7]; p.wd = (const u16*)d_in[8];
  p.out = (u16*)d_out; p.ws = (unsigned char*)d_ws;
  fused_decode<<<NWG, NT, 0, stream>>>(p);
}
```

```cpp
#include <hip/hip_runtime.h>
#include <stdint.h>

#define NE 264
#define STAMPS 0

namespace {
constexpr int D = 640, H = 32, SK = 16384, DH = 128, DF = 1728;
constexpr int NWG = 256, NT = 512;
constexpr int PRE = 3;

typedef unsigned u32;
typedef unsigned long long u64;
typedef unsigned short u16;
typedef u32 u32x2 __attribute__((ext_vector_type(2)));
typedef u32 u32x4 __attribute__((ext_vector_type(4)));
typedef float f32x2 __attribute__((ext_vector_type(2)));
typedef float f32x4 __attribute__((ext_vector_type(4)));
typedef __bf16 bf16x2 __attribute__((ext_vector_type(2)));
typedef __bf16 bf16x8 __attribute__((ext_vector_type(8)));
typedef __amdgpu_buffer_rsrc_t rsrc_t;
#define GAS __attribute__((address_space(1)))

constexpr int AUX_NT = 2, AUX_SC1 = 16;
constexpr u32 OOB = 0x80000000u;

constexpr u32 WS_EPOCH = 0;
constexpr u32 WS_QP = 4096;
constexpr u32 WS_F1 = WS_QP + NWG * 2048;
constexpr u32 WS_H1 = WS_F1 + NWG * 2048;
constexpr u32 H1_STRIDE = 144 * 8;
constexpr u32 WS_H2 = WS_H1 + NWG * H1_STRIDE;
constexpr u32 H2_STRIDE = 320 * 8;
constexpr u32 WS_STAMP = WS_H2 + NWG * H2_STRIDE;
constexpr u32 WS_BYTES = WS_STAMP + (STAMPS ? NWG * 8 * 16 * 4 : 0);

constexpr int L_Q = 0;
constexpr int L_REC = L_Q + 64;
constexpr int L_F1 = L_REC + 8 * 132;
constexpr int L_R1 = L_F1 + 8 * 256;
constexpr int L_SR = L_R1 + 8 * 132;
constexpr int L_FR = L_SR + 8 * 256;
constexpr int L_FIN = L_FR + 8 * 256;
constexpr int L_END = L_FIN + 8 * 64;

constexpr float LOG2E = 1.4426950408889634f;
constexpr float SCALE = 0.08838834764831845f;
constexpr u32 SPIN_CAP = 1u << 18;

__device__ __forceinline__ float lo16(u32 v) { return __uint_as_float(v << 16); }
__device__ __forceinline__ float hi16(u32 v) { return __uint_as_float(v & 0xffff0000u); }
__device__ __forceinline__ u32 pk2(float a, float b) { bf16x2 r = {(__bf16)a, (__bf16)b}; return __builtin_bit_cast(u32, r); }
__device__ __forceinline__ float rbf(float a) { return lo16(pk2(a, a)); }
__device__ __forceinline__ float dot2(u32 a, u32 b, float c) { return __builtin_amdgcn_fdot2_f32_bf16(__builtin_bit_cast(bf16x2, a), __builtin_bit_cast(bf16x2, b), c, false); }
template <int CTRL> __device__ __forceinline__ float dppf(float x) { return __builtin_bit_cast(float, __builtin_amdgcn_mov_dpp(__builtin_bit_cast(int, x), CTRL, 0xf, 0xf, true)); }
template <int CTRL> __device__ __forceinline__ u32 dppu(u32 x) { return (u32)__builtin_amdgcn_mov_dpp((int)x, CTRL, 0xf, 0xf, true); }
constexpr int XOR1 = 0xB1, XOR2 = 0x4E, HMIR = 0x141, ROR4 = 0x124, ROR8 = 0x128, NEWB = 0x150;
__device__ __forceinline__ float xrow_max(float x) {
  auto s = __builtin_amdgcn_permlane16_swap(__float_as_uint(x), __float_as_uint(x), false, false);
  x = fmaxf(__uint_as_float(s[0]), __uint_as_float(s[1]));
  auto t = __builtin_amdgcn_permlane32_swap(__float_as_uint(x), __float_as_uint(x), false, false);
  return fmaxf(__uint_as_float(t[0]), __uint_as_float(t[1]));
}
__device__ __forceinline__ float xrow_sum(float x) {
  auto s = __builtin_amdgcn_permlane16_swap(__float_as_uint(x), __float_as_uint(x), false, false);
  x = __uint_as_float(s[0]) + __uint_as_float(s[1]);
  auto t = __builtin_amdgcn_permlane32_swap(__float_as_uint(x), __float_as_uint(x), false, false);
  return __uint_as_float(t[0]) + __uint_as_float(t[1]);
}
__device__ __forceinline__ f32x4 mfma(u32x4 a, u32x4 b, f32x4 c) {
  return __builtin_amdgcn_mfma_f32_16x16x32_bf16(__builtin_bit_cast(bf16x8, a), __builtin_bit_cast(bf16x8, b), c, 0, 0, 0);
}
__device__ __forceinline__ rsrc_t mk(const void* p, u32 bytes) { return __builtin_amdgcn_make_buffer_rsrc((void*)p, 0, bytes, 0x00020000); }
template <int AUX> __device__ __forceinline__ u32x4 ld128(rsrc_t r, u32 voff, u32 soff) { return __builtin_amdgcn_raw_buffer_load_b128(r, voff, soff, AUX); }
template <int AUX> __device__ __forceinline__ u32x2 ld64(rsrc_t r, u32 voff, u32 soff) { return __builtin_amdgcn_raw_buffer_load_b64(r, voff, soff, AUX); }
__device__ __forceinline__ void st_granule(rsrc_t r, u32 off, float v, u32 tag) { __builtin_amdgcn_raw_buffer_store_b64(u32x2{__float_as_uint(v), tag}, r, off, 0, AUX_SC1); }
__device__ __forceinline__ void st_granule2(rsrc_t r, u32 off, float v0, float v1, u32 tag) { __builtin_amdgcn_raw_buffer_store_b128(u32x4{__float_as_uint(v0), tag, __float_as_uint(v1), tag}, r, off, 0, AUX_SC1); }
__device__ __forceinline__ bool tagged(const u32x4& a, u32 tag) { return (a[1] == tag) & (a[3] == tag); }
#define SB() __builtin_amdgcn_sched_barrier(0)
#define FRESH() asm volatile("" ::: "memory")
#define STAMP(k) do { } while (0)
#define CSTAMP(k) do { } while (0)

struct Params {
  const u16 *x, *kc, *vc, *wq, *wo, *wg, *wu, *wd;
  u16* out;
  unsigned char* ws;
};

struct Sel { u32 k0, k1, k2, k3; };
__device__ __forceinline__ u32x4 sel(u32 pp, const Sel& s) { return u32x4{pp & s.k0, pp & s.k1, pp & s.k2, pp & s.k3}; }

__global__ void __launch_bounds__(NT, 2) fused_decode(Params p) {
  __shared__ __attribute__((aligned(16))) float lds[L_END];
  const int tid = threadIdx.x, lane = tid & 63, w = __builtin_amdgcn_readfirstlane(tid >> 6);
  const int g = lane >> 4, c = lane & 15, bb = c >> 3;
  const int bid = blockIdx.x, xc = bid & 7, jj = bid >> 3;
  const int par = xc & 1, h = (xc >> 1) + 4 * (jj >> 2), b = (jj >> 1) & 1, half = jj & 1;
  const int j = par + 2 * half + 4 * b;
  const int pid = h * 8 + j;
  const int jc = 4 * par + (j >> 1);
  const bool ffn = h < DF / 64;
  asm volatile("" ::: "v140");
  STAMP(0);
  const rsrc_t rws = mk(p.ws, WS_BYTES);
  const u32 tag = *(const __attribute__((address_space(4))) u32*)(p.ws + WS_EPOCH) + 0x9E3779B9u;

  const size_t slab = ((size_t)(b * H + h) * SK + half * (SK / 2)) * DH;
  const rsrc_t rk = mk(p.kc + slab, SK / 2 * DH * 2), rv = mk(p.vc + slab, SK / 2 * DH * 2);
  const int split = NE / 8 + (w < NE % 8 ? 1 : 0);
  const int nst = par ? 64 - split : split;
  const u32 kvbase = (par ? split : 0) * 32768u;
  u32 vo[4];
#pragma unroll
  for (int i = 0; i < 4; ++i) vo[i] = c * 16 + g * 256 + w * 1024 + i * 8192;
  u32x4 KR[PRE][4], VR[PRE][4];
  auto fillk = [&](int s, int step) __attribute__((always_inline)) {
#pragma unroll
    for (int i = 0; i < 4; ++i) KR[s][i] = ld128<AUX_NT>(rk, vo[i], kvbase + step * 32768u);
    SB();
  };
  auto fillv = [&](int s, int step) __attribute__((always_inline)) {
#pragma unroll
    for (int i = 0; i < 4; ++i) VR[s][i] = ld128<AUX_NT>(rv, vo[i], kvbase + step * 32768u);
    SB();
  };
  u32x4 wor[4], wdr[2];
  auto prefetch = [&]() __attribute__((always_inline)) {
#pragma unroll
    for (int s = 0; s < PRE; ++s) { fillk(s, s); fillv(s, s); }
    const rsrc_t ro = mk(p.wo, H * DH * D * 2), rd = mk(p.wd, DF * D * 2);
    const u32 vw = c < 10 ? (128 * h + 16 * w + 4 * g) * (D * 2) + jc * 160 + c * 16 : OOB;
    const u32 vd = (ffn && c < 10) ? (64 * h + 8 * w + 2 * g) * (D * 2) + jc * 160 + c * 16 : OOB;
#pragma unroll
    for (int i = 0; i < 4; ++i) wor[i] = ld128<0>(ro, vw, i * (D * 2));
#pragma unroll
    for (int i = 0; i < 2; ++i) wdr[i] = ld128<0>(rd, vd, i * (D * 2));
    SB();
  };
  Sel sl;
  {
    const int mm = lane & 7;
    const u32 hm = (mm & 1) ? 0xffff0000u : 0x0000ffffu;
    sl.k0 = (mm >> 1) == 0 ? hm : 0u; sl.k1 = (mm >> 1) == 1 ? hm : 0u; sl.k2 = (mm >> 1) == 2 ? hm : 0u; sl.k3 = (mm >> 1) == 3 ? hm : 0u;
  }

  if (w == 0 || (w == 1 && ffn)) {
    u32x2 xs[5], xo[5];
    u32x4 wr[20];
    const rsrc_t rx = mk(p.x, 2 * D * 2);
#pragma unroll
    for (int i = 0; i < 5; ++i) xs[i] = ld64<0>(rx, (bb * D + 20 * (8 * g + (c & 7))) * 2 + i * 8, 0);
#pragma unroll
    for (int i = 0; i < 5; ++i) xo[i] = ld64<0>(rx, (bb * D + 80 * j + 20 * g) * 2 + i * 8, 0);
    const int ld = w == 0 ? H * DH : DF;
    const GAS u16* src = w == 0 ? (const GAS u16*)p.wq + h * DH + c * 8 : (const GAS u16*)(c < 8 ? p.wg : p.wu) + h * 64 + (c & 7) * 8;
    src += (80 * j + 20 * g) * ld;
#pragma unroll
    for (int i = 0; i < 20; ++i) wr[i] = __builtin_nontemporal_load((const GAS u32x4*)(src + i * ld));
    SB();
    __builtin_amdgcn_s_barrier();
    SB();
    float xv[20];
#pragma unroll
    for (int i = 0; i < 5; ++i) { xv[4 * i] = lo16(xs[i][0]); xv[4 * i + 1] = hi16(xs[i][0]); xv[4 * i + 2] = lo16(xs[i][1]); xv[4 * i + 3] = hi16(xs[i][1]); }
    float s = 0.f;
#pragma unroll
    for (int i = 0; i < 20; ++i) s += xv[i];
    s += dppf<XOR1>(s); s += dppf<XOR2>(s); s += dppf<HMIR>(s); s = xrow_sum(s);
    const float mean = s * (1.f / D);
    float q2 = 0.f;
#pragma unroll
    for (int i = 0; i < 20; ++i) { const float d = xv[i] - mean; q2 = fmaf(d, d, q2); }
    q2 += dppf<XOR1>(q2); q2 += dppf<XOR2>(q2); q2 += dppf<HMIR>(q2); q2 = xrow_sum(q2);
    const float mean_b = rbf(mean);
    const float rden = 1.f / rbf(__builtin_sqrtf(rbf(rbf(q2 * (1.f / D)) + 1e-5f)));
    STAMP(8);
    u32 xn[20];
#pragma unroll
    for (int i = 0; i < 5; ++i) {
      const float t0 = rbf(lo16(xo[i][0]) - mean_b) * rden, t1 = rbf(hi16(xo[i][0]) - mean_b) * rden, t2 = rbf(lo16(xo[i][1]) - mean_b) * rden, t3 = rbf(hi16(xo[i][1]) - mean_b) * rden;
      xn[4 * i] = pk2(t0, t0); xn[4 * i + 1] = pk2(t1, t1); xn[4 * i + 2] = pk2(t2, t2); xn[4 * i + 3] = pk2(t3, t3);
    }
    f32x4 a = {0.f, 0.f, 0.f, 0.f}, a2 = a;
#pragma unroll
    for (int i = 0; i < 20; i += 2) { a = mfma(sel(xn[i], sl), wr[i], a); a2 = mfma(sel(xn[i + 1], sl), wr[i + 1], a2); }
    a += a2;
    const u32 o = (w == 0 ? WS_QP : WS_F1) + pid * 2048 + lane * 16;
    st_granule2(rws, o, a[0], a[1], tag);
    st_granule2(rws, o + 1024, a[2], a[3], tag);
    STAMP(10);
    if (w == 0) {
      const u32 oq = WS_QP + h * 8 * 2048 + (lane >> 5) * 1024 + (32 * b + (lane & 31)) * 16;
      u32x4 qa[8];
      for (u32 spins = 0; spins < SPIN_CAP; ++spins) {
        FRESH();
#pragma unroll
        for (int pp = 0; pp < 8; ++pp) qa[pp] = ld128<AUX_SC1>(rws, oq + pp * 2048, 0);
        bool ok = true;
#pragma unroll
        for (int pp = 0; pp < 8; ++pp) ok &= tagged(qa[pp], tag);
        if (__all(ok)) break;
        __builtin_amdgcn_s_sleep(1);
      }
      f32x2 q = {0.f, 0.f};
#pragma unroll
      for (int pp = 0; pp < 8; ++pp) { q[0] += __uint_as_float(qa[pp][0]); q[1] += __uint_as_float(qa[pp][2]); }
      ((u32*)lds)[L_Q + 4 * (lane & 15) + 2 * ((lane >> 4) & 1) + (lane >> 5)] = pk2(q[0], q[1]);
      STAMP(9);
      __syncthreads();
      prefetch();
    } else {
      prefetch();
      __syncthreads();
    }
  } else {
    __builtin_amdgcn_s_barrier();
    SB();
    prefetch();
    __syncthreads();
  }
  STAMP(1); CSTAMP(14);
  const u32x4 qv = *(const u32x4*)&lds[L_Q + 4 * c];

  float m = -INFINITY, l = 0.f;
  f32x4 acc = {0.f, 0.f, 0.f, 0.f};
  const bool c3 = (lane & 8) != 0, c2 = (lane & 4) != 0;
  float xd[4];
  auto dots = [&](int s) __attribute__((always_inline)) {
#pragma unroll
    for (int t = 0; t < 4; ++t) {
      float d = dot2(KR[s][t][0], qv[0], 0.f);
      d = dot2(KR[s][t][1], qv[1], d); d = dot2(KR[s][t][2], qv[2], d); xd[t] = dot2(KR[s][t][3], qv[3], d);
    }
    asm volatile("" : "+v"(xd[0]), "+v"(xd[1]), "+v"(xd[2]), "+v"(xd[3])); SB();
  };
  auto softmax = [&]() __attribute__((always_inline)) -> u32 {
    const float y0 = (c3 ? xd[2] : xd[0]) + dppf<ROR8>(c3 ? xd[0] : xd[2]);
    const float y1 = (c3 ? xd[3] : xd[1]) + dppf<ROR8>(c3 ? xd[1] : xd[3]);
    float sc = (c2 ? y1 : y0) + dppf<HMIR>(c2 ? y0 : y1);
    sc += dppf<XOR1>(sc); sc += dppf<XOR2>(sc);
    sc = rbf(rbf(sc) * SCALE);
    float pm = fmaxf(sc, dppf<ROR4>(sc)); pm = fmaxf(pm, dppf<ROR8>(pm)); pm = xrow_max(pm);
    const float mn = fmaxf(m, pm);
    const float al = __builtin_amdgcn_exp2f((m - mn) * LOG2E), pr = __builtin_amdgcn_exp2f((sc - mn) * LOG2E);
    m = mn; l = fmaf(l, al, pr);
    acc *= al;
    return pk2(pr, pr);
  };
  auto pv = [&](int s, u32 pp) __attribute__((always_inline)) {
    acc = mfma(sel(dppu<NEWB + 0>(pp), sl), VR[s][0], acc);
    acc = mfma(sel(dppu<NEWB + 4>(pp), sl), VR[s][1], acc);
    acc = mfma(sel(dppu<NEWB + 8>(pp), sl), VR[s][2], acc);
    acc = mfma(sel(dppu<NEWB + 12>(pp), sl), VR[s][3], acc);
    SB();
  };
  auto landedK = [&]() __attribute__((always_inline)) { asm volatile("" :: "v"(KR[0][0]), "v"(KR[0][1]), "v"(KR[0][2]), "v"(KR[0][3])); SB(); };
  auto landedV = [&](u32 pp) __attribute__((always_inline)) { asm volatile("" :: "v"(VR[0][0]), "v"(VR[0][1]), "v"(VR[0][2]), "v"(VR[0][3]), "v"(pp)); SB(); };
  auto step = [&](int t, bool more) __attribute__((always_inline)) {
    landedK(); fillv(0, t); dots(0);
    const u32 pp = softmax();
    landedV(pp); if (more) fillk(0, t + 1);
    pv(0, pp);
  };
  { dots(0); const u32 pp = softmax(); landedV(pp); fillk(0, PRE); pv(0, pp); }
#pragma unroll
  for (int s = 1; s < PRE; ++s) { dots(s); const u32 pp = softmax(); pv(s, pp); }
  STAMP(13);
#pragma unroll 1
  for (int t = PRE; t < nst - 1; ++t) {
    step(t, true);
  }
  const u32 of1 = ffn ? WS_F1 + (h * 8 + w) * 2048 + lane * 16 : OOB;
  u32x4 f0 = ld128<AUX_SC1>(rws, of1, 0), f1 = ld128<AUX_SC1>(rws, of1, 1024);
  const int fk0 = 32 * (h < 5 ? h : 0) + 2 * c, fbo = fk0 >= 80, fcol = 80 * jc + fk0 - 80 * fbo;
  const u32 xraw = __builtin_amdgcn_raw_buffer_load_b32(mk(p.x, 2 * D * 2), (fbo * D + fcol) * 2, 0, 0);
  SB();
  step(nst - 1, false);
  STAMP(3); CSTAMP(15);
  if (ffn && !__all(tagged(f0, tag) & tagged(f1, tag))) {
    for (u32 spins = 0; spins < SPIN_CAP; ++spins) {
      __builtin_amdgcn_s_sleep(8);
      FRESH();
      f0 = ld128<AUX_SC1>(rws, of1, 0); f1 = ld128<AUX_SC1>(rws, of1, 1024);
      if (__all(tagged(f0, tag) & tagged(f1, tag))) break;
    }
  }

  {
    float ls = l; ls += dppf<XOR1>(ls); ls += dppf<XOR2>(ls); ls += dppf<ROR4>(ls); ls += dppf<ROR8>(ls); ls = xrow_sum(ls) * 0.25f;
    float* rec = &lds[L_REC + w * 132];
    if (lane < 32) *(f32x4*)&rec[4 + 8 * c + 4 * g] = acc;
    if (lane == 0) { rec[0] = m; rec[1] = ls; }
    *(u32x4*)&lds[L_F1 + w * 256 + (g >> 1) * 128 + 8 * c + 4 * (g & 1)] = u32x4{f0[0], f0[2], f1[0], f1[2]};
  }
  __syncthreads();
  STAMP(4);
  auto ffn2 = [&]() __attribute__((always_inline)) {
    f32x4 af = {0.f, 0.f, 0.f, 0.f};
#pragma unroll
    for (int i = 0; i < 2; ++i) {
      float gs = 0.f, us = 0.f;
#pragma unroll
      for (int pp = 0; pp < 8; ++pp) { gs += lds[L_F1 + pp * 256 + bb * 128 + 8 * w + 2 * g + i]; us += lds[L_F1 + pp * 256 + bb * 128 + 64 + 8 * w + 2 * g + i]; }
      const float gg = rbf(gs), uu = rbf(us);
      const float z = 0.7978845608028654f * (gg + 0.044715f * gg * gg * gg);
      const float th = 1.f - 2.f / (__builtin_amdgcn_exp2f(z * (2.f * LOG2E)) + 1.f);
      const float hv = rbf(0.5f * gg * (1.f + th)) * uu;
      af = mfma(sel(pk2(hv, hv), sl), wdr[i], af);
    }
    *(f32x4*)&lds[L_FR + w * 256 + (g >> 1) * 128 + 8 * c + 4 * (g & 1)] = af;
    asm volatile("s_waitcnt lgkmcnt(0)" ::: "memory");
    __builtin_amdgcn_s_barrier();
    FRESH();
  };
  if (w == 0) {
    float mw[8], M = -INFINITY;
#pragma unroll
    for (int ww = 0; ww < 8; ++ww) { mw[ww] = lds[L_REC + ww * 132]; M = fmaxf(M, mw[ww]); }
    f32x2 o = {0.f, 0.f}; float L = 0.f;
#pragma unroll
    for (int ww = 0; ww < 8; ++ww) {
      const float e = __builtin_amdgcn_exp2f((mw[ww] - M) * LOG2E);
      o += *(const f32x2*)&lds[L_REC + ww * 132 + 4 + 2 * lane] * e; L = fmaf(e, lds[L_REC + ww * 132 + 1], L);
    }
    const u32 o1 = WS_H1 + pid * H1_STRIDE;
    st_granule2(rws, o1 + lane * 16, o[0], o[1], tag);
    st_granule2(rws, lane == 0 ? o1 + 1024 : OOB, M, L, tag);
    float* r1 = &lds[L_R1 + j * 132];
    *(f32x2*)&r1[4 + 2 * lane] = o;
    if (lane == 0) *(f32x2*)&r1[0] = f32x2{M, L};
    SB();
    ffn2();
  } else {
    const int mj = j ^ w;
    const u32 o1 = WS_H1 + (h * 8 + mj) * H1_STRIDE;
    const u32 oml = lane == 0 ? o1 + 1024 : OOB;
    u32x4 ra = ld128<AUX_SC1>(rws, o1 + lane * 16, 0), rml = ld128<AUX_SC1>(rws, oml, 0);
    SB();
    ffn2();
    const bool have = __all(tagged(ra, tag) & ((lane != 0) | tagged(rml, tag)));
    SB();
    if (w < 4 && tid - 64 < 160) {
      const int k = tid - 64, bo = k >= 80, col = k - 80 * bo;
      float v = 0.f;
#pragma unroll
      for (int ww = 0; ww < 8; ++ww) v += lds[L_FR + ww * 256 + bo * 128 + col];
      st_granule(rws, WS_H2 + pid * H2_STRIDE + (160 + k) * 8, v, tag);
    }
    if (!have) {
      for (u32 spins = 0; spins < SPIN_CAP; ++spins) {
        __builtin_amdgcn_s_sleep(1);
        FRESH();
        rml = ld128<AUX_SC1>(rws, oml, 0);
        ra = ld128<AUX_SC1>(rws, o1 + lane * 16, 0);
        if (__all(tagged(ra, tag) & ((lane != 0) | tagged(rml, tag)))) break;
      }
    }
    float* r1 = &lds[L_R1 + mj * 132];
    *(u32x2*)&r1[4 + 2 * lane] = u32x2{ra[0], ra[2]};
    if (lane == 0) *(u32x2*)&r1[0] = u32x2{rml[0], rml[2]};
  }
  __syncthreads();
  STAMP(5);
  {
    const float* r1 = &lds[L_R1 + bb * 4 * 132];
    const float m0 = r1[0], m1 = r1[132], m2 = r1[264], m3 = r1[396];
    const float M = fmaxf(fmaxf(m0, m1), fmaxf(m2, m3));
    const float e0 = __builtin_amdgcn_exp2f((m0 - M) * LOG2E), e1 = __builtin_amdgcn_exp2f((m1 - M) * LOG2E), e2 = __builtin_amdgcn_exp2f((m2 - M) * LOG2E), e3 = __builtin_amdgcn_exp2f((m3 - M) * LOG2E);
    const float L = e0 * r1[1] + e1 * r1[133] + e2 * r1[265] + e3 * r1[397];
    const int d0 = 4 + 16 * w + 4 * g;
    f32x4 o = *(const f32x4*)&r1[d0] * e0 + *(const f32x4*)&r1[132 + d0] * e1 + *(const f32x4*)&r1[264 + d0] * e2 + *(const f32x4*)&r1[396 + d0] * e3;
    o *= 1.f / L;
    const f32x4 z4 = {0.f, 0.f, 0.f, 0.f};
    f32x4 as = mfma(sel(pk2(o[0], o[0]), sl), wor[0], z4), as2 = mfma(sel(pk2(o[1], o[1]), sl), wor[1], z4);
    as = mfma(sel(pk2(o[2], o[2]), sl), wor[2], as); as2 = mfma(sel(pk2(o[3], o[3]), sl), wor[3], as2);
    *(f32x4*)&lds[L_SR + w * 256 + (g >> 1) * 128 + 8 * c + 4 * (g & 1)] = as + as2;
  }
  __syncthreads();
  if (tid < 160) {
    const int bo = tid >= 80, col = tid - 80 * bo;
    float v = 0.f;
#pragma unroll
    for (int ww = 0; ww < 8; ++ww) v += lds[L_SR + ww * 256 + bo * 128 + col];
    st_granule(rws, WS_H2 + pid * H2_STRIDE + tid * 8, v, tag);
  }
  STAMP(6);
  if (h < 5) {
    const u32 o2 = WS_H2 + ((4 * w + g) * 8 + j) * H2_STRIDE + fk0 * 8;
    u32x4 aS = {0u, 0u, 0u, 0u}, aF = aS;
    bool nS = true, nF = true;
    for (u32 spins = 0; spins < SPIN_CAP; ++spins) {
      FRESH();
      if (nS) aS = ld128<AUX_SC1>(rws, o2, 0);
      if (nF) aF = ld128<AUX_SC1>(rws, o2 + 1280, 0);
      nS = !tagged(aS, tag); nF = !tagged(aF, tag);
      if (!__any(nS || nF)) break;
      __builtin_amdgcn_s_sleep(1);
    }
    const f32x4 part = {xrow_sum(__uint_as_float(aS[0])), xrow_sum(__uint_as_float(aS[2])), xrow_sum(__uint_as_float(aF[0])), xrow_sum(__uint_as_float(aF[2]))};
    if (g == 0) *(f32x4*)&lds[L_FIN + w * 64 + 4 * c] = part;
    __syncthreads();
    if (w == 7) {
      f32x4 t = {0.f, 0.f, 0.f, 0.f};
#pragma unroll
      for (int ww = 0; ww < 8; ++ww) t += *(const f32x4*)&lds[L_FIN + ww * 64 + 4 * c];
      const float s0 = rbf(t[0]), s1 = rbf(t[1]), f0v = rbf(t[2]), f1v = rbf(t[3]);
      const float g0 = rbf(f0v / (1.f + __builtin_amdgcn_exp2f(-f0v * LOG2E))), g1 = rbf(f1v / (1.f + __builtin_amdgcn_exp2f(-f1v * LOG2E)));
      if (g == 0) *(u32*)(p.out + fbo * D + fcol) = pk2(lo16(xraw) + rbf(s0 * g0), hi16(xraw) + rbf(s1 * g1));
      if (bid == 0 && lane == 0) __builtin_amdgcn_raw_buffer_store_b32(tag, rws, WS_EPOCH, 0, AUX_SC1);
      STAMP(7);
    }
  }
}
}

extern "C" void kernel_launch(void* const* d_in, const int* in_sizes, int n_in, void* d_out, int out_size, void* d_ws, size_t ws_size, hipStream_t stream) {
  Params p;
  p.x = (const u16*)d_in[0]; p.kc = (const u16*)d_in[1]; p.vc = (const u16*)d_in[2]; p.wq = (const u16*)d_in[3]; p.wo = (const u16*)d_in[4];
  p.wg = (const u16*)d_in[6]; p.wu = (const u16*)d_in[7]; p.wd = (const u16*)d_in[8];
  p.out = (u16*)d_out; p.ws = (unsigned char*)d_ws;
  fused_decode<<<NWG, NT, 0, stream>>>(p);
}
```

```cpp
#include <hip/hip_runtime.h>
#include <stdint.h>

#define NE 264
#define STAMPS 0

namespace {
constexpr int D = 640, H = 32, SK = 16384, DH = 128, DF = 1728;
constexpr int NWG = 256, NT = 512;
constexpr int PRE = 6;
constexpr int MAXST = 34;

typedef unsigned u32;
typedef unsigned long long u64;
typedef unsigned short u16;
typedef u32 u32x2 __attribute__((ext_vector_type(2)));
typedef u32 u32x4 __attribute__((ext_vector_type(4)));
typedef float f32x2 __attribute__((ext_vector_type(2)));
typedef float f32x4 __attribute__((ext_vector_type(4)));
typedef __bf16 bf16x2 __attribute__((ext_vector_type(2)));
typedef __bf16 bf16x8 __attribute__((ext_vector_type(8)));
typedef __amdgpu_buffer_rsrc_t rsrc_t;
#define GAS __attribute__((address_space(1)))

constexpr int AUX_NT = 2, AUX_SC1 = 16;
constexpr u32 OOB = 0x80000000u;

constexpr u32 WS_EPOCH = 0;
constexpr u32 WS_QP = 4096;
constexpr u32 WS_F1 = WS_QP + NWG * 2048;
constexpr u32 WS_H1 = WS_F1 + NWG * 2048;
constexpr u32 H1_STRIDE = 144 * 8;
constexpr u32 WS_H2 = WS_H1 + NWG * H1_STRIDE;
constexpr u32 H2_STRIDE = 320 * 8;
constexpr u32 WS_STAMP = WS_H2 + NWG * H2_STRIDE;
constexpr u32 WS_BYTES = WS_STAMP + (STAMPS ? NWG * 8 * 16 * 4 : 0);

constexpr int L_Q = 0;
constexpr int L_REC = L_Q + 64;
constexpr int L_F1 = L_REC + 8 * 132;
constexpr int L_R1 = L_F1 + 8 * 256;
constexpr int L_SR = L_R1 + 8 * 132;
constexpr int L_FR = L_SR + 8 * 256;
constexpr int L_FIN = L_FR + 8 * 256;
constexpr int L_SC = L_FIN + 8 * 64;
constexpr int L_END = L_SC + 8 * MAXST * 16;

constexpr float LOG2E = 1.4426950408889634f;
constexpr float SCALE = 0.08838834764831845f;
constexpr u32 SPIN_CAP = 1u << 18;

__device__ __forceinline__ float lo16(u32 v) { return __uint_as_float(v << 16); }
__device__ __forceinline__ float hi16(u32 v) { return __uint_as_float(v & 0xffff0000u); }
__device__ __forceinline__ u32 pk2(float a, float b) { bf16x2 r = {(__bf16)a, (__bf16)b}; return __builtin_bit_cast(u32, r); }
__device__ __forceinline__ float rbf(float a) { return lo16(pk2(a, a)); }
__device__ __forceinline__ float dot2(u32 a, u32 b, float c) { return __builtin_amdgcn_fdot2_f32_bf16(__builtin_bit_cast(bf16x2, a), __builtin_bit_cast(bf16x2, b), c, false); }
template <int CTRL> __device__ __forceinline__ float dppf(float x) { return __builtin_bit_cast(float, __builtin_amdgcn_mov_dpp(__builtin_bit_cast(int, x), CTRL, 0xf, 0xf, true)); }
template <int CTRL> __device__ __forceinline__ u32 dppu(u32 x) { return (u32)__builtin_amdgcn_mov_dpp((int)x, CTRL, 0xf, 0xf, true); }
constexpr int XOR1 = 0xB1, XOR2 = 0x4E, HMIR = 0x141, ROR4 = 0x124, ROR8 = 0x128, NEWB = 0x150;
__device__ __forceinline__ float xrow_max(float x) {
  auto s = __builtin_amdgcn_permlane16_swap(__float_as_uint(x), __float_as_uint(x), false, false);
  x = fmaxf(__uint_as_float(s[0]), __uint_as_float(s[1]));
  auto t = __builtin_amdgcn_permlane32_swap(__float_as_uint(x), __float_as_uint(x), false, false);
  return fmaxf(__uint_as_float(t[0]), __uint_as_float(t[1]));
}
__device__ __forceinline__ float xrow_sum(float x) {
  auto s = __builtin_amdgcn_permlane16_swap(__float_as_uint(x), __float_as_uint(x), false, false);
  x = __uint_as_float(s[0]) + __uint_as_float(s[1]);
  auto t = __builtin_amdgcn_permlane32_swap(__float_as_uint(x), __float_as_uint(x), false, false);
  return __uint_as_float(t[0]) + __uint_as_float(t[1]);
}
__device__ __forceinline__ f32x4 mfma(u32x4 a, u32x4 b, f32x4 c) {
  return __builtin_amdgcn_mfma_f32_16x16x32_bf16(__builtin_bit_cast(bf16x8, a), __builtin_bit_cast(bf16x8, b), c, 0, 0, 0);
}
__device__ __forceinline__ rsrc_t mk(const void* p, u32 bytes) { return __builtin_amdgcn_make_buffer_rsrc((void*)p, 0, bytes, 0x00020000); }
template <int AUX> __device__ __forceinline__ u32x4 ld128(rsrc_t r, u32 voff, u32 soff) { return __builtin_amdgcn_raw_buffer_load_b128(r, voff, soff, AUX); }
template <int AUX> __device__ __forceinline__ u32x2 ld64(rsrc_t r, u32 voff, u32 soff) { return __builtin_amdgcn_raw_buffer_load_b64(r, voff, soff, AUX); }
__device__ __forceinline__ void st_granule(rsrc_t r, u32 off, float v, u32 tag) { __builtin_amdgcn_raw_buffer_store_b64(u32x2{__float_as_uint(v), tag}, r, off, 0, AUX_SC1); }
__device__ __forceinline__ void st_granule2(rsrc_t r, u32 off, float v0, float v1, u32 tag) { __builtin_amdgcn_raw_buffer_store_b128(u32x4{__float_as_uint(v0), tag, __float_as_uint(v1), tag}, r, off, 0, AUX_SC1); }
__device__ __forceinline__ bool tagged(const u32x4& a, u32 tag) { return (a[1] == tag) & (a[3] == tag); }
#define SB() __builtin_amdgcn_sched_barrier(0)
#define FRESH() asm volatile("" ::: "memory")
#define STAMP(k) do { } while (0)
#define CSTAMP(k) do { } while (0)

struct Params {
  const u16 *x, *kc, *vc, *wq, *wo, *wg, *wu, *wd;
  u16* out;
  unsigned char* ws;
};

struct Sel { u32 k0, k1, k2, k3; };
__device__ __forceinline__ u32x4 sel(u32 pp, const Sel& s) { return u32x4{pp & s.k0, pp & s.k1, pp & s.k2, pp & s.k3}; }

__global__ void __launch_bounds__(NT, 2) fused_decode(Params p) {
  __shared__ __attribute__((aligned(16))) float lds[L_END];
  const int tid = threadIdx.x, lane = tid & 63, w = __builtin_amdgcn_readfirstlane(tid >> 6);
  const int g = lane >> 4, c = lane & 15, bb = c >> 3;
  const int bid = blockIdx.x, xc = bid & 7, jj = bid >> 3;
  const int par = xc & 1, h = (xc >> 1) + 4 * (jj >> 2), b = (jj >> 1) & 1, half = jj & 1;
  const int j = par + 2 * half + 4 * b;
  const int pid = h * 8 + j;
  const int jc = 4 * par + (j >> 1);
  const bool ffn = h < DF / 64;
  asm volatile("" ::: "v140");
  STAMP(0);
  const rsrc_t rws = mk(p.ws, WS_BYTES);
  const u32 tag = *(const __attribute__((address_space(4))) u32*)(p.ws + WS_EPOCH) + 0x9E3779B9u;

  const size_t slab = ((size_t)(b * H + h) * SK + half * (SK / 2)) * DH;
  const rsrc_t rk = mk(p.kc + slab, SK / 2 * DH * 2), rv = mk(p.vc + slab, SK / 2 * DH * 2);
  const int split = NE / 8 + (w < NE % 8 ? 1 : 0);
  const int nst = par ? 64 - split : split;
  const u32 kvbase = (par ? split : 0) * 32768u;
  u32 vo[4];
#pragma unroll
  for (int i = 0; i < 4; ++i) vo[i] = c * 16 + g * 256 + w * 1024 + i * 8192;
  u32x4 R[PRE][4];
  auto fillk = [&](int s, int step) __attribute__((always_inline)) {
    const u32 so = step < nst ? kvbase + step * 32768u : OOB;
#pragma unroll
    for (int i = 0; i < 4; ++i) R[s][i] = ld128<AUX_NT>(rk, vo[i], so);
    SB();
  };
  auto fillv = [&](int s, int step) __attribute__((always_inline)) {
    const u32 so = step < nst ? kvbase + step * 32768u : OOB;
#pragma unroll
    for (int i = 0; i < 4; ++i) R[s][i] = ld128<AUX_NT>(rv, vo[i], so);
    SB();
  };
  u32x4 wor[4], wdr[2];
  auto prefetch = [&]() __attribute__((always_inline)) {
#pragma unroll
    for (int s = 0; s < PRE; ++s) fillk(s, s);
    const rsrc_t ro = mk(p.wo, H * DH * D * 2), rd = mk(p.wd, DF * D * 2);
    const u32 vw = c < 10 ? (128 * h + 16 * w + 4 * g) * (D * 2) + jc * 160 + c * 16 : OOB;
    const u32 vd = (ffn && c < 10) ? (64 * h + 8 * w + 2 * g) * (D * 2) + jc * 160 + c * 16 : OOB;
#pragma unroll
    for (int i = 0; i < 4; ++i) wor[i] = ld128<0>(ro, vw, i * (D * 2));
#pragma unroll
    for (int i = 0; i < 2; ++i) wdr[i] = ld128<0>(rd, vd, i * (D * 2));
    SB();
  };
  Sel sl;
  {
    const int mm = lane & 7;
    const u32 hm = (mm & 1) ? 0xffff0000u : 0x0000ffffu;
    sl.k0 = (mm >> 1) == 0 ? hm : 0u; sl.k1 = (mm >> 1) == 1 ? hm : 0u; sl.k2 = (mm >> 1) == 2 ? hm : 0u; sl.k3 = (mm >> 1) == 3 ? hm : 0u;
  }

  if (w == 0 || (w == 1 && ffn)) {
    u32x2 xs[5], xo[5];
    u32x4 wr[20];
    const rsrc_t rx = mk(p.x, 2 * D * 2);
#pragma unroll
    for (int i = 0; i < 5; ++i) xs[i] = ld64<0>(rx, (bb * D + 20 * (8 * g + (c & 7))) * 2 + i * 8, 0);
#pragma unroll
    for (int i = 0; i < 5; ++i) xo[i] = ld64<0>(rx, (bb * D + 80 * j + 20 * g) * 2 + i * 8, 0);
    const int ld = w == 0 ? H * DH : DF;
    const GAS u16* src = w == 0 ? (const GAS u16*)p.wq + h * DH + c * 8 : (const GAS u16*)(c < 8 ? p.wg : p.wu) + h * 64 + (c & 7) * 8;
    src += (80 * j + 20 * g) * ld;
#pragma unroll
    for (int i = 0; i < 20; ++i) wr[i] = __builtin_nontemporal_load((const GAS u32x4*)(src + i * ld));
    SB();
    __builtin_amdgcn_s_barrier();
    SB();
    float xv[20];
#pragma unroll
    for (int i = 0; i < 5; ++i) { xv[4 * i] = lo16(xs[i][0]); xv[4 * i + 1] = hi16(xs[i][0]); xv[4 * i + 2] = lo16(xs[i][1]); xv[4 * i + 3] = hi16(xs[i][1]); }
    float s = 0.f;
#pragma unroll
    for (int i = 0; i < 20; ++i) s += xv[i];
    s += dppf<XOR1>(s); s += dppf<XOR2>(s); s += dppf<HMIR>(s); s = xrow_sum(s);
    const float mean = s * (1.f / D);
    float q2 = 0.f;
#pragma unroll
    for (int i = 0; i < 20; ++i) { const float d = xv[i] - mean; q2 = fmaf(d, d, q2); }
    q2 += dppf<XOR1>(q2); q2 += dppf<XOR2>(q2); q2 += dppf<HMIR>(q2); q2 = xrow_sum(q2);
    const float mean_b = rbf(mean);
    const float rden = 1.f / rbf(__builtin_sqrtf(rbf(rbf(q2 * (1.f / D)) + 1e-5f)));
    STAMP(8);
    u32 xn[20];
#pragma unroll
    for (int i = 0; i < 5; ++i) {
      const float t0 = rbf(lo16(xo[i][0]) - mean_b) * rden, t1 = rbf(hi16(xo[i][0]) - mean_b) * rden, t2 = rbf(lo16(xo[i][1]) - mean_b) * rden, t3 = rbf(hi16(xo[i][1]) - mean_b) * rden;
      xn[4 * i] = pk2(t0, t0); xn[4 * i + 1] = pk2(t1, t1); xn[4 * i + 2] = pk2(t2, t2); xn[4 * i + 3] = pk2(t3, t3);
    }
    f32x4 a = {0.f, 0.f, 0.f, 0.f}, a2 = a;
#pragma unroll
    for (int i = 0; i < 20; i += 2) { a = mfma(sel(xn[i], sl), wr[i], a); a2 = mfma(sel(xn[i + 1], sl), wr[i + 1], a2); }
    a += a2;
    const u32 o = (w == 0 ? WS_QP : WS_F1) + pid * 2048 + lane * 16;
    st_granule2(rws, o, a[0], a[1], tag);
    st_granule2(rws, o + 1024, a[2], a[3], tag);
    STAMP(10);
    if (w == 0) {
      const u32 oq = WS_QP + h * 8 * 2048 + (lane >> 5) * 1024 + (32 * b + (lane & 31)) * 16;
      u32x4 qa[8];
      for (u32 spins = 0; spins < SPIN_CAP; ++spins) {
        FRESH();
#pragma unroll
        for (int pp = 0; pp < 8; ++pp) qa[pp] = ld128<AUX_SC1>(rws, oq + pp * 2048, 0);
        bool ok = true;
#pragma unroll
        for (int pp = 0; pp < 8; ++pp) ok &= tagged(qa[pp], tag);
        if (__all(ok)) break;
        __builtin_amdgcn_s_sleep(1);
      }
      f32x2 q = {0.f, 0.f};
#pragma unroll
      for (int pp = 0; pp < 8; ++pp) { q[0] += __uint_as_float(qa[pp][0]); q[1] += __uint_as_float(qa[pp][2]); }
      ((u32*)lds)[L_Q + 4 * (lane & 15) + 2 * ((lane >> 4) & 1) + (lane >> 5)] = pk2(q[0], q[1]);
      STAMP(9);
      __syncthreads();
      prefetch();
    } else {
      prefetch();
      __syncthreads();
    }
  } else {
    __builtin_amdgcn_s_barrier();
    SB();
    prefetch();
    __syncthreads();
  }
  STAMP(1); CSTAMP(14);
  const u32x4 qv = *(const u32x4*)&lds[L_Q + 4 * c];

  const bool c3 = (lane & 8) != 0, c2 = (lane & 4) != 0;
  float* sco = &lds[L_SC + w * (MAXST * 16) + g * 4 + (c >> 2)];
  float m = -INFINITY;
  auto landed = [&](int s) __attribute__((always_inline)) { asm volatile("" :: "v"(R[s][0]), "v"(R[s][1]), "v"(R[s][2]), "v"(R[s][3])); SB(); };
  auto score = [&](int s, int t) __attribute__((always_inline)) {
    float xd[4];
#pragma unroll
    for (int i = 0; i < 4; ++i) {
      float d = dot2(R[s][i][0], qv[0], 0.f);
      d = dot2(R[s][i][1], qv[1], d); d = dot2(R[s][i][2], qv[2], d); xd[i] = dot2(R[s][i][3], qv[3], d);
    }
    const float y0 = (c3 ? xd[2] : xd[0]) + dppf<ROR8>(c3 ? xd[0] : xd[2]);
    const float y1 = (c3 ? xd[3] : xd[1]) + dppf<ROR8>(c3 ? xd[1] : xd[3]);
    float sc = (c2 ? y1 : y0) + dppf<HMIR>(c2 ? y0 : y1);
    sc += dppf<XOR1>(sc); sc += dppf<XOR2>(sc);
    sc = rbf(rbf(sc) * SCALE);
    m = fmaxf(m, sc);
    sco[t * 16] = sc;
    SB();
  };
  score(0, 0); fillk(0, PRE);
#pragma unroll
  for (int s = 1; s < PRE; ++s) score(s, s);
  STAMP(13);
  int t = PRE;
#pragma unroll 1
  for (; t + 1 < nst; t += 2) {
    landed(0); fillk(1, t + 1); score(0, t);
    landed(1); fillk(0, t + 2); score(1, t + 1);
  }
  landed(0); fillv(1, 0);
  if (t < nst) score(0, t);
  STAMP(11);
  m = fmaxf(m, dppf<ROR4>(m)); m = fmaxf(m, dppf<ROR8>(m)); m = xrow_max(m);

  float l = 0.f;
  f32x4 acc = {0.f, 0.f, 0.f, 0.f};
  auto weigh = [&](int s, int tt) __attribute__((always_inline)) {
    const float pr = __builtin_amdgcn_exp2f((sco[tt * 16] - m) * LOG2E);
    l += pr;
    const u32 pp = pk2(pr, pr);
    acc = mfma(sel(dppu<NEWB + 0>(pp), sl), R[s][0], acc);
    acc = mfma(sel(dppu<NEWB + 4>(pp), sl), R[s][1], acc);
    acc = mfma(sel(dppu<NEWB + 8>(pp), sl), R[s][2], acc);
    acc = mfma(sel(dppu<NEWB + 12>(pp), sl), R[s][3], acc);
    SB();
  };
  t = 0;
#pragma unroll 1
  for (; t + 3 < nst; t += 2) {
    landed(1); fillv(0, t + 1); weigh(1, t);
    landed(0); fillv(1, t + 2); weigh(0, t + 1);
  }
  const u32 of1 = ffn ? WS_F1 + (h * 8 + w) * 2048 + lane * 16 : OOB;
  u32x4 f0 = ld128<AUX_SC1>(rws, of1, 0), f1 = ld128<AUX_SC1>(rws, of1, 1024);
  const int fk0 = 32 * (h < 5 ? h : 0) + 2 * c, fbo = fk0 >= 80, fcol = 80 * jc + fk0 - 80 * fbo;
  const u32 xraw = __builtin_amdgcn_raw_buffer_load_b32(mk(p.x, 2 * D * 2), (fbo * D + fcol) * 2, 0, 0);
  SB();
  landed(1); fillv(0, t + 1); weigh(1, t);
  landed(0); fillv(1, t + 2); weigh(0, t + 1);
  if (t + 2 < nst) weigh(1, t + 2);
  STAMP(3); CSTAMP(15);
  if (ffn && !__all(tagged(f0, tag) & tagged(f1, tag))) {
    for (u32 spins = 0; spins < SPIN_CAP; ++spins) {
      __builtin_amdgcn_s_sleep(8);
      FRESH();
      f0 = ld128<AUX_SC1>(rws, of1, 0); f1 = ld128<AUX_SC1>(rws, of1, 1024);
      if (__all(tagged(f0, tag) & tagged(f1, tag))) break;
    }
  }

  {
    float ls = l; ls += dppf<XOR1>(ls); ls += dppf<XOR2>(ls); ls += dppf<ROR4>(ls); ls += dppf<ROR8>(ls); ls = xrow_sum(ls) * 0.25f;
    float* rec = &lds[L_REC + w * 132];
    if (lane < 32) *(f32x4*)&rec[4 + 8 * c + 4 * g] = acc;
    if (lane == 0) { rec[0] = m; rec[1] = ls; }
    *(u32x4*)&lds[L_F1 + w * 256 + (g >> 1) * 128 + 8 * c + 4 * (g & 1)] = u32x4{f0[0], f0[2], f1[0], f1[2]};
  }
  __syncthreads();
  STAMP(4);
  auto ffn2 = [&]() __attribute__((always_inline)) {
    f32x4 af = {0.f, 0.f, 0.f, 0.f};
#pragma unroll
    for (int i = 0; i < 2; ++i) {
      float gs = 0.f, us = 0.f;
#pragma unroll
      for (int pp = 0; pp < 8; ++pp) { gs += lds[L_F1 + pp * 256 + bb * 128 + 8 * w + 2 * g + i]; us += lds[L_F1 + pp * 256 + bb * 128 + 64 + 8 * w + 2 * g + i]; }
      const float gg = rbf(gs), uu = rbf(us);
      const float z = 0.7978845608028654f * (gg + 0.044715f * gg * gg * gg);
      const float th = 1.f - 2.f / (__builtin_amdgcn_exp2f(z * (2.f * LOG2E)) + 1.f);
      const float hv = rbf(0.5f * gg * (1.f + th)) * uu;
      af = mfma(sel(pk2(hv, hv), sl), wdr[i], af);
    }
    *(f32x4*)&lds[L_FR + w * 256 + (g >> 1) * 128 + 8 * c + 4 * (g & 1)] = af;
    asm volatile("s_waitcnt lgkmcnt(0)" ::: "memory");
    __builtin_amdgcn_s_barrier();
    FRESH();
  };
  if (w == 0) {
    float mw[8], M = -INFINITY;
#pragma unroll
    for (int ww = 0; ww < 8; ++ww) { mw[ww] = lds[L_REC + ww * 132]; M = fmaxf(M, mw[ww]); }
    f32x2 o = {0.f, 0.f}; float L = 0.f;
#pragma unroll
    for (int ww = 0; ww < 8; ++ww) {
      const float e = __builtin_amdgcn_exp2f((mw[ww] - M) * LOG2E);
      o += *(const f32x2*)&lds[L_REC + ww * 132 + 4 + 2 * lane] * e; L = fmaf(e, lds[L_REC + ww * 132 + 1], L);
    }
    const u32 o1 = WS_H1 + pid * H1_STRIDE;
    st_granule2(rws, o1 + lane * 16, o[0], o[1], tag);
    st_granule2(rws, lane == 0 ? o1 + 1024 : OOB, M, L, tag);
    float* r1 = &lds[L_R1 + j * 132];
    *(f32x2*)&r1[4 + 2 * lane] = o;
    if (lane == 0) *(f32x2*)&r1[0] = f32x2{M, L};
    SB();
    ffn2();
  } else {
    const int mj = j ^ w;
    const u32 o1 = WS_H1 + (h * 8 + mj) * H1_STRIDE;
    const u32 oml = lane == 0 ? o1 + 1024 : OOB;
    u32x4 ra = ld128<AUX_SC1>(rws, o1 + lane * 16, 0), rml = ld128<AUX_SC1>(rws, oml, 0);
    SB();
    ffn2();
    const bool have = __all(tagged(ra, tag) & ((lane != 0) | tagged(rml, tag)));
    SB();
    if (w < 4 && tid - 64 < 160) {
      const int k = tid - 64, bo = k >= 80, col = k - 80 * bo;
      float v = 0.f;
#pragma unroll
      for (int ww = 0; ww < 8; ++ww) v += lds[L_FR + ww * 256 + bo * 128 + col];
      st_granule(rws, WS_H2 + pid * H2_STRIDE + (160 + k) * 8, v, tag);
    }
    if (!have) {
      for (u32 spins = 0; spins < SPIN_CAP; ++spins) {
        __builtin_amdgcn_s_sleep(1);
        FRESH();
        rml = ld128<AUX_SC1>(rws, oml, 0);
        ra = ld128<AUX_SC1>(rws, o1 + lane * 16, 0);
        if (__all(tagged(ra, tag) & ((lane != 0) | tagged(rml, tag)))) break;
      }
    }
    float* r1 = &lds[L_R1 + mj * 132];
    *(u32x2*)&r1[4 + 2 * lane] = u32x2{ra[0], ra[2]};
    if (lane == 0) *(u32x2*)&r1[0] = u32x2{rml[0], rml[2]};
  }
  __syncthreads();
  STAMP(5);
  {
    const float* r1 = &lds[L_R1 + bb * 4 * 132];
    const float m0 = r1[0], m1 = r1[132], m2 = r1[264], m3 = r1[396];
    const float M = fmaxf(fmaxf(m0, m1), fmaxf(m2, m3));
    const float e0 = __builtin_amdgcn_exp2f((m0 - M) * LOG2E), e1 = __builtin_amdgcn_exp2f((m1 - M) * LOG2E), e2 = __builtin_amdgcn_exp2f((m2 - M) * LOG2E), e3 = __builtin_amdgcn_exp2f((m3 - M) * LOG2E);
    const float L = e0 * r1[1] + e1 * r1[133] + e2 * r1[265] + e3 * r1[397];
    const int d0 = 4 + 16 * w + 4 * g;
    f32x4 o = *(const f32x4*)&r1[d0] * e0 + *(const f32x4*)&r1[132 + d0] * e1 + *(const f32x4*)&r1[264 + d0] * e2 + *(const f32x4*)&r1[396 + d0] * e3;
    o *= 1.f / L;
    const f32x4 z4 = {0.f, 0.f, 0.f, 0.f};
    f32x4 as = mfma(sel(pk2(o[0], o[0]), sl), wor[0], z4), as2 = mfma(sel(pk2(o[1], o[1]), sl), wor[1], z4);
    as = mfma(sel(pk2(o[2], o[2]), sl), wor[2], as); as2 = mfma(sel(pk2(o[3], o[3]), sl), wor[3], as2);
    *(f32x4*)&lds[L_SR + w * 256 + (g >> 1) * 128 + 8 * c + 4 * (g & 1)] = as + as2;
  }
  __syncthreads();
  if (tid < 160) {
    const int bo = tid >= 80, col = tid - 80 * bo;
    float v = 0.f;
#pragma unroll
    for (int ww = 0; ww < 8; ++ww) v += lds[L_SR + ww * 256 + bo * 128 + col];
    st_granule(rws, WS_H2 + pid * H2_STRIDE + tid * 8, v, tag);
  }
  STAMP(6);
  if (h < 5) {
    const u32 o2 = WS_H2 + ((4 * w + g) * 8 + j) * H2_STRIDE + fk0 * 8;
    u32x4 aS = {0u, 0u, 0u, 0u}, aF = aS;
    bool nS = true, nF = true;
    for (u32 spins = 0; spins < SPIN_CAP; ++spins) {
      FRESH();
      if (nS) aS = ld128<AUX_SC1>(rws, o2, 0);
      if (nF) aF = ld128<AUX_SC1>(rws, o2 + 1280, 0);
      nS = !tagged(aS, tag); nF = !tagged(aF, tag);
      if (!__any(nS || nF)) break;
      __builtin_amdgcn_s_sleep(1);
    }
    const f32x4 part = {xrow_sum(__uint_as_float(aS[0])), xrow_sum(__uint_as_float(aS[2])), xrow_sum(__uint_as_float(aF[0])), xrow_sum(__uint_as_float(aF[2]))};
    if (g == 0) *(f32x4*)&lds[L_FIN + w * 64 + 4 * c] = part;
    __syncthreads();
    if (w == 7) {
      f32x4 t = {0.f, 0.f, 0.f, 0.f};
#pragma unroll
      for (int ww = 0; ww < 8; ++ww) t += *(const f32x4*)&lds[L_FIN + ww * 64 + 4 * c];
      const float s0 = rbf(t[0]), s1 = rbf(t[1]), f0v = rbf(t[2]), f1v = rbf(t[3]);
      const float g0 = rbf(f0v / (1.f + __builtin_amdgcn_exp2f(-f0v * LOG2E))), g1 = rbf(f1v / (1.f + __builtin_amdgcn_exp2f(-f1v * LOG2E)));
      if (g == 0) *(u32*)(p.out + fbo * D + fcol) = pk2(lo16(xraw) + rbf(s0 * g0), hi16(xraw) + rbf(s1 * g1));
      if (bid == 0 && lane == 0) __builtin_amdgcn_raw_buffer_store_b32(tag, rws, WS_EPOCH, 0, AUX_SC1);
      STAMP(7);
    }
  }
}
}

extern "C" void kernel_launch(void* const* d_in, const int* in_sizes, int n_in, void* d_out, int out_size, void* d_ws, size_t ws_size, hipStream_t stream) {
  Params p;
  p.x = (const u16*)d_in[0]; p.kc = (const u16*)d_in[1]; p.vc = (const u16*)d_in[2]; p.wq = (const u16*)d_in[3]; p.wo = (const u16*)d_in[4];
  p.wg = (const u16*)d_in[6]; p.wu = (const u16*)d_in[7]; p.wd = (const u16*)d_in[8];
  p.out = (u16*)d_out; p.ws = (unsigned char*)d_ws;
  fused_decode<<<NWG, NT, 0, stream>>>(p);
}
```

```cpp
#include <hip/hip_runtime.h>
#include <stdint.h>

#define NE 264
#define STAMPS 0

namespace {
constexpr int D = 640, H = 32, SK = 16384, DH = 128, DF = 1728;
constexpr int NWG = 256, NT = 512;
constexpr int PRE = 6;
constexpr int MAXST = 34;

typedef unsigned u32;
typedef unsigned long long u64;
typedef unsigned short u16;
typedef u32 u32x2 __attribute__((ext_vector_type(2)));
typedef u32 u32x4 __attribute__((ext_vector_type(4)));
typedef float f32x2 __attribute__((ext_vector_type(2)));
typedef float f32x4 __attribute__((ext_vector_type(4)));
typedef __bf16 bf16x2 __attribute__((ext_vector_type(2)));
typedef __bf16 bf16x8 __attribute__((ext_vector_type(8)));
typedef __amdgpu_buffer_rsrc_t rsrc_t;
#define GAS __attribute__((address_space(1)))

constexpr int AUX_NT = 2, AUX_SC1 = 16;
constexpr u32 OOB = 0x80000000u;

constexpr u32 WS_EPOCH = 0;
constexpr u32 WS_QP = 4096;
constexpr u32 WS_F1 = WS_QP + NWG * 2048;
constexpr u32 WS_H1 = WS_F1 + NWG * 2048;
constexpr u32 H1_STRIDE = 144 * 8;
constexpr u32 WS_H2 = WS_H1 + NWG * H1_STRIDE;
constexpr u32 H2_STRIDE = 320 * 8;
constexpr u32 WS_STAMP = WS_H2 + NWG * H2_STRIDE;
constexpr u32 WS_BYTES = WS_STAMP + (STAMPS ? NWG * 8 * 16 * 4 : 0);

constexpr int L_Q = 0;
constexpr int L_REC = L_Q + 64;
constexpr int L_F1 = L_REC + 8 * 132;
constexpr int L_R1 = L_F1 + 8 * 256;
constexpr int L_SR = L_R1 + 8 * 132;
constexpr int L_FR = L_SR + 8 * 256;
constexpr int L_FIN = L_FR + 8 * 256;
constexpr int L_SC = L_FIN + 8 * 64;
constexpr int L_END = L_SC + 8 * MAXST * 16;

constexpr float LOG2E = 1.4426950408889634f;
constexpr float SCALE = 0.08838834764831845f;
constexpr u32 SPIN_CAP = 1u << 18;

__device__ __forceinline__ float lo16(u32 v) { return __uint_as_float(v << 16); }
__device__ __forceinline__ float hi16(u32 v) { return __uint_as_float(v & 0xffff0000u); }
__device__ __forceinline__ u32 pk2(float a, float b) { bf16x2 r = {(__bf16)a, (__bf16)b}; return __builtin_bit_cast(u32, r); }
__device__ __forceinline__ float rbf(float a) { return lo16(pk2(a, a)); }
__device__ __forceinline__ float dot2(u32 a, u32 b, float c) { return __builtin_amdgcn_fdot2_f32_bf16(__builtin_bit_cast(bf16x2, a), __builtin_bit_cast(bf16x2, b), c, false); }
template <int CTRL> __device__ __forceinline__ float dppf(float x) { return __builtin_bit_cast(float, __builtin_amdgcn_mov_dpp(__builtin_bit_cast(int, x), CTRL, 0xf, 0xf, true)); }
template <int CTRL> __device__ __forceinline__ u32 dppu(u32 x) { return (u32)__builtin_amdgcn_mov_dpp((int)x, CTRL, 0xf, 0xf, true); }
constexpr int XOR1 = 0xB1, XOR2 = 0x4E, HMIR = 0x141, ROR4 = 0x124, ROR8 = 0x128, NEWB = 0x150;
__device__ __forceinline__ float xrow_max(float x) {
  auto s = __builtin_amdgcn_permlane16_swap(__float_as_uint(x), __float_as_uint(x), false, false);
  x = fmaxf(__uint_as_float(s[0]), __uint_as_float(s[1]));
  auto t = __builtin_amdgcn_permlane32_swap(__float_as_uint(x), __float_as_uint(x), false, false);
  return fmaxf(__uint_as_float(t[0]), __uint_as_float(t[1]));
}
__device__ __forceinline__ float xrow_sum(float x) {
  auto s = __builtin_amdgcn_permlane16_swap(__float_as_uint(x), __float_as_uint(x), false, false);
  x = __uint_as_float(s[0]) + __uint_as_float(s[1]);
  auto t = __builtin_amdgcn_permlane32_swap(__float_as_uint(x), __float_as_uint(x), false, false);
  return __uint_as_float(t[0]) + __uint_as_float(t[1]);
}
__device__ __forceinline__ f32x4 mfma(u32x4 a, u32x4 b, f32x4 c) {
  return __builtin_amdgcn_mfma_f32_16x16x32_bf16(__builtin_bit_cast(bf16x8, a), __builtin_bit_cast(bf16x8, b), c, 0, 0, 0);
}
__device__ __forceinline__ rsrc_t mk(const void* p, u32 bytes) { return __builtin_amdgcn_make_buffer_rsrc((void*)p, 0, bytes, 0x00020000); }
template <int AUX> __device__ __forceinline__ u32x4 ld128(rsrc_t r, u32 voff, u32 soff) { return __builtin_amdgcn_raw_buffer_load_b128(r, voff, soff, AUX); }
template <int AUX> __device__ __forceinline__ u32x2 ld64(rsrc_t r, u32 voff, u32 soff) { return __builtin_amdgcn_raw_buffer_load_b64(r, voff, soff, AUX); }
__device__ __forceinline__ void st_granule(rsrc_t r, u32 off, float v, u32 tag) { __builtin_amdgcn_raw_buffer_store_b64(u32x2{__float_as_uint(v), tag}, r, off, 0, AUX_SC1); }
__device__ __forceinline__ void st_granule2(rsrc_t r, u32 off, float v0, float v1, u32 tag) { __builtin_amdgcn_raw_buffer_store_b128(u32x4{__float_as_uint(v0), tag, __float_as_uint(v1), tag}, r, off, 0, AUX_SC1); }
__device__ __forceinline__ bool tagged(const u32x4& a, u32 tag) { return (a[1] == tag) & (a[3] == tag); }
#define SB() __builtin_amdgcn_sched_barrier(0)
#define FRESH() asm volatile("" ::: "memory")
#define STAMP(k) do { } while (0)
#define CSTAMP(k) do { } while (0)

struct Params {
  const u16 *x, *kc, *vc, *wq, *wo, *wg, *wu, *wd;
  u16* out;
  unsigned char* ws;
};

struct Sel { u32 k0, k1, k2, k3; };
__device__ __forceinline__ u32x4 sel(u32 pp, const Sel& s) { return u32x4{pp & s.k0, pp & s.k1, pp & s.k2, pp & s.k3}; }

__global__ void __launch_bounds__(NT, 2) fused_decode(Params p) {
  __shared__ __attribute__((aligned(16))) float lds[L_END];
  const int tid = threadIdx.x, lane = tid & 63, w = __builtin_amdgcn_readfirstlane(tid >> 6);
  const int g = lane >> 4, c = lane & 15, bb = c >> 3;
  const int bid = blockIdx.x, xc = bid & 7, jj = bid >> 3;
  const int par = xc & 1, h = (xc >> 1) + 4 * (jj >> 2), b = (jj >> 1) & 1, half = jj & 1;
  const int j = par + 2 * half + 4 * b;
  const int pid = h * 8 + j;
  const int jc = 4 * par + (j >> 1);
  const bool ffn = h < DF / 64;
  asm volatile("" ::: "v140");
  STAMP(0);
  const rsrc_t rws = mk(p.ws, WS_BYTES);
  const u32 tag = *(const __attribute__((address_space(4))) u32*)(p.ws + WS_EPOCH) + 0x9E3779B9u;

  const size_t slab = ((size_t)(b * H + h) * SK + half * (SK / 2)) * DH;
  const rsrc_t rk = mk(p.kc + slab, SK / 2 * DH * 2), rv = mk(p.vc + slab, SK / 2 * DH * 2);
  const int split = NE / 8 + (w < NE % 8 ? 1 : 0);
  const int nst = par ? 64 - split : split;
  const u32 kvbase = (par ? split : 0) * 32768u;
  u32 vo[4];
#pragma unroll
  for (int i = 0; i < 4; ++i) vo[i] = c * 16 + g * 256 + w * 1024 + i * 8192;
  u32x4 R[PRE][4];
  u32x4 wor[4], wdr[2];
  auto prefetch = [&]() __attribute__((always_inline)) {
#pragma unroll
    for (int s = 0; s < PRE; ++s) {
#pragma unroll
      for (int i = 0; i < 4; ++i) R[s][i] = ld128<AUX_NT>(rk, vo[i], kvbase + s * 32768u);
      SB();
    }
    const rsrc_t ro = mk(p.wo, H * DH * D * 2), rd = mk(p.wd, DF * D * 2);
    const u32 vw = c < 10 ? (128 * h + 16 * w + 4 * g) * (D * 2) + jc * 160 + c * 16 : OOB;
    const u32 vd = (ffn && c < 10) ? (64 * h + 8 * w + 2 * g) * (D * 2) + jc * 160 + c * 16 : OOB;
#pragma unroll
    for (int i = 0; i < 4; ++i) wor[i] = ld128<0>(ro, vw, i * (D * 2));
#pragma unroll
    for (int i = 0; i < 2; ++i) wdr[i] = ld128<0>(rd, vd, i * (D * 2));
    SB();
  };
  Sel sl;
  {
    const int mm = lane & 7;
    const u32 hm = (mm & 1) ? 0xffff0000u : 0x0000ffffu;
    sl.k0 = (mm >> 1) == 0 ? hm : 0u; sl.k1 = (mm >> 1) == 1 ? hm : 0u; sl.k2 = (mm >> 1) == 2 ? hm : 0u; sl.k3 = (mm >> 1) == 3 ? hm : 0u;
  }

  if (w == 0 || (w == 1 && ffn)) {
    u32x2 xs[5], xo[5];
    u32x4 wr[20];
    const rsrc_t rx = mk(p.x, 2 * D * 2);
#pragma unroll
    for (int i = 0; i < 5; ++i) xs[i] = ld64<0>(rx, (bb * D + 20 * (8 * g + (c & 7))) * 2 + i * 8, 0);
#pragma unroll
    for (int i = 0; i < 5; ++i) xo[i] = ld64<0>(rx, (bb * D + 80 * j + 20 * g) * 2 + i * 8, 0);
    const int ld = w == 0 ? H * DH : DF;
    const GAS u16* src = w == 0 ? (const GAS u16*)p.wq + h * DH + c * 8 : (const GAS u16*)(c < 8 ? p.wg : p.wu) + h * 64 + (c & 7) * 8;
    src += (80 * j + 20 * g) * ld;
#pragma unroll
    for (int i = 0; i < 20; ++i) wr[i] = __builtin_nontemporal_load((const GAS u32x4*)(src + i * ld));
    SB();
    __builtin_amdgcn_s_barrier();
    SB();
    float xv[20];
#pragma unroll
    for (int i = 0; i < 5; ++i) { xv[4 * i] = lo16(xs[i][0]); xv[4 * i + 1] = hi16(xs[i][0]); xv[4 * i + 2] = lo16(xs[i][1]); xv[4 * i + 3] = hi16(xs[i][1]); }
    float s = 0.f;
#pragma unroll
    for (int i = 0; i < 20; ++i) s += xv[i];
    s += dppf<XOR1>(s); s += dppf<XOR2>(s); s += dppf<HMIR>(s); s = xrow_sum(s);
    const float mean = s * (1.f / D);
    float q2 = 0.f;
#pragma unroll
    for (int i = 0; i < 20; ++i) { const float d = xv[i] - mean; q2 = fmaf(d, d, q2); }
    q2 += dppf<XOR1>(q2); q2 += dppf<XOR2>(q2); q2 += dppf<HMIR>(q2); q2 = xrow_sum(q2);
    const float mean_b = rbf(mean);
    const float rden = 1.f / rbf(__builtin_sqrtf(rbf(rbf(q2 * (1.f / D)) + 1e-5f)));
    STAMP(8);
    u32 xn[20];
#pragma unroll
    for (int i = 0; i < 5; ++i) {
      const float t0 = rbf(lo16(xo[i][0]) - mean_b) * rden, t1 = rbf(hi16(xo[i][0]) - mean_b) * rden, t2 = rbf(lo16(xo[i][1]) - mean_b) * rden, t3 = rbf(hi16(xo[i][1]) - mean_b) * rden;
      xn[4 * i] = pk2(t0, t0); xn[4 * i + 1] = pk2(t1, t1); xn[4 * i + 2] = pk2(t2, t2); xn[4 * i + 3] = pk2(t3, t3);
    }
    f32x4 a = {0.f, 0.f, 0.f, 0.f}, a2 = a;
#pragma unroll
    for (int i = 0; i < 20; i += 2) { a = mfma(sel(xn[i], sl), wr[i], a); a2 = mfma(sel(xn[i + 1], sl), wr[i + 1], a2); }
    a += a2;
    const u32 o = (w == 0 ? WS_QP : WS_F1) + pid * 2048 + lane * 16;
    st_granule2(rws, o, a[0], a[1], tag);
    st_granule2(rws, o + 1024, a[2], a[3], tag);
    STAMP(10);
    if (w == 0) {
      const u32 oq = WS_QP + h * 8 * 2048 + (lane >> 5) * 1024 + (32 * b + (lane & 31)) * 16;
      u32x4 qa[8];
      for (u32 spins = 0; spins < SPIN_CAP; ++spins) {
        FRESH();
#pragma unroll
        for (int pp = 0; pp < 8; ++pp) qa[pp] = ld128<AUX_SC1>(rws, oq + pp * 2048, 0);
        bool ok = true;
#pragma unroll
        for (int pp = 0; pp < 8; ++pp) ok &= tagged(qa[pp], tag);
        if (__all(ok)) break;
        __builtin_amdgcn_s_sleep(1);
      }
      f32x2 q = {0.f, 0.f};
#pragma unroll
      for (int pp = 0; pp < 8; ++pp) { q[0] += __uint_as_float(qa[pp][0]); q[1] += __uint_as_float(qa[pp][2]); }
      ((u32*)lds)[L_Q + 4 * (lane & 15) + 2 * ((lane >> 4) & 1) + (lane >> 5)] = pk2(q[0], q[1]);
      STAMP(9);
      __syncthreads();
      prefetch();
    } else {
      prefetch();
      __syncthreads();
    }
  } else {
    __builtin_amdgcn_s_barrier();
    SB();
    prefetch();
    __syncthreads();
  }
  STAMP(1); CSTAMP(14);
  const u32x4 qv = *(const u32x4*)&lds[L_Q + 4 * c];

  const bool c3 = (lane & 8) != 0, c2 = (lane & 4) != 0;
  float* sco = &lds[L_SC + w * (MAXST * 16) + g * 4 + (c >> 2)];
  float m = -INFINITY;
  auto score = [&](int s, int t, int NEXT, int nx) __attribute__((always_inline)) {
    float xd[4];
#pragma unroll
    for (int i = 0; i < 4; ++i) {
      float d = dot2(R[s][i][0], qv[0], 0.f);
      d = dot2(R[s][i][1], qv[1], d); d = dot2(R[s][i][2], qv[2], d); xd[i] = dot2(R[s][i][3], qv[3], d);
      if (NEXT) {
        asm volatile("" : "+v"(xd[i])); SB();
        R[s][i] = ld128<AUX_NT>(NEXT == 1 ? rk : rv, vo[i], kvbase + nx * 32768u);
        SB();
      }
    }
    const float y0 = (c3 ? xd[2] : xd[0]) + dppf<ROR8>(c3 ? xd[0] : xd[2]);
    const float y1 = (c3 ? xd[3] : xd[1]) + dppf<ROR8>(c3 ? xd[1] : xd[3]);
    float sc = (c2 ? y1 : y0) + dppf<HMIR>(c2 ? y0 : y1);
    sc += dppf<XOR1>(sc); sc += dppf<XOR2>(sc);
    sc = rbf(rbf(sc) * SCALE);
    m = fmaxf(m, sc);
    sco[t * 16] = sc;
    SB();
  };
  score(0, 0, 1, PRE);
#pragma unroll
  for (int s = 1; s < PRE; ++s) score(s, s, 0, 0);
  STAMP(13);
#pragma unroll 1
  for (int t = PRE; t < nst - 1; ++t) {
    score(0, t, 1, t + 1);
  }
  score(0, nst - 1, 2, 0);
  STAMP(11);
  m = fmaxf(m, dppf<ROR4>(m)); m = fmaxf(m, dppf<ROR8>(m)); m = xrow_max(m);

  float l = 0.f;
  f32x4 acc = {0.f, 0.f, 0.f, 0.f};
  auto weigh = [&](int t, bool more) __attribute__((always_inline)) {
    const float pr = __builtin_amdgcn_exp2f((sco[t * 16] - m) * LOG2E);
    l += pr;
    const u32 pp = pk2(pr, pr);
    const u32x4 a0 = sel(dppu<NEWB + 0>(pp), sl), a1 = sel(dppu<NEWB + 4>(pp), sl), a2 = sel(dppu<NEWB + 8>(pp), sl), a3 = sel(dppu<NEWB + 12>(pp), sl);
    const u32 so = kvbase + (t + 1) * 32768u;
    acc = mfma(a0, R[0][0], acc); SB(); if (more) R[0][0] = ld128<AUX_NT>(rv, vo[0], so); SB();
    acc = mfma(a1, R[0][1], acc); SB(); if (more) R[0][1] = ld128<AUX_NT>(rv, vo[1], so); SB();
    acc = mfma(a2, R[0][2], acc); SB(); if (more) R[0][2] = ld128<AUX_NT>(rv, vo[2], so); SB();
    acc = mfma(a3, R[0][3], acc); SB(); if (more) R[0][3] = ld128<AUX_NT>(rv, vo[3], so); SB();
  };
#pragma unroll 1
  for (int t = 0; t < nst - 2; ++t) {
    weigh(t, true);
  }
  const u32 of1 = ffn ? WS_F1 + (h * 8 + w) * 2048 + lane * 16 : OOB;
  u32x4 f0 = ld128<AUX_SC1>(rws, of1, 0), f1 = ld128<AUX_SC1>(rws, of1, 1024);
  const int fk0 = 32 * (h < 5 ? h : 0) + 2 * c, fbo = fk0 >= 80, fcol = 80 * jc + fk0 - 80 * fbo;
  const u32 xraw = __builtin_amdgcn_raw_buffer_load_b32(mk(p.x, 2 * D * 2), (fbo * D + fcol) * 2, 0, 0);
  SB();
  weigh(nst - 2, true);
  weigh(nst - 1, false);
  STAMP(3); CSTAMP(15);
  if (ffn && !__all(tagged(f0, tag) & tagged(f1, tag))) {
    for (u32 spins = 0; spins < SPIN_CAP; ++spins) {
      __builtin_amdgcn_s_sleep(8);
      FRESH();
      f0 = ld128<AUX_SC1>(rws, of1, 0); f1 = ld128<AUX_SC1>(rws, of1, 1024);
      if (__all(tagged(f0, tag) & tagged(f1, tag))) break;
    }
  }

  {
    float ls = l; ls += dppf<XOR1>(ls); ls += dppf<XOR2>(ls); ls += dppf<ROR4>(ls); ls += dppf<ROR8>(ls); ls = xrow_sum(ls) * 0.25f;
    float* rec = &lds[L_REC + w * 132];
    if (lane < 32) *(f32x4*)&rec[4 + 8 * c + 4 * g] = acc;
    if (lane == 0) { rec[0] = m; rec[1] = ls; }
    *(u32x4*)&lds[L_F1 + w * 256 + (g >> 1) * 128 + 8 * c + 4 * (g & 1)] = u32x4{f0[0], f0[2], f1[0], f1[2]};
  }
  __syncthreads();
  STAMP(4);
  auto ffn2 = [&]() __attribute__((always_inline)) {
    f32x4 af = {0.f, 0.f, 0.f, 0.f};
#pragma unroll
    for (int i = 0; i < 2; ++i) {
      float gs = 0.f, us = 0.f;
#pragma unroll
      for (int pp = 0; pp < 8; ++pp) { gs += lds[L_F1 + pp * 256 + bb * 128 + 8 * w + 2 * g + i]; us += lds[L_F1 + pp * 256 + bb * 128 + 64 + 8 * w + 2 * g + i]; }
      const float gg = rbf(gs), uu = rbf(us);
      const float z = 0.7978845608028654f * (gg + 0.044715f * gg * gg * gg);
      const float th = 1.f - 2.f / (__builtin_amdgcn_exp2f(z * (2.f * LOG2E)) + 1.f);
      const float hv = rbf(0.5f * gg * (1.f + th)) * uu;
      af = mfma(sel(pk2(hv, hv), sl), wdr[i], af);
    }
    *(f32x4*)&lds[L_FR + w * 256 + (g >> 1) * 128 + 8 * c + 4 * (g & 1)] = af;
    asm volatile("s_waitcnt lgkmcnt(0)" ::: "memory");
    __builtin_amdgcn_s_barrier();
    FRESH();
  };
  if (w == 0) {
    float mw[8], M = -INFINITY;
#pragma unroll
    for (int ww = 0; ww < 8; ++ww) { mw[ww] = lds[L_REC + ww * 132]; M = fmaxf(M, mw[ww]); }
    f32x2 o = {0.f, 0.f}; float L = 0.f;
#pragma unroll
    for (int ww = 0; ww < 8; ++ww) {
      const float e = __builtin_amdgcn_exp2f((mw[ww] - M) * LOG2E);
      o += *(const f32x2*)&lds[L_REC + ww * 132 + 4 + 2 * lane] * e; L = fmaf(e, lds[L_REC + ww * 132 + 1], L);
    }
    const u32 o1 = WS_H1 + pid * H1_STRIDE;
    st_granule2(rws, o1 + lane * 16, o[0], o[1], tag);
    st_granule2(rws, lane == 0 ? o1 + 1024 : OOB, M, L, tag);
    float* r1 = &lds[L_R1 + j * 132];
    *(f32x2*)&r1[4 + 2 * lane] = o;
    if (lane == 0) *(f32x2*)&r1[0] = f32x2{M, L};
    SB();
    ffn2();
  } else {
    const int mj = j ^ w;
    const u32 o1 = WS_H1 + (h * 8 + mj) * H1_STRIDE;
    const u32 oml = lane == 0 ? o1 + 1024 : OOB;
    u32x4 ra = ld128<AUX_SC1>(rws, o1 + lane * 16, 0), rml = ld128<AUX_SC1>(rws, oml, 0);
    SB();
    ffn2();
    const bool have = __all(tagged(ra, tag) & ((lane != 0) | tagged(rml, tag)));
    SB();
    if (w < 4 && tid - 64 < 160) {
      const int k = tid - 64, bo = k >= 80, col = k - 80 * bo;
      float v = 0.f;
#pragma unroll
      for (int ww = 0; ww < 8; ++ww) v += lds[L_FR + ww * 256 + bo * 128 + col];
      st_granule(rws, WS_H2 + pid * H2_STRIDE + (160 + k) * 8, v, tag);
    }
    if (!have) {
      for (u32 spins = 0; spins < SPIN_CAP; ++spins) {
        __builtin_amdgcn_s_sleep(1);
        FRESH();
        rml = ld128<AUX_SC1>(rws, oml, 0);
        ra = ld128<AUX_SC1>(rws, o1 + lane * 16, 0);
        if (__all(tagged(ra, tag) & ((lane != 0) | tagged(rml, tag)))) break;
      }
    }
    float* r1 = &lds[L_R1 + mj * 132];
    *(u32x2*)&r1[4 + 2 * lane] = u32x2{ra[0], ra[2]};
    if (lane == 0) *(u32x2*)&r1[0] = u32x2{rml[0], rml[2]};
  }
  __syncthreads();
  STAMP(5);
  {
    const float* r1 = &lds[L_R1 + bb * 4 * 132];
    const float m0 = r1[0], m1 = r1[132], m2 = r1[264], m3 = r1[396];
    const float M = fmaxf(fmaxf(m0, m1), fmaxf(m2, m3));
    const float e0 = __builtin_amdgcn_exp2f((m0 - M) * LOG2E), e1 = __builtin_amdgcn_exp2f((m1 - M) * LOG2E), e2 = __builtin_amdgcn_exp2f((m2 - M) * LOG2E), e3 = __builtin_amdgcn_exp2f((m3 - M) * LOG2E);
    const float L = e0 * r1[1] + e1 * r1[133] + e2 * r1[265] + e3 * r1[397];
    const int d0 = 4 + 16 * w + 4 * g;
    f32x4 o = *(const f32x4*)&r1[d0] * e0 + *(const f32x4*)&r1[132 + d0] * e1 + *(const f32x4*)&r1[264 + d0] * e2 + *(const f32x4*)&r1[396 + d0] * e3;
    o *= 1.f / L;
    const f32x4 z4 = {0.f, 0.f, 0.f, 0.f};
    f32x4 as = mfma(sel(pk2(o[0], o[0]), sl), wor[0], z4), as2 = mfma(sel(pk2(o[1], o[1]), sl), wor[1], z4);
    as = mfma(sel(pk2(o[2], o[2]), sl), wor[2], as); as2 = mfma(sel(pk2(o[3], o[3]), sl), wor[3], as2);
    *(f32x4*)&lds[L_SR + w * 256 + (g >> 1) * 128 + 8 * c + 4 * (g & 1)] = as + as2;
  }
  __syncthreads();
  if (tid < 160) {
    const int bo = tid >= 80, col = tid - 80 * bo;
    float v = 0.f;
#pragma unroll
    for (int ww = 0; ww < 8; ++ww) v += lds[L_SR + ww * 256 + bo * 128 + col];
    st_granule(rws, WS_H2 + pid * H2_STRIDE + tid * 8, v, tag);
  }
  STAMP(6);
  if (h < 5) {
    const u32 o2 = WS_H2 + ((4 * w + g) * 8 + j) * H2_STRIDE + fk0 * 8;
    u32x4 aS = {0u, 0u, 0u, 0u}, aF = aS;
    bool nS = true, nF = true;
    for (u32 spins = 0; spins < SPIN_CAP; ++spins) {
      FRESH();
      if (nS) aS = ld128<AUX_SC1>(rws, o2, 0);
      if (nF) aF = ld128<AUX_SC1>(rws, o2 + 1280, 0);
      nS = !tagged(aS, tag); nF = !tagged(aF, tag);
      if (!__any(nS || nF)) break;
      __builtin_amdgcn_s_sleep(1);
    }
    const f32x4 part = {xrow_sum(__uint_as_float(aS[0])), xrow_sum(__uint_as_float(aS[2])), xrow_sum(__uint_as_float(aF[0])), xrow_sum(__uint_as_float(aF[2]))};
    if (g == 0) *(f32x4*)&lds[L_FIN + w * 64 + 4 * c] = part;
    __syncthreads();
    if (w == 7) {
      f32x4 t = {0.f, 0.f, 0.f, 0.f};
#pragma unroll
      for (int ww = 0; ww < 8; ++ww) t += *(const f32x4*)&lds[L_FIN + ww * 64 + 4 * c];
      const float s0 = rbf(t[0]), s1 = rbf(t[1]), f0v = rbf(t[2]), f1v = rbf(t[3]);
      const float g0 = rbf(f0v / (1.f + __builtin_amdgcn_exp2f(-f0v * LOG2E))), g1 = rbf(f1v / (1.f + __builtin_amdgcn_exp2f(-f1v * LOG2E)));
      if (g == 0) *(u32*)(p.out + fbo * D + fcol) = pk2(lo16(xraw) + rbf(s0 * g0), hi16(xraw) + rbf(s1 * g1));
      if (bid == 0 && lane == 0) __builtin_amdgcn_raw_buffer_store_b32(tag, rws, WS_EPOCH, 0, AUX_SC1);
      STAMP(7);
    }
  }
}
}

extern "C" void kernel_launch(void* const* d_in, const int* in_sizes, int n_in, void* d_out, int out_size, void* d_ws, size_t ws_size, hipStream_t stream) {
  Params p;
  p.x = (const u16*)d_in[0]; p.kc = (const u16*)d_in[1]; p.vc = (const u16*)d_in[2]; p.wq = (const u16*)d_in[3]; p.wo = (const u16*)d_in[4];
  p.wg = (const u16*)d_in[6]; p.wu = (const u16*)d_in[7]; p.wd = (const u16*)d_in[8];
  p.out = (u16*)d_out; p.ws = (unsigned char*)d_ws;
  fused_decode<<<NWG, NT, 0, stream>>>(p);
}
```

```cpp
#include <hip/hip_runtime.h>
#include <stdint.h>

#define NE 264
#define STAMPS 0
#define TSCHED 7550
#define TOFF 120

namespace {
constexpr int D = 640, H = 32, SK = 16384, DH = 128, DF = 1728;
constexpr int NWG = 256, NT = 512;
constexpr int PRE = 6;
constexpr int MAXST = 34;

typedef unsigned u32;
typedef unsigned long long u64;
typedef unsigned short u16;
typedef u32 u32x2 __attribute__((ext_vector_type(2)));
typedef u32 u32x4 __attribute__((ext_vector_type(4)));
typedef float f32x2 __attribute__((ext_vector_type(2)));
typedef float f32x4 __attribute__((ext_vector_type(4)));
typedef __bf16 bf16x2 __attribute__((ext_vector_type(2)));
typedef __bf16 bf16x8 __attribute__((ext_vector_type(8)));
typedef __amdgpu_buffer_rsrc_t rsrc_t;
#define GAS __attribute__((address_space(1)))

constexpr int AUX_NT = 2, AUX_SC1 = 16;
constexpr u32 OOB = 0x80000000u;

constexpr u32 WS_EPOCH = 0;
constexpr u32 WS_QP = 4096;
constexpr u32 WS_F1 = WS_QP + NWG * 2048;
constexpr u32 WS_H1 = WS_F1 + NWG * 2048;
constexpr u32 H1_STRIDE = 144 * 8;
constexpr u32 WS_H2 = WS_H1 + NWG * H1_STRIDE;
constexpr u32 H2_STRIDE = 320 * 8;
constexpr u32 WS_STAMP = WS_H2 + NWG * H2_STRIDE;
constexpr u32 WS_BYTES = WS_STAMP + (STAMPS ? NWG * 8 * 16 * 4 : 0);

constexpr int L_Q = 0;
constexpr int L_REC = L_Q + 64;
constexpr int L_F1 = L_REC + 8 * 132;
constexpr int L_R1 = L_F1 + 8 * 256;
constexpr int L_SR = L_R1 + 8 * 132;
constexpr int L_FR = L_SR + 8 * 256;
constexpr int L_FIN = L_FR + 8 * 256;
constexpr int L_SC = L_FIN + 8 * 64;
constexpr int L_END = L_SC + 8 * MAXST * 16;

constexpr float LOG2E = 1.4426950408889634f;
constexpr float SCALE = 0.08838834764831845f;
constexpr u32 SPIN_CAP = 1u << 18;

__device__ __forceinline__ float lo16(u32 v) { return __uint_as_float(v << 16); }
__device__ __forceinline__ float hi16(u32 v) { return __uint_as_float(v & 0xffff0000u); }
__device__ __forceinline__ u32 pk2(float a, float b) { bf16x2 r = {(__bf16)a, (__bf16)b}; return __builtin_bit_cast(u32, r); }
__device__ __forceinline__ float rbf(float a) { return lo16(pk2(a, a)); }
__device__ __forceinline__ float dot2(u32 a, u32 b, float c) { return __builtin_amdgcn_fdot2_f32_bf16(__builtin_bit_cast(bf16x2, a), __builtin_bit_cast(bf16x2, b), c, false); }
template <int CTRL> __device__ __forceinline__ float dppf(float x) { return __builtin_bit_cast(float, __builtin_amdgcn_mov_dpp(__builtin_bit_cast(int, x), CTRL, 0xf, 0xf, true)); }
template <int CTRL> __device__ __forceinline__ u32 dppu(u32 x) { return (u32)__builtin_amdgcn_mov_dpp((int)x, CTRL, 0xf, 0xf, true); }
constexpr int XOR1 = 0xB1, XOR2 = 0x4E, HMIR = 0x141, ROR4 = 0x124, ROR8 = 0x128, NEWB = 0x150;
__device__ __forceinline__ float xrow_max(float x) {
  auto s = __builtin_amdgcn_permlane16_swap(__float_as_uint(x), __float_as_uint(x), false, false);
  x = fmaxf(__uint_as_float(s[0]), __uint_as_float(s[1]));
  auto t = __builtin_amdgcn_permlane32_swap(__float_as_uint(x), __float_as_uint(x), false, false);
  return fmaxf(__uint_as_float(t[0]), __uint_as_float(t[1]));
}
__device__ __forceinline__ float xrow_sum(float x) {
  auto s = __builtin_amdgcn_permlane16_swap(__float_as_uint(x), __float_as_uint(x), false, false);
  x = __uint_as_float(s[0]) + __uint_as_float(s[1]);
  auto t = __builtin_amdgcn_permlane32_swap(__float_as_uint(x), __float_as_uint(x), false, false);
  return __uint_as_float(t[0]) + __uint_as_float(t[1]);
}
__device__ __forceinline__ f32x4 mfma(u32x4 a, u32x4 b, f32x4 c) {
  return __builtin_amdgcn_mfma_f32_16x16x32_bf16(__builtin_bit_cast(bf16x8, a), __builtin_bit_cast(bf16x8, b), c, 0, 0, 0);
}
__device__ __forceinline__ rsrc_t mk(const void* p, u32 bytes) { return __builtin_amdgcn_make_buffer_rsrc((void*)p, 0, bytes, 0x00020000); }
template <int AUX> __device__ __forceinline__ u32x4 ld128(rsrc_t r, u32 voff, u32 soff) { return __builtin_amdgcn_raw_buffer_load_b128(r, voff, soff, AUX); }
template <int AUX> __device__ __forceinline__ u32x2 ld64(rsrc_t r, u32 voff, u32 soff) { return __builtin_amdgcn_raw_buffer_load_b64(r, voff, soff, AUX); }
__device__ __forceinline__ void st_granule(rsrc_t r, u32 off, float v, u32 tag) { __builtin_amdgcn_raw_buffer_store_b64(u32x2{__float_as_uint(v), tag}, r, off, 0, AUX_SC1); }
__device__ __forceinline__ void st_granule2(rsrc_t r, u32 off, float v0, float v1, u32 tag) { __builtin_amdgcn_raw_buffer_store_b128(u32x4{__float_as_uint(v0), tag, __float_as_uint(v1), tag}, r, off, 0, AUX_SC1); }
__device__ __forceinline__ bool tagged(const u32x4& a, u32 tag) { return (a[1] == tag) & (a[3] == tag); }
#define SB() __builtin_amdgcn_sched_barrier(0)
#define FRESH() asm volatile("" ::: "memory")
#define STAMP(k) do { } while (0)
#define CSTAMP(k) do { } while (0)

struct Params {
  const u16 *x, *kc, *vc, *wq, *wo, *wg, *wu, *wd;
  u16* out;
  unsigned char* ws;
};

struct Sel { u32 k0, k1, k2, k3; };
__device__ __forceinline__ u32x4 sel(u32 pp, const Sel& s) { return u32x4{pp & s.k0, pp & s.k1, pp & s.k2, pp & s.k3}; }

__global__ void __launch_bounds__(NT, 2) fused_decode(Params p) {
  __shared__ __attribute__((aligned(16))) float lds[L_END];
  const int tid = threadIdx.x, lane = tid & 63, w = __builtin_amdgcn_readfirstlane(tid >> 6);
  const int g = lane >> 4, c = lane & 15, bb = c >> 3;
  const int bid = blockIdx.x, xc = bid & 7, jj = bid >> 3;
  const int par = xc & 1, h = (xc >> 1) + 4 * (jj >> 2), b = (jj >> 1) & 1, half = jj & 1;
  const int j = par + 2 * half + 4 * b;
  const int pid = h * 8 + j;
  const int jc = 4 * par + (j >> 1);
  const bool ffn = h < DF / 64;
  asm volatile("" ::: "v140");
  STAMP(0);
  const rsrc_t rws = mk(p.ws, WS_BYTES);
  const u32 tsch0 = (u32)__builtin_amdgcn_s_memrealtime();
  auto pace = [&](int u, int n2) __attribute__((always_inline)) { const u32 due = (u32)TOFF + (u32)(u * (TSCHED - TOFF) / n2); while ((u32)__builtin_amdgcn_s_memrealtime() - tsch0 < due) __builtin_amdgcn_s_sleep(1); };
  const u32 tag = *(const __attribute__((address_space(4))) u32*)(p.ws + WS_EPOCH) + 0x9E3779B9u;

  const size_t slab = ((size_t)(b * H + h) * SK + half * (SK / 2)) * DH;
  const rsrc_t rk = mk(p.kc + slab, SK / 2 * DH * 2), rv = mk(p.vc + slab, SK / 2 * DH * 2);
  const int split = NE / 8 + (w < NE % 8 ? 1 : 0);
  const int nst = par ? 64 - split : split;
  const u32 kvbase = (par ? split : 0) * 32768u;
  u32 vo[4];
#pragma unroll
  for (int i = 0; i < 4; ++i) vo[i] = c * 16 + g * 256 + w * 1024 + i * 8192;
  u32x4 R[PRE][4];
  u32x4 wor[4], wdr[2];
  auto prefetch = [&]() __attribute__((always_inline)) {
#pragma unroll
    for (int s = 0; s < PRE; ++s) {
#pragma unroll
      for (int i = 0; i < 4; ++i) R[s][i] = ld128<AUX_NT>(rk, vo[i], kvbase + s * 32768u);
      SB();
    }
    const rsrc_t ro = mk(p.wo, H * DH * D * 2), rd = mk(p.wd, DF * D * 2);
    const u32 vw = c < 10 ? (128 * h + 16 * w + 4 * g) * (D * 2) + jc * 160 + c * 16 : OOB;
    const u32 vd = (ffn && c < 10) ? (64 * h + 8 * w + 2 * g) * (D * 2) + jc * 160 + c * 16 : OOB;
#pragma unroll
    for (int i = 0; i < 4; ++i) wor[i] = ld128<0>(ro, vw, i * (D * 2));
#pragma unroll
    for (int i = 0; i < 2; ++i) wdr[i] = ld128<0>(rd, vd, i * (D * 2));
    SB();
  };
  Sel sl;
  {
    const int mm = lane & 7;
    const u32 hm = (mm & 1) ? 0xffff0000u : 0x0000ffffu;
    sl.k0 = (mm >> 1) == 0 ? hm : 0u; sl.k1 = (mm >> 1) == 1 ? hm : 0u; sl.k2 = (mm >> 1) == 2 ? hm : 0u; sl.k3 = (mm >> 1) == 3 ? hm : 0u;
  }

  if (w == 0 || (w == 1 && ffn)) {
    u32x2 xs[5], xo[5];
    u32x4 wr[20];
    const rsrc_t rx = mk(p.x, 2 * D * 2);
#pragma unroll
    for (int i = 0; i < 5; ++i) xs[i] = ld64<0>(rx, (bb * D + 20 * (8 * g + (c & 7))) * 2 + i * 8, 0);
#pragma unroll
    for (int i = 0; i < 5; ++i) xo[i] = ld64<0>(rx, (bb * D + 80 * j + 20 * g) * 2 + i * 8, 0);
    const int ld = w == 0 ? H * DH : DF;
    const GAS u16* src = w == 0 ? (const GAS u16*)p.wq + h * DH + c * 8 : (const GAS u16*)(c < 8 ? p.wg : p.wu) + h * 64 + (c & 7) * 8;
    src += (80 * j + 20 * g) * ld;
#pragma unroll
    for (int i = 0; i < 20; ++i) wr[i] = __builtin_nontemporal_load((const GAS u32x4*)(src + i * ld));
    SB();
    __builtin_amdgcn_s_barrier();
    SB();
    float xv[20];
#pragma unroll
    for (int i = 0; i < 5; ++i) { xv[4 * i] = lo16(xs[i][0]); xv[4 * i + 1] = hi16(xs[i][0]); xv[4 * i + 2] = lo16(xs[i][1]); xv[4 * i + 3] = hi16(xs[i][1]); }
    float s = 0.f;
#pragma unroll
    for (int i = 0; i < 20; ++i) s += xv[i];
    s += dppf<XOR1>(s); s += dppf<XOR2>(s); s += dppf<HMIR>(s); s = xrow_sum(s);
    const float mean = s * (1.f / D);
    float q2 = 0.f;
#pragma unroll
    for (int i = 0; i < 20; ++i) { const float d = xv[i] - mean; q2 = fmaf(d, d, q2); }
    q2 += dppf<XOR1>(q2); q2 += dppf<XOR2>(q2); q2 += dppf<HMIR>(q2); q2 = xrow_sum(q2);
    const float mean_b = rbf(mean);
    const float rden = 1.f / rbf(__builtin_sqrtf(rbf(rbf(q2 * (1.f / D)) + 1e-5f)));
    STAMP(8);
    u32 xn[20];
#pragma unroll
    for (int i = 0; i < 5; ++i) {
      const float t0 = rbf(lo16(xo[i][0]) - mean_b) * rden, t1 = rbf(hi16(xo[i][0]) - mean_b) * rden, t2 = rbf(lo16(xo[i][1]) - mean_b) * rden, t3 = rbf(hi16(xo[i][1]) - mean_b) * rden;
      xn[4 * i] = pk2(t0, t0); xn[4 * i + 1] = pk2(t1, t1); xn[4 * i + 2] = pk2(t2, t2); xn[4 * i + 3] = pk2(t3, t3);
    }
    f32x4 a = {0.f, 0.f, 0.f, 0.f}, a2 = a;
#pragma unroll
    for (int i = 0; i < 20; i += 2) { a = mfma(sel(xn[i], sl), wr[i], a); a2 = mfma(sel(xn[i + 1], sl), wr[i + 1], a2); }
    a += a2;
    const u32 o = (w == 0 ? WS_QP : WS_F1) + pid * 2048 + lane * 16;
    st_granule2(rws, o, a[0], a[1], tag);
    st_granule2(rws, o + 1024, a[2], a[3], tag);
    STAMP(10);
    if (w == 0) {
      const u32 oq = WS_QP + h * 8 * 2048 + (lane >> 5) * 1024 + (32 * b + (lane & 31)) * 16;
      u32x4 qa[8];
      for (u32 spins = 0; spins < SPIN_CAP; ++spins) {
        FRESH();
#pragma unroll
        for (int pp = 0; pp < 8; ++pp) qa[pp] = ld128<AUX_SC1>(rws, oq + pp * 2048, 0);
        bool ok = true;
#pragma unroll
        for (int pp = 0; pp < 8; ++pp) ok &= tagged(qa[pp], tag);
        if (__all(ok)) break;
        __builtin_amdgcn_s_sleep(1);
      }
      f32x2 q = {0.f, 0.f};
#pragma unroll
      for (int pp = 0; pp < 8; ++pp) { q[0] += __uint_as_float(qa[pp][0]); q[1] += __uint_as_float(qa[pp][2]); }
      ((u32*)lds)[L_Q + 4 * (lane & 15) + 2 * ((lane >> 4) & 1) + (lane >> 5)] = pk2(q[0], q[1]);
      STAMP(9);
      __syncthreads();
      prefetch();
    } else {
      prefetch();
      __syncthreads();
    }
  } else {
    __builtin_amdgcn_s_barrier();
    SB();
    prefetch();
    __syncthreads();
  }
  STAMP(1); CSTAMP(14);
  const u32x4 qv = *(const u32x4*)&lds[L_Q + 4 * c];

  const bool c3 = (lane & 8) != 0, c2 = (lane & 4) != 0;
  float* sco = &lds[L_SC + w * (MAXST * 16) + g * 4 + (c >> 2)];
  float m = -INFINITY;
  auto score = [&](int s, int t, int NEXT, int nx) __attribute__((always_inline)) {
    float xd[4];
#pragma unroll
    for (int i = 0; i < 4; ++i) {
      float d = dot2(R[s][i][0], qv[0], 0.f);
      d = dot2(R[s][i][1], qv[1], d); d = dot2(R[s][i][2], qv[2], d); xd[i] = dot2(R[s][i][3], qv[3], d);
      if (NEXT) {
        asm volatile("" : "+v"(xd[i])); SB();
        R[s][i] = ld128<AUX_NT>(NEXT == 1 ? rk : rv, vo[i], kvbase + nx * 32768u);
        SB();
      }
    }
    const float y0 = (c3 ? xd[2] : xd[0]) + dppf<ROR8>(c3 ? xd[0] : xd[2]);
    const float y1 = (c3 ? xd[3] : xd[1]) + dppf<ROR8>(c3 ? xd[1] : xd[3]);
    float sc = (c2 ? y1 : y0) + dppf<HMIR>(c2 ? y0 : y1);
    sc += dppf<XOR1>(sc); sc += dppf<XOR2>(sc);
    sc = rbf(rbf(sc) * SCALE);
    m = fmaxf(m, sc);
    sco[t * 16] = sc;
    SB();
  };
  score(0, 0, 1, PRE);
#pragma unroll
  for (int s = 1; s < PRE; ++s) score(s, s, 0, 0);
  STAMP(13);
#pragma unroll 1
  for (int t = PRE; t < nst - 1; ++t) {
    pace(t, 2 * nst);
    score(0, t, 1, t + 1);
  }
  score(0, nst - 1, 2, 0);
  STAMP(11);
  m = fmaxf(m, dppf<ROR4>(m)); m = fmaxf(m, dppf<ROR8>(m)); m = xrow_max(m);

  float l = 0.f;
  f32x4 acc = {0.f, 0.f, 0.f, 0.f};
  auto weigh = [&](int t, bool more) __attribute__((always_inline)) {
    const float pr = __builtin_amdgcn_exp2f((sco[t * 16] - m) * LOG2E);
    l += pr;
    const u32 pp = pk2(pr, pr);
    const u32x4 a0 = sel(dppu<NEWB + 0>(pp), sl), a1 = sel(dppu<NEWB + 4>(pp), sl), a2 = sel(dppu<NEWB + 8>(pp), sl), a3 = sel(dppu<NEWB + 12>(pp), sl);
    const u32 so = kvbase + (t + 1) * 32768u;
    acc = mfma(a0, R[0][0], acc); SB(); if (more) R[0][0] = ld128<AUX_NT>(rv, vo[0], so); SB();
    acc = mfma(a1, R[0][1], acc); SB(); if (more) R[0][1] = ld128<AUX_NT>(rv, vo[1], so); SB();
    acc = mfma(a2, R[0][2], acc); SB(); if (more) R[0][2] = ld128<AUX_NT>(rv, vo[2], so); SB();
    acc = mfma(a3, R[0][3], acc); SB(); if (more) R[0][3] = ld128<AUX_NT>(rv, vo[3], so); SB();
  };
#pragma unroll 1
  for (int t = 0; t < nst - 2; ++t) {
    pace(nst + t, 2 * nst);
    weigh(t, true);
  }
  const u32 of1 = ffn ? WS_F1 + (h * 8 + w) * 2048 + lane * 16 : OOB;
  u32x4 f0 = ld128<AUX_SC1>(rws, of1, 0), f1 = ld128<AUX_SC1>(rws, of1, 1024);
  const int fk0 = 32 * (h < 5 ? h : 0) + 2 * c, fbo = fk0 >= 80, fcol = 80 * jc + fk0 - 80 * fbo;
  const u32 xraw = __builtin_amdgcn_raw_buffer_load_b32(mk(p.x, 2 * D * 2), (fbo * D + fcol) * 2, 0, 0);
  SB();
  weigh(nst - 2, true);
  weigh(nst - 1, false);
  STAMP(3); CSTAMP(15);
  if (ffn && !__all(tagged(f0, tag) & tagged(f1, tag))) {
    for (u32 spins = 0; spins < SPIN_CAP; ++spins) {
      __builtin_amdgcn_s_sleep(8);
      FRESH();
      f0 = ld128<AUX_SC1>(rws, of1, 0); f1 = ld128<AUX_SC1>(rws, of1, 1024);
      if (__all(tagged(f0, tag) & tagged(f1, tag))) break;
    }
  }

  {
    float ls = l; ls += dppf<XOR1>(ls); ls += dppf<XOR2>(ls); ls += dppf<ROR4>(ls); ls += dppf<ROR8>(ls); ls = xrow_sum(ls) * 0.25f;
    float* rec = &lds[L_REC + w * 132];
    if (lane < 32) *(f32x4*)&rec[4 + 8 * c + 4 * g] = acc;
    if (lane == 0) { rec[0] = m; rec[1] = ls; }
    *(u32x4*)&lds[L_F1 + w * 256 + (g >> 1) * 128 + 8 * c + 4 * (g & 1)] = u32x4{f0[0], f0[2], f1[0], f1[2]};
  }
  __syncthreads();
  STAMP(4);
  auto ffn2 = [&]() __attribute__((always_inline)) {
    f32x4 af = {0.f, 0.f, 0.f, 0.f};
#pragma unroll
    for (int i = 0; i < 2; ++i) {
      float gs = 0.f, us = 0.f;
#pragma unroll
      for (int pp = 0; pp < 8; ++pp) { gs += lds[L_F1 + pp * 256 + bb * 128 + 8 * w + 2 * g + i]; us += lds[L_F1 + pp * 256 + bb * 128 + 64 + 8 * w + 2 * g + i]; }
      const float gg = rbf(gs), uu = rbf(us);
      const float z = 0.7978845608028654f * (gg + 0.044715f * gg * gg * gg);
      const float th = 1.f - 2.f / (__builtin_amdgcn_exp2f(z * (2.f * LOG2E)) + 1.f);
      const float hv = rbf(0.5f * gg * (1.f + th)) * uu;
      af = mfma(sel(pk2(hv, hv), sl), wdr[i], af);
    }
    *(f32x4*)&lds[L_FR + w * 256 + (g >> 1) * 128 + 8 * c + 4 * (g & 1)] = af;
    asm volatile("s_waitcnt lgkmcnt(0)" ::: "memory");
    __builtin_amdgcn_s_barrier();
    FRESH();
  };
  if (w == 0) {
    float mw[8], M = -INFINITY;
#pragma unroll
    for (int ww = 0; ww < 8; ++ww) { mw[ww] = lds[L_REC + ww * 132]; M = fmaxf(M, mw[ww]); }
    f32x2 o = {0.f, 0.f}; float L = 0.f;
#pragma unroll
    for (int ww = 0; ww < 8; ++ww) {
      const float e = __builtin_amdgcn_exp2f((mw[ww] - M) * LOG2E);
      o += *(const f32x2*)&lds[L_REC + ww * 132 + 4 + 2 * lane] * e; L = fmaf(e, lds[L_REC + ww * 132 + 1], L);
    }
    const u32 o1 = WS_H1 + pid * H1_STRIDE;
    st_granule2(rws, o1 + lane * 16, o[0], o[1], tag);
    st_granule2(rws, lane == 0 ? o1 + 1024 : OOB, M, L, tag);
    float* r1 = &lds[L_R1 + j * 132];
    *(f32x2*)&r1[4 + 2 * lane] = o;
    if (lane == 0) *(f32x2*)&r1[0] = f32x2{M, L};
    SB();
    ffn2();
  } else {
    const int mj = j ^ w;
    const u32 o1 = WS_H1 + (h * 8 + mj) * H1_STRIDE;
    const u32 oml = lane == 0 ? o1 + 1024 : OOB;
    u32x4 ra = ld128<AUX_SC1>(rws, o1 + lane * 16, 0), rml = ld128<AUX_SC1>(rws, oml, 0);
    SB();
    ffn2();
    const bool have = __all(tagged(ra, tag) & ((lane != 0) | tagged(rml, tag)));
    SB();
    if (w < 4 && tid - 64 < 160) {
      const int k = tid - 64, bo = k >= 80, col = k - 80 * bo;
      float v = 0.f;
#pragma unroll
      for (int ww = 0; ww < 8; ++ww) v += lds[L_FR + ww * 256 + bo * 128 + col];
      st_granule(rws, WS_H2 + pid * H2_STRIDE + (160 + k) * 8, v, tag);
    }
    if (!have) {
      for (u32 spins = 0; spins < SPIN_CAP; ++spins) {
        __builtin_amdgcn_s_sleep(1);
        FRESH();
        rml = ld128<AUX_SC1>(rws, oml, 0);
        ra = ld128<AUX_SC1>(rws, o1 + lane * 16, 0);
        if (__all(tagged(ra, tag) & ((lane != 0) | tagged(rml, tag)))) break;
      }
    }
    float* r1 = &lds[L_R1 + mj * 132];
    *(u32x2*)&r1[4 + 2 * lane] = u32x2{ra[0], ra[2]};
    if (lane == 0) *(u32x2*)&r1[0] = u32x2{rml[0], rml[2]};
  }
  __syncthreads();
  STAMP(5);
  {
    const float* r1 = &lds[L_R1 + bb * 4 * 132];
    const float m0 = r1[0], m1 = r1[132], m2 = r1[264], m3 = r1[396];
    const float M = fmaxf(fmaxf(m0, m1), fmaxf(m2, m3));
    const float e0 = __builtin_amdgcn_exp2f((m0 - M) * LOG2E), e1 = __builtin_amdgcn_exp2f((m1 - M) * LOG2E), e2 = __builtin_amdgcn_exp2f((m2 - M) * LOG2E), e3 = __builtin_amdgcn_exp2f((m3 - M) * LOG2E);
    const float L = e0 * r1[1] + e1 * r1[133] + e2 * r1[265] + e3 * r1[397];
    const int d0 = 4 + 16 * w + 4 * g;
    f32x4 o = *(const f32x4*)&r1[d0] * e0 + *(const f32x4*)&r1[132 + d0] * e1 + *(const f32x4*)&r1[264 + d0] * e2 + *(const f32x4*)&r1[396 + d0] * e3;
    o *= 1.f / L;
    const f32x4 z4 = {0.f, 0.f, 0.f, 0.f};
    f32x4 as = mfma(sel(pk2(o[0], o[0]), sl), wor[0], z4), as2 = mfma(sel(pk2(o[1], o[1]), sl), wor[1], z4);
    as = mfma(sel(pk2(o[2], o[2]), sl), wor[2], as); as2 = mfma(sel(pk2(o[3], o[3]), sl), wor[3], as2);
    *(f32x4*)&lds[L_SR + w * 256 + (g >> 1) * 128 + 8 * c + 4 * (g & 1)] = as + as2;
  }
  __syncthreads();
  if (tid < 160) {
    const int bo = tid >= 80, col = tid - 80 * bo;
    float v = 0.f;
#pragma unroll
    for (int ww = 0; ww < 8; ++ww) v += lds[L_SR + ww * 256 + bo * 128 + col];
    st_granule(rws, WS_H2 + pid * H2_STRIDE + tid * 8, v, tag);
  }
  STAMP(6);
  if (h < 5) {
    const u32 o2 = WS_H2 + ((4 * w + g) * 8 + j) * H2_STRIDE + fk0 * 8;
    u32x4 aS = {0u, 0u, 0u, 0u}, aF = aS;
    bool nS = true, nF = true;
    for (u32 spins = 0; spins < SPIN_CAP; ++spins) {
      FRESH();
      if (nS) aS = ld128<AUX_SC1>(rws, o2, 0);
      if (nF) aF = ld128<AUX_SC1>(rws, o2 + 1280, 0);
      nS = !tagged(aS, tag); nF = !tagged(aF, tag);
      if (!__any(nS || nF)) break;
      __builtin_amdgcn_s_sleep(1);
    }
    const f32x4 part = {xrow_sum(__uint_as_float(aS[0])), xrow_sum(__uint_as_float(aS[2])), xrow_sum(__uint_as_float(aF[0])), xrow_sum(__uint_as_float(aF[2]))};
    if (g == 0) *(f32x4*)&lds[L_FIN + w * 64 + 4 * c] = part;
    __syncthreads();
    if (w == 7) {
      f32x4 t = {0.f, 0.f, 0.f, 0.f};
#pragma unroll
      for (int ww = 0; ww < 8; ++ww) t += *(const f32x4*)&lds[L_FIN + ww * 64 + 4 * c];
      const float s0 = rbf(t[0]), s1 = rbf(t[1]), f0v = rbf(t[2]), f1v = rbf(t[3]);
      const float g0 = rbf(f0v / (1.f + __builtin_amdgcn_exp2f(-f0v * LOG2E))), g1 = rbf(f1v / (1.f + __builtin_amdgcn_exp2f(-f1v * LOG2E)));
      if (g == 0) *(u32*)(p.out + fbo * D + fcol) = pk2(lo16(xraw) + rbf(s0 * g0), hi16(xraw) + rbf(s1 * g1));
      if (bid == 0 && lane == 0) __builtin_amdgcn_raw_buffer_store_b32(tag, rws, WS_EPOCH, 0, AUX_SC1);
      STAMP(7);
    }
  }
}
}

extern "C" void kernel_launch(void* const* d_in, const int* in_sizes, int n_in, void* d_out, int out_size, void* d_ws, size_t ws_size, hipStream_t stream) {
  Params p;
  p.x = (const u16*)d_in[0]; p.kc = (const u16*)d_in[1]; p.vc = (const u16*)d_in[2]; p.wq = (const u16*)d_in[3]; p.wo = (const u16*)d_in[4];
  p.wg = (const u16*)d_in[6]; p.wu = (const u16*)d_in[7]; p.wd = (const u16*)d_in[8];
  p.out = (u16*)d_out; p.ws = (unsigned char*)d_ws;
  fused_decode<<<NWG, NT, 0, stream>>>(p);
}
```

```cpp
#include <hip/hip_runtime.h>
#include <stdint.h>

#define NE 264
#define STAMPS 0

namespace {
constexpr int D = 640, H = 32, SK = 16384, DH = 128, DF = 1728;
constexpr int NWG = 256, NT = 512;
constexpr int PRE = 6;
constexpr int MAXST = 34;
constexpr unsigned PACE_T = 7550, PACE_0 = 400;

typedef unsigned u32;
typedef unsigned long long u64;
typedef unsigned short u16;
typedef u32 u32x2 __attribute__((ext_vector_type(2)));
typedef u32 u32x4 __attribute__((ext_vector_type(4)));
typedef float f32x2 __attribute__((ext_vector_type(2)));
typedef float f32x4 __attribute__((ext_vector_type(4)));
typedef __bf16 bf16x2 __attribute__((ext_vector_type(2)));
typedef __bf16 bf16x8 __attribute__((ext_vector_type(8)));
typedef __amdgpu_buffer_rsrc_t rsrc_t;
#define GAS __attribute__((address_space(1)))

constexpr int AUX_NT = 2, AUX_SC1 = 16;
constexpr u32 OOB = 0x80000000u;

constexpr u32 WS_EPOCH = 0;
constexpr u32 WS_QP = 4096;
constexpr u32 WS_F1 = WS_QP + NWG * 2048;
constexpr u32 WS_H1 = WS_F1 + NWG * 2048;
constexpr u32 H1_STRIDE = 144 * 8;
constexpr u32 WS_H2 = WS_H1 + NWG * H1_STRIDE;
constexpr u32 H2_STRIDE = 320 * 8;
constexpr u32 WS_STAMP = WS_H2 + NWG * H2_STRIDE;
constexpr u32 WS_BYTES = WS_STAMP + (STAMPS ? NWG * 8 * 16 * 4 : 0);

constexpr int L_Q = 0;
constexpr int L_REC = L_Q + 64;
constexpr int L_F1 = L_REC + 8 * 132;
constexpr int L_R1 = L_F1 + 8 * 256;
constexpr int L_SR = L_R1 + 8 * 132;
constexpr int L_FR = L_SR + 8 * 256;
constexpr int L_FIN = L_FR + 8 * 256;
constexpr int L_SC = L_FIN + 8 * 64;
constexpr int L_END = L_SC + 8 * MAXST * 16;

constexpr float LOG2E = 1.4426950408889634f;
constexpr float SCALE = 0.08838834764831845f;
constexpr u32 SPIN_CAP = 1u << 18;

__device__ __forceinline__ float lo16(u32 v) { return __uint_as_float(v << 16); }
__device__ __forceinline__ float hi16(u32 v) { return __uint_as_float(v & 0xffff0000u); }
__device__ __forceinline__ u32 pk2(float a, float b) { bf16x2 r = {(__bf16)a, (__bf16)b}; return __builtin_bit_cast(u32, r); }
__device__ __forceinline__ float rbf(float a) { return lo16(pk2(a, a)); }
__device__ __forceinline__ float dot2(u32 a, u32 b, float c) { return __builtin_amdgcn_fdot2_f32_bf16(__builtin_bit_cast(bf16x2, a), __builtin_bit_cast(bf16x2, b), c, false); }
template <int CTRL> __device__ __forceinline__ float dppf(float x) { return __builtin_bit_cast(float, __builtin_amdgcn_mov_dpp(__builtin_bit_cast(int, x), CTRL, 0xf, 0xf, true)); }
template <int CTRL> __device__ __forceinline__ u32 dppu(u32 x) { return (u32)__builtin_amdgcn_mov_dpp((int)x, CTRL, 0xf, 0xf, true); }
constexpr int XOR1 = 0xB1, XOR2 = 0x4E, HMIR = 0x141, ROR4 = 0x124, ROR8 = 0x128, NEWB = 0x150;
__device__ __forceinline__ float xrow_max(float x) {
  auto s = __builtin_amdgcn_permlane16_swap(__float_as_uint(x), __float_as_uint(x), false, false);
  x = fmaxf(__uint_as_float(s[0]), __uint_as_float(s[1]));
  auto t = __builtin_amdgcn_permlane32_swap(__float_as_uint(x), __float_as_uint(x), false, false);
  return fmaxf(__uint_as_float(t[0]), __uint_as_float(t[1]));
}
__device__ __forceinline__ float xrow_sum(float x) {
  auto s = __builtin_amdgcn_permlane16_swap(__float_as_uint(x), __float_as_uint(x), false, false);
  x = __uint_as_float(s[0]) + __uint_as_float(s[1]);
  auto t = __builtin_amdgcn_permlane32_swap(__float_as_uint(x), __float_as_uint(x), false, false);
  return __uint_as_float(t[0]) + __uint_as_float(t[1]);
}
__device__ __forceinline__ f32x4 mfma(u32x4 a, u32x4 b, f32x4 c) {
  return __builtin_amdgcn_mfma_f32_16x16x32_bf16(__builtin_bit_cast(bf16x8, a), __builtin_bit_cast(bf16x8, b), c, 0, 0, 0);
}
__device__ __forceinline__ rsrc_t mk(const void* p, u32 bytes) { return __builtin_amdgcn_make_buffer_rsrc((void*)p, 0, bytes, 0x00020000); }
template <int AUX> __device__ __forceinline__ u32x4 ld128(rsrc_t r, u32 voff, u32 soff) { return __builtin_amdgcn_raw_buffer_load_b128(r, voff, soff, AUX); }
template <int AUX> __device__ __forceinline__ u32x2 ld64(rsrc_t r, u32 voff, u32 soff) { return __builtin_amdgcn_raw_buffer_load_b64(r, voff, soff, AUX); }
__device__ __forceinline__ void st_granule(rsrc_t r, u32 off, float v, u32 tag) { __builtin_amdgcn_raw_buffer_store_b64(u32x2{__float_as_uint(v), tag}, r, off, 0, AUX_SC1); }
__device__ __forceinline__ void st_granule2(rsrc_t r, u32 off, float v0, float v1, u32 tag) { __builtin_amdgcn_raw_buffer_store_b128(u32x4{__float_as_uint(v0), tag, __float_as_uint(v1), tag}, r, off, 0, AUX_SC1); }
__device__ __forceinline__ bool tagged(const u32x4& a, u32 tag) { return (a[1] == tag) & (a[3] == tag); }
#define SB() __builtin_amdgcn_sched_barrier(0)
#define FRESH() asm volatile("" ::: "memory")
#define STAMP(k) do { } while (0)
#define CSTAMP(k) do { } while (0)

struct Params {
  const u16 *x, *kc, *vc, *wq, *wo, *wg, *wu, *wd;
  u16* out;
  unsigned char* ws;
};

struct Sel { u32 k0, k1, k2, k3; };
__device__ __forceinline__ u32x4 sel(u32 pp, const Sel& s) { return u32x4{pp & s.k0, pp & s.k1, pp & s.k2, pp & s.k3}; }

__global__ void __launch_bounds__(NT, 2) fused_decode(Params p) {
  __shared__ __attribute__((aligned(16))) float lds[L_END];
  const int tid = threadIdx.x, lane = tid & 63, w = __builtin_amdgcn_readfirstlane(tid >> 6);
  const int g = lane >> 4, c = lane & 15, bb = c >> 3;
  const int bid = blockIdx.x, xc = bid & 7, jj = bid >> 3;
  const int par = xc & 1, h = (xc >> 1) + 4 * (jj >> 2), b = (jj >> 1) & 1, half = jj & 1;
  const int j = par + 2 * half + 4 * b;
  const int pid = h * 8 + j;
  const int jc = 4 * par + (j >> 1);
  const bool ffn = h < DF / 64;
  asm volatile("" ::: "v140");
  STAMP(0);
  const u32 t_start = (u32)__builtin_amdgcn_s_memrealtime();
  const rsrc_t rws = mk(p.ws, WS_BYTES);
  const u32 tag = *(const __attribute__((address_space(4))) u32*)(p.ws + WS_EPOCH) + 0x9E3779B9u;

  const size_t slab = ((size_t)(b * H + h) * SK + half * (SK / 2)) * DH;
  const rsrc_t rk = mk(p.kc + slab, SK / 2 * DH * 2), rv = mk(p.vc + slab, SK / 2 * DH * 2);
  const int split = NE / 8 + (w < NE % 8 ? 1 : 0);
  const int nst = par ? 64 - split : split;
  const u32 kvbase = (par ? split : 0) * 32768u;
  const u32 slope = ((PACE_T - PACE_0) << 8) / (2 * nst);
  auto pace = [&](int u) __attribute__((always_inline)) {
    const u32 due = PACE_0 + ((u32)u * slope >> 8);
    while ((u32)__builtin_amdgcn_s_memrealtime() - t_start < due) __builtin_amdgcn_s_sleep(1);
  };
  u32 vo[4];
#pragma unroll
  for (int i = 0; i < 4; ++i) vo[i] = c * 16 + g * 256 + w * 1024 + i * 8192;
  u32x4 R[PRE][4];
  u32x4 wor[4], wdr[2];
  auto prefetch = [&]() __attribute__((always_inline)) {
#pragma unroll
    for (int s = 0; s < PRE; ++s) {
#pragma unroll
      for (int i = 0; i < 4; ++i) R[s][i] = ld128<AUX_NT>(rk, vo[i], kvbase + s * 32768u);
      SB();
    }
    const rsrc_t ro = mk(p.wo, H * DH * D * 2), rd = mk(p.wd, DF * D * 2);
    const u32 vw = c < 10 ? (128 * h + 16 * w + 4 * g) * (D * 2) + jc * 160 + c * 16 : OOB;
    const u32 vd = (ffn && c < 10) ? (64 * h + 8 * w + 2 * g) * (D * 2) + jc * 160 + c * 16 : OOB;
#pragma unroll
    for (int i = 0; i < 4; ++i) wor[i] = ld128<0>(ro, vw, i * (D * 2));
#pragma unroll
    for (int i = 0; i < 2; ++i) wdr[i] = ld128<0>(rd, vd, i * (D * 2));
    SB();
  };
  Sel sl;
  {
    const int mm = lane & 7;
    const u32 hm = (mm & 1) ? 0xffff0000u : 0x0000ffffu;
    sl.k0 = (mm >> 1) == 0 ? hm : 0u; sl.k1 = (mm >> 1) == 1 ? hm : 0u; sl.k2 = (mm >> 1) == 2 ? hm : 0u; sl.k3 = (mm >> 1) == 3 ? hm : 0u;
  }

  if (w == 0 || (w == 1 && ffn)) {
    u32x2 xs[5], xo[5];
    u32x4 wr[20];
    const rsrc_t rx = mk(p.x, 2 * D * 2);
#pragma unroll
    for (int i = 0; i < 5; ++i) xs[i] = ld64<0>(rx, (bb * D + 20 * (8 * g + (c & 7))) * 2 + i * 8, 0);
#pragma unroll
    for (int i = 0; i < 5; ++i) xo[i] = ld64<0>(rx, (bb * D + 80 * j + 20 * g) * 2 + i * 8, 0);
    const int ld = w == 0 ? H * DH : DF;
    const GAS u16* src = w == 0 ? (const GAS u16*)p.wq + h * DH + c * 8 : (const GAS u16*)(c < 8 ? p.wg : p.wu) + h * 64 + (c & 7) * 8;
    src += (80 * j + 20 * g) * ld;
#pragma unroll
    for (int i = 0; i < 20; ++i) wr[i] = __builtin_nontemporal_load((const GAS u32x4*)(src + i * ld));
    SB();
    __builtin_amdgcn_s_barrier();
    SB();
    float xv[20];
#pragma unroll
    for (int i = 0; i < 5; ++i) { xv[4 * i] = lo16(xs[i][0]); xv[4 * i + 1] = hi16(xs[i][0]); xv[4 * i + 2] = lo16(xs[i][1]); xv[4 * i + 3] = hi16(xs[i][1]); }
    float s = 0.f;
#pragma unroll
    for (int i = 0; i < 20; ++i) s += xv[i];
    s += dppf<XOR1>(s); s += dppf<XOR2>(s); s += dppf<HMIR>(s); s = xrow_sum(s);
    const float mean = s * (1.f / D);
    float q2 = 0.f;
#pragma unroll
    for (int i = 0; i < 20; ++i) { const float d = xv[i] - mean; q2 = fmaf(d, d, q2); }
    q2 += dppf<XOR1>(q2); q2 += dppf<XOR2>(q2); q2 += dppf<HMIR>(q2); q2 = xrow_sum(q2);
    const float mean_b = rbf(mean);
    const float rden = 1.f / rbf(__builtin_sqrtf(rbf(rbf(q2 * (1.f / D)) + 1e-5f)));
    STAMP(8);
    u32 xn[20];
#pragma unroll
    for (int i = 0; i < 5; ++i) {
      const float t0 = rbf(lo16(xo[i][0]) - mean_b) * rden, t1 = rbf(hi16(xo[i][0]) - mean_b) * rden, t2 = rbf(lo16(xo[i][1]) - mean_b) * rden, t3 = rbf(hi16(xo[i][1]) - mean_b) * rden;
      xn[4 * i] = pk2(t0, t0); xn[4 * i + 1] = pk2(t1, t1); xn[4 * i + 2] = pk2(t2, t2); xn[4 * i + 3] = pk2(t3, t3);
    }
    f32x4 a = {0.f, 0.f, 0.f, 0.f}, a2 = a;
#pragma unroll
    for (int i = 0; i < 20; i += 2) { a = mfma(sel(xn[i], sl), wr[i], a); a2 = mfma(sel(xn[i + 1], sl), wr[i + 1], a2); }
    a += a2;
    const u32 o = (w == 0 ? WS_QP : WS_F1) + pid * 2048 + lane * 16;
    st_granule2(rws, o, a[0], a[1], tag);
    st_granule2(rws, o + 1024, a[2], a[3], tag);
    STAMP(10);
    if (w == 0) {
      const u32 oq = WS_QP + h * 8 * 2048 + (lane >> 5) * 1024 + (32 * b + (lane & 31)) * 16;
      u32x4 qa[8];
      for (u32 spins = 0; spins < SPIN_CAP; ++spins) {
        FRESH();
#pragma unroll
        for (int pp = 0; pp < 8; ++pp) qa[pp] = ld128<AUX_SC1>(rws, oq + pp * 2048, 0);
        bool ok = true;
#pragma unroll
        for (int pp = 0; pp < 8; ++pp) ok &= tagged(qa[pp], tag);
        if (__all(ok)) break;
        __builtin_amdgcn_s_sleep(1);
      }
      f32x2 q = {0.f, 0.f};
#pragma unroll
      for (int pp = 0; pp < 8; ++pp) { q[0] += __uint_as_float(qa[pp][0]); q[1] += __uint_as_float(qa[pp][2]); }
      ((u32*)lds)[L_Q + 4 * (lane & 15) + 2 * ((lane >> 4) & 1) + (lane >> 5)] = pk2(q[0], q[1]);
      STAMP(9);
      __syncthreads();
      prefetch();
    } else {
      prefetch();
      __syncthreads();
    }
  } else {
    __builtin_amdgcn_s_barrier();
    SB();
    prefetch();
    __syncthreads();
  }
  STAMP(1); CSTAMP(14);
  const u32x4 qv = *(const u32x4*)&lds[L_Q + 4 * c];

  const bool c3 = (lane & 8) != 0, c2 = (lane & 4) != 0;
  float* sco = &lds[L_SC + w * (MAXST * 16) + g * 4 + (c >> 2)];
  float m = -INFINITY;
  auto score = [&](int s, int t, int NEXT, int nx) __attribute__((always_inline)) {
    float xd[4];
#pragma unroll
    for (int i = 0; i < 4; ++i) {
      float d = dot2(R[s][i][0], qv[0], 0.f);
      d = dot2(R[s][i][1], qv[1], d); d = dot2(R[s][i][2], qv[2], d); xd[i] = dot2(R[s][i][3], qv[3], d);
      if (NEXT) {
        asm volatile("" : "+v"(xd[i])); SB();
        R[s][i] = ld128<AUX_NT>(NEXT == 1 ? rk : rv, vo[i], kvbase + nx * 32768u);
        SB();
      }
    }
    const float y0 = (c3 ? xd[2] : xd[0]) + dppf<ROR8>(c3 ? xd[0] : xd[2]);
    const float y1 = (c3 ? xd[3] : xd[1]) + dppf<ROR8>(c3 ? xd[1] : xd[3]);
    float sc = (c2 ? y1 : y0) + dppf<HMIR>(c2 ? y0 : y1);
    sc += dppf<XOR1>(sc); sc += dppf<XOR2>(sc);
    sc = rbf(rbf(sc) * SCALE);
    m = fmaxf(m, sc);
    sco[t * 16] = sc;
    SB();
  };
  score(0, 0, 1, PRE);
#pragma unroll
  for (int s = 1; s < PRE; ++s) score(s, s, 0, 0);
  STAMP(13);
#pragma unroll 1
  for (int t = PRE; t < nst - 1; ++t) {
    pace(t);
    score(0, t, 1, t + 1);
  }
  score(0, nst - 1, 2, 0);
  STAMP(11);
  m = fmaxf(m, dppf<ROR4>(m)); m = fmaxf(m, dppf<ROR8>(m)); m = xrow_max(m);

  float l = 0.f;
  f32x4 acc = {0.f, 0.f, 0.f, 0.f};
  auto weigh = [&](int t, bool more) __attribute__((always_inline)) {
    const float pr = __builtin_amdgcn_exp2f((sco[t * 16] - m) * LOG2E);
    l += pr;
    const u32 pp = pk2(pr, pr);
    const u32x4 a0 = sel(dppu<NEWB + 0>(pp), sl), a1 = sel(dppu<NEWB + 4>(pp), sl), a2 = sel(dppu<NEWB + 8>(pp), sl), a3 = sel(dppu<NEWB + 12>(pp), sl);
    const u32 so = kvbase + (t + 1) * 32768u;
    acc = mfma(a0, R[0][0], acc); SB(); if (more) R[0][0] = ld128<AUX_NT>(rv, vo[0], so); SB();
    acc = mfma(a1, R[0][1], acc); SB(); if (more) R[0][1] = ld128<AUX_NT>(rv, vo[1], so); SB();
    acc = mfma(a2, R[0][2], acc); SB(); if (more) R[0][2] = ld128<AUX_NT>(rv, vo[2], so); SB();
    acc = mfma(a3, R[0][3], acc); SB(); if (more) R[0][3] = ld128<AUX_NT>(rv, vo[3], so); SB();
  };
#pragma unroll 1
  for (int t = 0; t < nst - 2; ++t) {
    pace(nst + t);
    weigh(t, true);
  }
  const u32 of1 = ffn ? WS_F1 + (h * 8 + w) * 2048 + lane * 16 : OOB;
  u32x4 f0 = ld128<AUX_SC1>(rws, of1, 0), f1 = ld128<AUX_SC1>(rws, of1, 1024);
  const int fk0 = 32 * (h < 5 ? h : 0) + 2 * c, fbo = fk0 >= 80, fcol = 80 * jc + fk0 - 80 * fbo;
  const u32 xraw = __builtin_amdgcn_raw_buffer_load_b32(mk(p.x, 2 * D * 2), (fbo * D + fcol) * 2, 0, 0);
  SB();
  weigh(nst - 2, true);
  weigh(nst - 1, false);
  STAMP(3); CSTAMP(15);
  if (ffn && !__all(tagged(f0, tag) & tagged(f1, tag))) {
    for (u32 spins = 0; spins < SPIN_CAP; ++spins) {
      __builtin_amdgcn_s_sleep(8);
      FRESH();
      f0 = ld128<AUX_SC1>(rws, of1, 0); f1 = ld128<AUX_SC1>(rws, of1, 1024);
      if (__all(tagged(f0, tag) & tagged(f1, tag))) break;
    }
  }

  {
    float ls = l; ls += dppf<XOR1>(ls); ls += dppf<XOR2>(ls); ls += dppf<ROR4>(ls); ls += dppf<ROR8>(ls); ls = xrow_sum(ls) * 0.25f;
    float* rec = &lds[L_REC + w * 132];
    if (lane < 32) *(f32x4*)&rec[4 + 8 * c + 4 * g] = acc;
    if (lane == 0) { rec[0] = m; rec[1] = ls; }
    *(u32x4*)&lds[L_F1 + w * 256 + (g >> 1) * 128 + 8 * c + 4 * (g & 1)] = u32x4{f0[0], f0[2], f1[0], f1[2]};
  }
  __syncthreads();
  STAMP(4);
  auto ffn2 = [&]() __attribute__((always_inline)) {
    f32x4 af = {0.f, 0.f, 0.f, 0.f};
#pragma unroll
    for (int i = 0; i < 2; ++i) {
      float gs = 0.f, us = 0.f;
#pragma unroll
      for (int pp = 0; pp < 8; ++pp) { gs += lds[L_F1 + pp * 256 + bb * 128 + 8 * w + 2 * g + i]; us += lds[L_F1 + pp * 256 + bb * 128 + 64 + 8 * w + 2 * g + i]; }
      const float gg = rbf(gs), uu = rbf(us);
      const float z = 0.7978845608028654f * (gg + 0.044715f * gg * gg * gg);
      const float th = 1.f - 2.f / (__builtin_amdgcn_exp2f(z * (2.f * LOG2E)) + 1.f);
      const float hv = rbf(0.5f * gg * (1.f + th)) * uu;
      af = mfma(sel(pk2(hv, hv), sl), wdr[i], af);
    }
    *(f32x4*)&lds[L_FR + w * 256 + (g >> 1) * 128 + 8 * c + 4 * (g & 1)] = af;
    asm volatile("s_waitcnt lgkmcnt(0)" ::: "memory");
    __builtin_amdgcn_s_barrier();
    FRESH();
  };
  if (w == 0) {
    float mw[8], M = -INFINITY;
#pragma unroll
    for (int ww = 0; ww < 8; ++ww) { mw[ww] = lds[L_REC + ww * 132]; M = fmaxf(M, mw[ww]); }
    f32x2 o = {0.f, 0.f}; float L = 0.f;
#pragma unroll
    for (int ww = 0; ww < 8; ++ww) {
      const float e = __builtin_amdgcn_exp2f((mw[ww] - M) * LOG2E);
      o += *(const f32x2*)&lds[L_REC + ww * 132 + 4 + 2 * lane] * e; L = fmaf(e, lds[L_REC + ww * 132 + 1], L);
    }
    const u32 o1 = WS_H1 + pid * H1_STRIDE;
    st_granule2(rws, o1 + lane * 16, o[0], o[1], tag);
    st_granule2(rws, lane == 0 ? o1 + 1024 : OOB, M, L, tag);
    float* r1 = &lds[L_R1 + j * 132];
    *(f32x2*)&r1[4 + 2 * lane] = o;
    if (lane == 0) *(f32x2*)&r1[0] = f32x2{M, L};
    SB();
    ffn2();
  } else {
    const int mj = j ^ w;
    const u32 o1 = WS_H1 + (h * 8 + mj) * H1_STRIDE;
    const u32 oml = lane == 0 ? o1 + 1024 : OOB;
    u32x4 ra = ld128<AUX_SC1>(rws, o1 + lane * 16, 0), rml = ld128<AUX_SC1>(rws, oml, 0);
    SB();
    ffn2();
    const bool have = __all(tagged(ra, tag) & ((lane != 0) | tagged(rml, tag)));
    SB();
    if (w < 4 && tid - 64 < 160) {
      const int k = tid - 64, bo = k >= 80, col = k - 80 * bo;
      float v = 0.f;
#pragma unroll
      for (int ww = 0; ww < 8; ++ww) v += lds[L_FR + ww * 256 + bo * 128 + col];
      st_granule(rws, WS_H2 + pid * H2_STRIDE + (160 + k) * 8, v, tag);
    }
    if (!have) {
      for (u32 spins = 0; spins < SPIN_CAP; ++spins) {
        __builtin_amdgcn_s_sleep(1);
        FRESH();
        rml = ld128<AUX_SC1>(rws, oml, 0);
        ra = ld128<AUX_SC1>(rws, o1 + lane * 16, 0);
        if (__all(tagged(ra, tag) & ((lane != 0) | tagged(rml, tag)))) break;
      }
    }
    float* r1 = &lds[L_R1 + mj * 132];
    *(u32x2*)&r1[4 + 2 * lane] = u32x2{ra[0], ra[2]};
    if (lane == 0) *(u32x2*)&r1[0] = u32x2{rml[0], rml[2]};
  }
  __syncthreads();
  STAMP(5);
  {
    const float* r1 = &lds[L_R1 + bb * 4 * 132];
    const float m0 = r1[0], m1 = r1[132], m2 = r1[264], m3 = r1[396];
    const float M = fmaxf(fmaxf(m0, m1), fmaxf(m2, m3));
    const float e0 = __builtin_amdgcn_exp2f((m0 - M) * LOG2E), e1 = __builtin_amdgcn_exp2f((m1 - M) * LOG2E), e2 = __builtin_amdgcn_exp2f((m2 - M) * LOG2E), e3 = __builtin_amdgcn_exp2f((m3 - M) * LOG2E);
    const float L = e0 * r1[1] + e1 * r1[133] + e2 * r1[265] + e3 * r1[397];
    const int d0 = 4 + 16 * w + 4 * g;
    f32x4 o = *(const f32x4*)&r1[d0] * e0 + *(const f32x4*)&r1[132 + d0] * e1 + *(const f32x4*)&r1[264 + d0] * e2 + *(const f32x4*)&r1[396 + d0] * e3;
    o *= 1.f / L;
    const f32x4 z4 = {0.f, 0.f, 0.f, 0.f};
    f32x4 as = mfma(sel(pk2(o[0], o[0]), sl), wor[0], z4), as2 = mfma(sel(pk2(o[1], o[1]), sl), wor[1], z4);
    as = mfma(sel(pk2(o[2], o[2]), sl), wor[2], as); as2 = mfma(sel(pk2(o[3], o[3]), sl), wor[3], as2);
    *(f32x4*)&lds[L_SR + w * 256 + (g >> 1) * 128 + 8 * c + 4 * (g & 1)] = as + as2;
  }
  __syncthreads();
  if (tid < 160) {
    const int bo = tid >= 80, col = tid - 80 * bo;
    float v = 0.f;
#pragma unroll
    for (int ww = 0; ww < 8; ++ww) v += lds[L_SR + ww * 256 + bo * 128 + col];
    st_granule(rws, WS_H2 + pid * H2_STRIDE + tid * 8, v, tag);
  }
  STAMP(6);
  if (h < 5) {
    const u32 o2 = WS_H2 + ((4 * w + g) * 8 + j) * H2_STRIDE + fk0 * 8;
    u32x4 aS = {0u, 0u, 0u, 0u}, aF = aS;
    bool nS = true, nF = true;
    for (u32 spins = 0; spins < SPIN_CAP; ++spins) {
      FRESH();
      if (nS) aS = ld128<AUX_SC1>(rws, o2, 0);
      if (nF) aF = ld128<AUX_SC1>(rws, o2 + 1280, 0);
      nS = !tagged(aS, tag); nF = !tagged(aF, tag);
      if (!__any(nS || nF)) break;
      __builtin_amdgcn_s_sleep(1);
    }
    const f32x4 part = {xrow_sum(__uint_as_float(aS[0])), xrow_sum(__uint_as_float(aS[2])), xrow_sum(__uint_as_float(aF[0])), xrow_sum(__uint_as_float(aF[2]))};
    if (g == 0) *(f32x4*)&lds[L_FIN + w * 64 + 4 * c] = part;
    __syncthreads();
    if (w == 7) {
      f32x4 t = {0.f, 0.f, 0.f, 0.f};
#pragma unroll
      for (int ww = 0; ww < 8; ++ww) t += *(const f32x4*)&lds[L_FIN + ww * 64 + 4 * c];
      const float s0 = rbf(t[0]), s1 = rbf(t[1]), f0v = rbf(t[2]), f1v = rbf(t[3]);
      const float g0 = rbf(f0v / (1.f + __builtin_amdgcn_exp2f(-f0v * LOG2E))), g1 = rbf(f1v / (1.f + __builtin_amdgcn_exp2f(-f1v * LOG2E)));
      if (g == 0) *(u32*)(p.out + fbo * D + fcol) = pk2(lo16(xraw) + rbf(s0 * g0), hi16(xraw) + rbf(s1 * g1));
      if (bid == 0 && lane == 0) __builtin_amdgcn_raw_buffer_store_b32(tag, rws, WS_EPOCH, 0, AUX_SC1);
      STAMP(7);
    }
  }
}
}

extern "C" void kernel_launch(void* const* d_in, const int* in_sizes, int n_in, void* d_out, int out_size, void* d_ws, size_t ws_size, hipStream_t stream) {
  Params p;
  p.x = (const u16*)d_in[0]; p.kc = (const u16*)d_in[1]; p.vc = (const u16*)d_in[2]; p.wq = (const u16*)d_in[3]; p.wo = (const u16*)d_in[4];
  p.wg = (const u16*)d_in[6]; p.wu = (const u16*)d_in[7]; p.wd = (const u16*)d_in[8];
  p.out = (u16*)d_out; p.ws = (unsigned char*)d_ws;
  fused_decode<<<NWG, NT, 0, stream>>>(p);
}
```

```cpp
#include <hip/hip_runtime.h>
#include <stdint.h>

namespace {
constexpr int D = 640, H = 32, SK = 16384, DH = 128, DF = 1728;
constexpr int NWG = 256, NT = 512;
constexpr int NE = 264;
constexpr int PRE = 6;
constexpr int MAXST = 34;
constexpr unsigned PACE_T = 7550, PACE_0 = 450;

typedef unsigned u32;
typedef unsigned long long u64;
typedef unsigned short u16;
typedef u32 u32x2 __attribute__((ext_vector_type(2)));
typedef u32 u32x4 __attribute__((ext_vector_type(4)));
typedef float f32x2 __attribute__((ext_vector_type(2)));
typedef float f32x4 __attribute__((ext_vector_type(4)));
typedef __bf16 bf16x2 __attribute__((ext_vector_type(2)));
typedef __bf16 bf16x8 __attribute__((ext_vector_type(8)));
typedef __amdgpu_buffer_rsrc_t rsrc_t;
#define GAS __attribute__((address_space(1)))

constexpr int AUX_NT = 2, AUX_SC1 = 16;
constexpr u32 OOB = 0x80000000u;

constexpr u32 WS_EPOCH = 0;
constexpr u32 WS_QP = 4096;
constexpr u32 WS_F1 = WS_QP + NWG * 2048;
constexpr u32 WS_H1 = WS_F1 + NWG * 2048;
constexpr u32 H1_STRIDE = 144 * 8;
constexpr u32 WS_H2 = WS_H1 + NWG * H1_STRIDE;
constexpr u32 H2_STRIDE = 320 * 8;
constexpr u32 WS_BYTES = WS_H2 + NWG * H2_STRIDE;

constexpr int L_Q = 0;
constexpr int L_REC = L_Q + 64;
constexpr int L_F1 = L_REC + 8 * 132;
constexpr int L_R1 = L_F1 + 8 * 256;
constexpr int L_SR = L_R1 + 8 * 132;
constexpr int L_FR = L_SR + 8 * 256;
constexpr int L_FIN = L_FR + 8 * 256;
constexpr int L_SC = L_FIN + 8 * 64;
constexpr int L_END = L_SC + 8 * MAXST * 16;

constexpr float LOG2E = 1.4426950408889634f;
constexpr float SCALE = 0.08838834764831845f;
constexpr u32 SPIN_CAP = 1u << 18;

__device__ __forceinline__ float lo16(u32 v) { return __uint_as_float(v << 16); }
__device__ __forceinline__ float hi16(u32 v) { return __uint_as_float(v & 0xffff0000u); }
__device__ __forceinline__ u32 pk2(float a, float b) { bf16x2 r = {(__bf16)a, (__bf16)b}; return __builtin_bit_cast(u32, r); }
__device__ __forceinline__ float rbf(float a) { return lo16(pk2(a, a)); }
__device__ __forceinline__ float dot2(u32 a, u32 b, float c) { return __builtin_amdgcn_fdot2_f32_bf16(__builtin_bit_cast(bf16x2, a), __builtin_bit_cast(bf16x2, b), c, false); }
template <int CTRL> __device__ __forceinline__ float dppf(float x) { return __builtin_bit_cast(float, __builtin_amdgcn_mov_dpp(__builtin_bit_cast(int, x), CTRL, 0xf, 0xf, true)); }
template <int CTRL> __device__ __forceinline__ u32 dppu(u32 x) { return (u32)__builtin_amdgcn_mov_dpp((int)x, CTRL, 0xf, 0xf, true); }
constexpr int XOR1 = 0xB1, XOR2 = 0x4E, HMIR = 0x141, ROR4 = 0x124, ROR8 = 0x128, NEWB = 0x150;
__device__ __forceinline__ float xrow_max(float x) {
  auto s = __builtin_amdgcn_permlane16_swap(__float_as_uint(x), __float_as_uint(x), false, false);
  x = fmaxf(__uint_as_float(s[0]), __uint_as_float(s[1]));
  auto t = __builtin_amdgcn_permlane32_swap(__float_as_uint(x), __float_as_uint(x), false, false);
  return fmaxf(__uint_as_float(t[0]), __uint_as_float(t[1]));
}
__device__ __forceinline__ float xrow_sum(float x) {
  auto s = __builtin_amdgcn_permlane16_swap(__float_as_uint(x), __float_as_uint(x), false, false);
  x = __uint_as_float(s[0]) + __uint_as_float(s[1]);
  auto t = __builtin_amdgcn_permlane32_swap(__float_as_uint(x), __float_as_uint(x), false, false);
  return __uint_as_float(t[0]) + __uint_as_float(t[1]);
}
__device__ __forceinline__ f32x4 mfma(u32x4 a, u32x4 b, f32x4 c) {
  return __builtin_amdgcn_mfma_f32_16x16x32_bf16(__builtin_bit_cast(bf16x8, a), __builtin_bit_cast(bf16x8, b), c, 0, 0, 0);
}
__device__ __forceinline__ rsrc_t mk(const void* p, u32 bytes) { return __builtin_amdgcn_make_buffer_rsrc((void*)p, 0, bytes, 0x00020000); }
template <int AUX> __device__ __forceinline__ u32x4 ld128(rsrc_t r, u32 voff, u32 soff) { return __builtin_amdgcn_raw_buffer_load_b128(r, voff, soff, AUX); }
template <int AUX> __device__ __forceinline__ u32x2 ld64(rsrc_t r, u32 voff, u32 soff) { return __builtin_amdgcn_raw_buffer_load_b64(r, voff, soff, AUX); }
__device__ __forceinline__ void st_granule(rsrc_t r, u32 off, float v, u32 tag) { __builtin_amdgcn_raw_buffer_store_b64(u32x2{__float_as_uint(v), tag}, r, off, 0, AUX_SC1); }
__device__ __forceinline__ void st_granule2(rsrc_t r, u32 off, float v0, float v1, u32 tag) { __builtin_amdgcn_raw_buffer_store_b128(u32x4{__float_as_uint(v0), tag, __float_as_uint(v1), tag}, r, off, 0, AUX_SC1); }
__device__ __forceinline__ bool tagged(const u32x4& a, u32 tag) { return (a[1] == tag) & (a[3] == tag); }
#define SB() __builtin_amdgcn_sched_barrier(0)
#define FRESH() asm volatile("" ::: "memory")

struct Params {
  const u16 *x, *kc, *vc, *wq, *wo, *wg, *wu, *wd;
  u16* out;
  unsigned char* ws;
};

struct Sel { u32 k0, k1, k2, k3; };
__device__ __forceinline__ u32x4 sel(u32 pp, const Sel& s) { return u32x4{pp & s.k0, pp & s.k1, pp & s.k2, pp & s.k3}; }

__global__ void __launch_bounds__(NT, 2) fused_decode(Params p) {
  __shared__ __attribute__((aligned(16))) float lds[L_END];
  const u32 t_start = (u32)__builtin_amdgcn_s_memrealtime();
  __builtin_amdgcn_sched_barrier(0);
  const int tid = threadIdx.x, lane = tid & 63, w = __builtin_amdgcn_readfirstlane(tid >> 6);
  const int g = lane >> 4, c = lane & 15, bb = c >> 3;
  const int bid = blockIdx.x, xc = bid & 7, jj = bid >> 3;
  const int par = xc & 1, h = (xc >> 1) + 4 * (jj >> 2), b = (jj >> 1) & 1, half = jj & 1;
  const int j = par + 2 * half + 4 * b;
  const int pid = h * 8 + j;
  const int jc = 4 * par + (j >> 1);
  const bool ffn = h < DF / 64;
  asm volatile("" ::: "v140");
  const rsrc_t rws = mk(p.ws, WS_BYTES);
  u32 tag;
  auto read_tag = [&]() __attribute__((always_inline)) {
    SB();
    tag = *(const __attribute__((address_space(4))) u32*)(p.ws + WS_EPOCH) + 0x9E3779B9u;
    asm volatile("" : "+s"(tag));
    SB();
  };

  const size_t slab = ((size_t)(b * H + h) * SK + half * (SK / 2)) * DH;
  const rsrc_t rk = mk(p.kc + slab, SK / 2 * DH * 2), rv = mk(p.vc + slab, SK / 2 * DH * 2);
  const int split = NE / 8 + (w < NE % 8 ? 1 : 0);
  const int nst = par ? 64 - split : split;
  const u32 kvbase = (par ? split : 0) * 32768u;
  const u32 slope = ((PACE_T - PACE_0) << 8) / (2 * nst);
  auto pace = [&](int u) __attribute__((always_inline)) {
    const u32 due = PACE_0 + ((u32)u * slope >> 8);
    while ((u32)__builtin_amdgcn_s_memrealtime() - t_start < due) __builtin_amdgcn_s_sleep(1);
  };
  u32 vo[4];
#pragma unroll
  for (int i = 0; i < 4; ++i) vo[i] = c * 16 + g * 256 + w * 1024 + i * 8192;
  u32x4 R[PRE][4];
  u32x4 wor[4], wdr[2];
  auto prefetch = [&]() __attribute__((always_inline)) {
#pragma unroll
    for (int s = 0; s < PRE; ++s) {
#pragma unroll
      for (int i = 0; i < 4; ++i) R[s][i] = ld128<AUX_NT>(rk, vo[i], kvbase + s * 32768u);
      SB();
    }
    const rsrc_t ro = mk(p.wo, H * DH * D * 2), rd = mk(p.wd, DF * D * 2);
    const u32 vw = c < 10 ? (128 * h + 16 * w + 4 * g) * (D * 2) + jc * 160 + c * 16 : OOB;
    const u32 vd = (ffn && c < 10) ? (64 * h + 8 * w + 2 * g) * (D * 2) + jc * 160 + c * 16 : OOB;
#pragma unroll
    for (int i = 0; i < 4; ++i) wor[i] = ld128<0>(ro, vw, i * (D * 2));
#pragma unroll
    for (int i = 0; i < 2; ++i) wdr[i] = ld128<0>(rd, vd, i * (D * 2));
    SB();
  };
  Sel sl;
  {
    const int mm = lane & 7;
    const u32 hm = (mm & 1) ? 0xffff0000u : 0x0000ffffu;
    sl.k0 = (mm >> 1) == 0 ? hm : 0u; sl.k1 = (mm >> 1) == 1 ? hm : 0u; sl.k2 = (mm >> 1) == 2 ? hm : 0u; sl.k3 = (mm >> 1) == 3 ? hm : 0u;
  }

  if (w == 0 || (w == 1 && ffn)) {
    u32x2 xs[5], xo[5];
    u32x4 wr[20];
    const rsrc_t rx = mk(p.x, 2 * D * 2);
#pragma unroll
    for (int i = 0; i < 5; ++i) xs[i] = ld64<0>(rx, (bb * D + 20 * (8 * g + (c & 7))) * 2 + i * 8, 0);
#pragma unroll
    for (int i = 0; i < 5; ++i) xo[i] = ld64<0>(rx, (bb * D + 80 * j + 20 * g) * 2 + i * 8, 0);
    const int ld = w == 0 ? H * DH : DF;
    const GAS u16* src = w == 0 ? (const GAS u16*)p.wq + h * DH + c * 8 : (const GAS u16*)(c < 8 ? p.wg : p.wu) + h * 64 + (c & 7) * 8;
    src += (80 * j + 20 * g) * ld;
#pragma unroll
    for (int i = 0; i < 20; ++i) wr[i] = __builtin_nontemporal_load((const GAS u32x4*)(src + i * ld));
    SB();
    __builtin_amdgcn_s_barrier();
    read_tag();
    float xv[20];
#pragma unroll
    for (int i = 0; i < 5; ++i) { xv[4 * i] = lo16(xs[i][0]); xv[4 * i + 1] = hi16(xs[i][0]); xv[4 * i + 2] = lo16(xs[i][1]); xv[4 * i + 3] = hi16(xs[i][1]); }
    float s = 0.f;
#pragma unroll
    for (int i = 0; i < 20; ++i) s += xv[i];
    s += dppf<XOR1>(s); s += dppf<XOR2>(s); s += dppf<HMIR>(s); s = xrow_sum(s);
    const float mean = s * (1.f / D);
    float q2 = 0.f;
#pragma unroll
    for (int i = 0; i < 20; ++i) { const float d = xv[i] - mean; q2 = fmaf(d, d, q2); }
    q2 += dppf<XOR1>(q2); q2 += dppf<XOR2>(q2); q2 += dppf<HMIR>(q2); q2 = xrow_sum(q2);
    const float mean_b = rbf(mean);
    const float rden = 1.f / rbf(__builtin_sqrtf(rbf(rbf(q2 * (1.f / D)) + 1e-5f)));
    u32 xn[20];
#pragma unroll
    for (int i = 0; i < 5; ++i) {
      const float t0 = rbf(lo16(xo[i][0]) - mean_b) * rden, t1 = rbf(hi16(xo[i][0]) - mean_b) * rden, t2 = rbf(lo16(xo[i][1]) - mean_b) * rden, t3 = rbf(hi16(xo[i][1]) - mean_b) * rden;
      xn[4 * i] = pk2(t0, t0); xn[4 * i + 1] = pk2(t1, t1); xn[4 * i + 2] = pk2(t2, t2); xn[4 * i + 3] = pk2(t3, t3);
    }
    f32x4 a = {0.f, 0.f, 0.f, 0.f}, a2 = a;
#pragma unroll
    for (int i = 0; i < 20; i += 2) { a = mfma(sel(xn[i], sl), wr[i], a); a2 = mfma(sel(xn[i + 1], sl), wr[i + 1], a2); }
    a += a2;
    const u32 o = (w == 0 ? WS_QP : WS_F1) + pid * 2048 + lane * 16;
    st_granule2(rws, o, a[0], a[1], tag);
    st_granule2(rws, o + 1024, a[2], a[3], tag);
    if (w == 0) {
      const u32 oq = WS_QP + h * 8 * 2048 + (lane >> 5) * 1024 + (32 * b + (lane & 31)) * 16;
      u32x4 qa[8];
      for (u32 spins = 0; spins < SPIN_CAP; ++spins) {
        FRESH();
#pragma unroll
        for (int pp = 0; pp < 8; ++pp) qa[pp] = ld128<AUX_SC1>(rws, oq + pp * 2048, 0);
        bool ok = true;
#pragma unroll
        for (int pp = 0; pp < 8; ++pp) ok &= tagged(qa[pp], tag);
        if (__all(ok)) break;
        __builtin_amdgcn_s_sleep(1);
      }
      f32x2 q = {0.f, 0.f};
#pragma unroll
      for (int pp = 0; pp < 8; ++pp) { q[0] += __uint_as_float(qa[pp][0]); q[1] += __uint_as_float(qa[pp][2]); }
      ((u32*)lds)[L_Q + 4 * (lane & 15) + 2 * ((lane >> 4) & 1) + (lane >> 5)] = pk2(q[0], q[1]);
      __syncthreads();
      prefetch();
    } else {
      prefetch();
      __syncthreads();
    }
  } else {
    __builtin_amdgcn_s_barrier();
    SB();
    const u32 vtouch = __builtin_amdgcn_raw_buffer_load_b32(rv, (w == 7 && lane == 0) ? kvbase : OOB, 0, AUX_NT);
    prefetch();
    asm volatile("" :: "v"(vtouch));
    read_tag();
    __syncthreads();
  }
  const u32x4 qv = *(const u32x4*)&lds[L_Q + 4 * c];

  const bool c3 = (lane & 8) != 0, c2 = (lane & 4) != 0;
  float* sco = &lds[L_SC + w * (MAXST * 16) + g * 4 + (c >> 2)];
  float m = -INFINITY;
  auto score = [&](int s, int t, int NEXT, int nx) __attribute__((always_inline)) {
    float xd[4];
#pragma unroll
    for (int i = 0; i < 4; ++i) {
      float d = dot2(R[s][i][0], qv[0], 0.f);
      d = dot2(R[s][i][1], qv[1], d); d = dot2(R[s][i][2], qv[2], d); xd[i] = dot2(R[s][i][3], qv[3], d);
      if (NEXT) {
        asm volatile("" : "+v"(xd[i])); SB();
        R[s][i] = ld128<AUX_NT>(NEXT == 1 ? rk : rv, vo[i], kvbase + nx * 32768u);
        SB();
      }
    }
    const float y0 = (c3 ? xd[2] : xd[0]) + dppf<ROR8>(c3 ? xd[0] : xd[2]);
    const float y1 = (c3 ? xd[3] : xd[1]) + dppf<ROR8>(c3 ? xd[1] : xd[3]);
    float sc = (c2 ? y1 : y0) + dppf<HMIR>(c2 ? y0 : y1);
    sc += dppf<XOR1>(sc); sc += dppf<XOR2>(sc);
    sc = rbf(rbf(sc) * SCALE);
    m = fmaxf(m, sc);
    sco[t * 16] = sc;
    SB();
  };
  score(0, 0, 1, PRE);
#pragma unroll
  for (int s = 1; s < PRE; ++s) score(s, s, 0, 0);
#pragma unroll 1
  for (int t = PRE; t < nst - 1; ++t) {
    pace(t);
    score(0, t, 1, t + 1);
  }
  score(0, nst - 1, 2, 0);
  m = fmaxf(m, dppf<ROR4>(m)); m = fmaxf(m, dppf<ROR8>(m)); m = xrow_max(m);

  float l = 0.f;
  f32x4 acc = {0.f, 0.f, 0.f, 0.f};
  auto weigh = [&](int t, bool more) __attribute__((always_inline)) {
    const float pr = __builtin_amdgcn_exp2f((sco[t * 16] - m) * LOG2E);
    l += pr;
    const u32 pp = pk2(pr, pr);
    const u32x4 a0 = sel(dppu<NEWB + 0>(pp), sl), a1 = sel(dppu<NEWB + 4>(pp), sl), a2 = sel(dppu<NEWB + 8>(pp), sl), a3 = sel(dppu<NEWB + 12>(pp), sl);
    const u32 so = kvbase + (t + 1) * 32768u;
    acc = mfma(a0, R[0][0], acc); SB(); if (more) R[0][0] = ld128<AUX_NT>(rv, vo[0], so); SB();
    acc = mfma(a1, R[0][1], acc); SB(); if (more) R[0][1] = ld128<AUX_NT>(rv, vo[1], so); SB();
    acc = mfma(a2, R[0][2], acc); SB(); if (more) R[0][2] = ld128<AUX_NT>(rv, vo[2], so); SB();
    acc = mfma(a3, R[0][3], acc); SB(); if (more) R[0][3] = ld128<AUX_NT>(rv, vo[3], so); SB();
  };
#pragma unroll 1
  for (int t = 0; t < nst - 2; ++t) {
    pace(nst + t);
    weigh(t, true);
  }
  const u32 of1 = ffn ? WS_F1 + (h * 8 + w) * 2048 + lane * 16 : OOB;
  u32x4 f0 = ld128<AUX_SC1>(rws, of1, 0), f1 = ld128<AUX_SC1>(rws, of1, 1024);
  const int fk0 = 32 * (h < 5 ? h : 0) + 2 * c, fbo = fk0 >= 80, fcol = 80 * jc + fk0 - 80 * fbo;
  const u32 xraw = __builtin_amdgcn_raw_buffer_load_b32(mk(p.x, 2 * D * 2), (fbo * D + fcol) * 2, 0, 0);
  SB();
  weigh(nst - 2, true);
  weigh(nst - 1, false);
  if (ffn && !__all(tagged(f0, tag) & tagged(f1, tag))) {
    for (u32 spins = 0; spins < SPIN_CAP; ++spins) {
      __builtin_amdgcn_s_sleep(8);
      FRESH();
      f0 = ld128<AUX_SC1>(rws, of1, 0); f1 = ld128<AUX_SC1>(rws, of1, 1024);
      if (__all(tagged(f0, tag) & tagged(f1, tag))) break;
    }
  }

  {
    float ls = l; ls += dppf<XOR1>(ls); ls += dppf<XOR2>(ls); ls += dppf<ROR4>(ls); ls += dppf<ROR8>(ls); ls = xrow_sum(ls) * 0.25f;
    float* rec = &lds[L_REC + w * 132];
    if (lane < 32) *(f32x4*)&rec[4 + 8 * c + 4 * g] = acc;
    if (lane == 0) { rec[0] = m; rec[1] = ls; }
    *(u32x4*)&lds[L_F1 + w * 256 + (g >> 1) * 128 + 8 * c + 4 * (g & 1)] = u32x4{f0[0], f0[2], f1[0], f1[2]};
  }
  __syncthreads();
  auto ffn2 = [&]() __attribute__((always_inline)) {
    f32x4 af = {0.f, 0.f, 0.f, 0.f};
#pragma unroll
    for (int i = 0; i < 2; ++i) {
      float gs = 0.f, us = 0.f;
#pragma unroll
      for (int pp = 0; pp < 8; ++pp) { gs += lds[L_F1 + pp * 256 + bb * 128 + 8 * w + 2 * g + i]; us += lds[L_F1 + pp * 256 + bb * 128 + 64 + 8 * w + 2 * g + i]; }
      const float gg = rbf(gs), uu = rbf(us);
      const float z = 0.7978845608028654f * (gg + 0.044715f * gg * gg * gg);
      const float th = 1.f - 2.f / (__builtin_amdgcn_exp2f(z * (2.f * LOG2E)) + 1.f);
      const float hv = rbf(0.5f * gg * (1.f + th)) * uu;
      af = mfma(sel(pk2(hv, hv), sl), wdr[i], af);
    }
    *(f32x4*)&lds[L_FR + w * 256 + (g >> 1) * 128 + 8 * c + 4 * (g & 1)] = af;
    asm volatile("s_waitcnt lgkmcnt(0)" ::: "memory");
    __builtin_amdgcn_s_barrier();
    FRESH();
  };
  if (w == 0) {
    float mw[8], M = -INFINITY;
#pragma unroll
    for (int ww = 0; ww < 8; ++ww) { mw[ww] = lds[L_REC + ww * 132]; M = fmaxf(M, mw[ww]); }
    f32x2 o = {0.f, 0.f}; float L = 0.f;
#pragma unroll
    for (int ww = 0; ww < 8; ++ww) {
      const float e = __builtin_amdgcn_exp2f((mw[ww] - M) * LOG2E);
      o += *(const f32x2*)&lds[L_REC + ww * 132 + 4 + 2 * lane] * e; L = fmaf(e, lds[L_REC + ww * 132 + 1], L);
    }
    const u32 o1 = WS_H1 + pid * H1_STRIDE;
    st_granule2(rws, o1 + lane * 16, o[0], o[1], tag);
    st_granule2(rws, lane == 0 ? o1 + 1024 : OOB, M, L, tag);
    float* r1 = &lds[L_R1 + j * 132];
    *(f32x2*)&r1[4 + 2 * lane] = o;
    if (lane == 0) *(f32x2*)&r1[0] = f32x2{M, L};
    SB();
    ffn2();
  } else {
    const int mj = j ^ w;
    const u32 o1 = WS_H1 + (h * 8 + mj) * H1_STRIDE;
    const u32 oml = lane == 0 ? o1 + 1024 : OOB;
    u32x4 ra = ld128<AUX_SC1>(rws, o1 + lane * 16, 0), rml = ld128<AUX_SC1>(rws, oml, 0);
    SB();
    ffn2();
    const bool have = __all(tagged(ra, tag) & ((lane != 0) | tagged(rml, tag)));
    SB();
    if (w < 4 && tid - 64 < 160) {
      const int k = tid - 64, bo = k >= 80, col = k - 80 * bo;
      float v = 0.f;
#pragma unroll
      for (int ww = 0; ww < 8; ++ww) v += lds[L_FR + ww * 256 + bo * 128 + col];
      st_granule(rws, WS_H2 + pid * H2_STRIDE + (160 + k) * 8, v, tag);
    }
    if (!have) {
      for (u32 spins = 0; spins < SPIN_CAP; ++spins) {
        __builtin_amdgcn_s_sleep(1);
        FRESH();
        rml = ld128<AUX_SC1>(rws, oml, 0);
        ra = ld128<AUX_SC1>(rws, o1 + lane * 16, 0);
        if (__all(tagged(ra, tag) & ((lane != 0) | tagged(rml, tag)))) break;
      }
    }
    float* r1 = &lds[L_R1 + mj * 132];
    *(u32x2*)&r1[4 + 2 * lane] = u32x2{ra[0], ra[2]};
    if (lane == 0) *(u32x2*)&r1[0] = u32x2{rml[0], rml[2]};
  }
  __syncthreads();
  {
    const float* r1 = &lds[L_R1 + bb * 4 * 132];
    const float m0 = r1[0], m1 = r1[132], m2 = r1[264], m3 = r1[396];
    const float M = fmaxf(fmaxf(m0, m1), fmaxf(m2, m3));
    const float e0 = __builtin_amdgcn_exp2f((m0 - M) * LOG2E), e1 = __builtin_amdgcn_exp2f((m1 - M) * LOG2E), e2 = __builtin_amdgcn_exp2f((m2 - M) * LOG2E), e3 = __builtin_amdgcn_exp2f((m3 - M) * LOG2E);
    const float L = e0 * r1[1] + e1 * r1[133] + e2 * r1[265] + e3 * r1[397];
    const int d0 = 4 + 16 * w + 4 * g;
    f32x4 o = *(const f32x4*)&r1[d0] * e0 + *(const f32x4*)&r1[132 + d0] * e1 + *(const f32x4*)&r1[264 + d0] * e2 + *(const f32x4*)&r1[396 + d0] * e3;
    o *= 1.f / L;
    const f32x4 z4 = {0.f, 0.f, 0.f, 0.f};
    f32x4 as = mfma(sel(pk2(o[0], o[0]), sl), wor[0], z4), as2 = mfma(sel(pk2(o[1], o[1]), sl), wor[1], z4);
    as = mfma(sel(pk2(o[2], o[2]), sl), wor[2], as); as2 = mfma(sel(pk2(o[3], o[3]), sl), wor[3], as2);
    *(f32x4*)&lds[L_SR + w * 256 + (g >> 1) * 128 + 8 * c + 4 * (g & 1)] = as + as2;
  }
  __syncthreads();
  if (tid < 160) {
    const int bo = tid >= 80, col = tid - 80 * bo;
    float v = 0.f;
#pragma unroll
    for (int ww = 0; ww < 8; ++ww) v += lds[L_SR + ww * 256 + bo * 128 + col];
    st_granule(rws, WS_H2 + pid * H2_STRIDE + tid * 8, v, tag);
  }
  if (h < 5) {
    const u32 o2 = WS_H2 + ((4 * w + g) * 8 + j) * H2_STRIDE + fk0 * 8;
    u32x4 aS = {0u, 0u, 0u, 0u}, aF = aS;
    bool nS = true, nF = true;
    for (u32 spins = 0; spins < SPIN_CAP; ++spins) {
      FRESH();
      if (nS) aS = ld128<AUX_SC1>(rws, o2, 0);
      if (nF) aF = ld128<AUX_SC1>(rws, o2 + 1280, 0);
      nS = !tagged(aS, tag); nF = !tagged(aF, tag);
      if (!__any(nS || nF)) break;
      __builtin_amdgcn_s_sleep(1);
    }
    const f32x4 part = {xrow_sum(__uint_as_float(aS[0])), xrow_sum(__uint_as_float(aS[2])), xrow_sum(__uint_as_float(aF[0])), xrow_sum(__uint_as_float(aF[2]))};
    if (g == 0) *(f32x4*)&lds[L_FIN + w * 64 + 4 * c] = part;
    __syncthreads();
    if (w == 7) {
      f32x4 t = {0.f, 0.f, 0.f, 0.f};
#pragma unroll
      for (int ww = 0; ww < 8; ++ww) t += *(const f32x4*)&lds[L_FIN + ww * 64 + 4 * c];
      const float s0 = rbf(t[0]), s1 = rbf(t[1]), f0v = rbf(t[2]), f1v = rbf(t[3]);
      const float g0 = rbf(f0v / (1.f + __builtin_amdgcn_exp2f(-f0v * LOG2E))), g1 = rbf(f1v / (1.f + __builtin_amdgcn_exp2f(-f1v * LOG2E)));
      if (g == 0) *(u32*)(p.out + fbo * D + fcol) = pk2(lo16(xraw) + rbf(s0 * g0), hi16(xraw) + rbf(s1 * g1));
      if (bid == 0 && lane == 0) __builtin_amdgcn_raw_buffer_store_b32(tag, rws, WS_EPOCH, 0, AUX_SC1);
    }
  }
}
}

extern "C" void kernel_launch(void* const* d_in, const int* in_sizes, int n_in, void* d_out, int out_size, void* d_ws, size_t ws_size, hipStream_t stream) {
  Params p;
  p.x = (const u16*)d_in[0]; p.kc = (const u16*)d_in[1]; p.vc = (const u16*)d_in[2]; p.wq = (const u16*)d_in[3]; p.wo = (const u16*)d_in[4];
  p.wg = (const u16*)d_in[6]; p.wu = (const u16*)d_in[7]; p.wd = (const u16*)d_in[8];
  p.out = (u16*)d_out; p.ws = (unsigned char*)d_ws;
  fused_decode<<<NWG, NT, 0, stream>>>(p);
}
```

```cpp
#include <hip/hip_runtime.h>
#include <stdint.h>

namespace {
constexpr int D = 640, H = 32, SK = 16384, DH = 128, DF = 1728;
constexpr int NWG = 256, NT = 512;
constexpr int NE = 264;
constexpr int PRE = 6;
constexpr int MAXST = 34;
constexpr unsigned PACE_T = 7550, PACE_0 = 450;

typedef unsigned u32;
typedef unsigned long long u64;
typedef unsigned short u16;
typedef u32 u32x2 __attribute__((ext_vector_type(2)));
typedef u32 u32x4 __attribute__((ext_vector_type(4)));
typedef float f32x2 __attribute__((ext_vector_type(2)));
typedef float f32x4 __attribute__((ext_vector_type(4)));
typedef __bf16 bf16x2 __attribute__((ext_vector_type(2)));
typedef __bf16 bf16x8 __attribute__((ext_vector_type(8)));
typedef __amdgpu_buffer_rsrc_t rsrc_t;
#define GAS __attribute__((address_space(1)))

constexpr int AUX_NT = 2, AUX_SC1 = 16;
constexpr u32 OOB = 0x80000000u;

constexpr u32 WS_EPOCH = 0;
constexpr u32 WS_QP = 4096;
constexpr u32 WS_F1 = WS_QP + NWG * 2048;
constexpr u32 WS_H1 = WS_F1 + NWG * 2048;
constexpr u32 H1_STRIDE = 144 * 8;
constexpr u32 WS_H2 = WS_H1 + NWG * H1_STRIDE;
constexpr u32 H2_STRIDE = 320 * 8;
constexpr u32 WS_BYTES = WS_H2 + NWG * H2_STRIDE;

constexpr int L_Q = 0;
constexpr int L_REC = L_Q + 64;
constexpr int L_F1 = L_REC + 8 * 132;
constexpr int L_R1 = L_F1 + 8 * 256;
constexpr int L_SR = L_R1 + 8 * 132;
constexpr int L_FR = L_SR + 8 * 256;
constexpr int L_FIN = L_FR + 8 * 256;
constexpr int L_SC = L_FIN + 8 * 64;
constexpr int L_END = L_SC + 8 * MAXST * 16;

constexpr float LOG2E = 1.4426950408889634f;
constexpr float SCALE = 0.08838834764831845f;
constexpr u32 SPIN_CAP = 1u << 18;

__device__ __forceinline__ float lo16(u32 v) { return __uint_as_float(v << 16); }
__device__ __forceinline__ float hi16(u32 v) { return __uint_as_float(v & 0xffff0000u); }
__device__ __forceinline__ u32 pk2(float a, float b) { bf16x2 r = {(__bf16)a, (__bf16)b}; return __builtin_bit_cast(u32, r); }
__device__ __forceinline__ float rbf(float a) { return lo16(pk2(a, a)); }
__device__ __forceinline__ float dot2(u32 a, u32 b, float c) { return __builtin_amdgcn_fdot2_f32_bf16(__builtin_bit_cast(bf16x2, a), __builtin_bit_cast(bf16x2, b), c, false); }
template <int CTRL> __device__ __forceinline__ float dppf(float x) { return __builtin_bit_cast(float, __builtin_amdgcn_mov_dpp(__builtin_bit_cast(int, x), CTRL, 0xf, 0xf, true)); }
template <int CTRL> __device__ __forceinline__ u32 dppu(u32 x) { return (u32)__builtin_amdgcn_mov_dpp((int)x, CTRL, 0xf, 0xf, true); }
constexpr int XOR1 = 0xB1, XOR2 = 0x4E, HMIR = 0x141, ROR4 = 0x124, ROR8 = 0x128, NEWB = 0x150;
__device__ __forceinline__ float xrow_max(float x) {
  auto s = __builtin_amdgcn_permlane16_swap(__float_as_uint(x), __float_as_uint(x), false, false);
  x = fmaxf(__uint_as_float(s[0]), __uint_as_float(s[1]));
  auto t = __builtin_amdgcn_permlane32_swap(__float_as_uint(x), __float_as_uint(x), false, false);
  return fmaxf(__uint_as_float(t[0]), __uint_as_float(t[1]));
}
__device__ __forceinline__ float xrow_sum(float x) {
  auto s = __builtin_amdgcn_permlane16_swap(__float_as_uint(x), __float_as_uint(x), false, false);
  x = __uint_as_float(s[0]) + __uint_as_float(s[1]);
  auto t = __builtin_amdgcn_permlane32_swap(__float_as_uint(x), __float_as_uint(x), false, false);
  return __uint_as_float(t[0]) + __uint_as_float(t[1]);
}
__device__ __forceinline__ f32x4 mfma(u32x4 a, u32x4 b, f32x4 c) {
  return __builtin_amdgcn_mfma_f32_16x16x32_bf16(__builtin_bit_cast(bf16x8, a), __builtin_bit_cast(bf16x8, b), c, 0, 0, 0);
}
__device__ __forceinline__ rsrc_t mk(const void* p, u32 bytes) { return __builtin_amdgcn_make_buffer_rsrc((void*)p, 0, bytes, 0x00020000); }
template <int AUX> __device__ __forceinline__ u32x4 ld128(rsrc_t r, u32 voff, u32 soff) { return __builtin_amdgcn_raw_buffer_load_b128(r, voff, soff, AUX); }
template <int AUX> __device__ __forceinline__ u32x2 ld64(rsrc_t r, u32 voff, u32 soff) { return __builtin_amdgcn_raw_buffer_load_b64(r, voff, soff, AUX); }
__device__ __forceinline__ void st_granule(rsrc_t r, u32 off, float v, u32 tag) { __builtin_amdgcn_raw_buffer_store_b64(u32x2{__float_as_uint(v), tag}, r, off, 0, AUX_SC1); }
__device__ __forceinline__ void st_granule2(rsrc_t r, u32 off, float v0, float v1, u32 tag) { __builtin_amdgcn_raw_buffer_store_b128(u32x4{__float_as_uint(v0), tag, __float_as_uint(v1), tag}, r, off, 0, AUX_SC1); }
__device__ __forceinline__ bool tagged(const u32x4& a, u32 tag) { return (a[1] == tag) & (a[3] == tag); }
#define SB() __builtin_amdgcn_sched_barrier(0)
#define FRESH() asm volatile("" ::: "memory")

struct Params {
  const u16 *x, *kc, *vc, *wq, *wo, *wg, *wu, *wd;
  u16* out;
  unsigned char* ws;
};

struct Sel { u32 k0, k1, k2, k3; };
__device__ __forceinline__ u32x4 sel(u32 pp, const Sel& s) { return u32x4{pp & s.k0, pp & s.k1, pp & s.k2, pp & s.k3}; }

__global__ void __launch_bounds__(NT, 2) fused_decode(Params p) {
  __shared__ __attribute__((aligned(16))) float lds[L_END];
  const u32 t_start = (u32)__builtin_amdgcn_s_memrealtime();
  __builtin_amdgcn_sched_barrier(0);
  const int tid = threadIdx.x, lane = tid & 63, w = __builtin_amdgcn_readfirstlane(tid >> 6);
  const int g = lane >> 4, c = lane & 15, bb = c >> 3;
  const int bid = blockIdx.x, xc = bid & 7, jj = bid >> 3;
  const int par = xc & 1, h = (xc >> 1) + 4 * (jj >> 2), b = (jj >> 1) & 1, half = jj & 1;
  const int j = par + 2 * half + 4 * b;
  const int pid = h * 8 + j;
  const int jc = 4 * par + (j >> 1);
  const bool ffn = h < DF / 64;
  asm volatile("" ::: "v140");
  const rsrc_t rws = mk(p.ws, WS_BYTES);
  u32 tag;
  auto read_tag = [&]() __attribute__((always_inline)) {
    SB();
    tag = *(const __attribute__((address_space(4))) u32*)(p.ws + WS_EPOCH) + 0x9E3779B9u;
    asm volatile("" : "+s"(tag));
    SB();
  };

  const size_t slab = ((size_t)(b * H + h) * SK + half * (SK / 2)) * DH;
  const rsrc_t rk = mk(p.kc + slab, SK / 2 * DH * 2), rv = mk(p.vc + slab, SK / 2 * DH * 2);
  const int split = NE / 8 + (w < NE % 8 ? 1 : 0);
  const int nst = par ? 64 - split : split;
  const u32 kvbase = (par ? split : 0) * 32768u;
  const u32 slope = ((PACE_T - PACE_0) << 8) / (2 * nst);
  auto pace = [&](int u) __attribute__((always_inline)) {
    const u32 due = PACE_0 + ((u32)u * slope >> 8);
    while ((u32)__builtin_amdgcn_s_memrealtime() - t_start < due) __builtin_amdgcn_s_sleep(1);
  };
  u32 vo[4];
#pragma unroll
  for (int i = 0; i < 4; ++i) vo[i] = c * 16 + g * 256 + w * 1024 + i * 8192;
  u32x4 R[PRE][4];
  u32x4 wor[4], wdr[2];
  auto fillk = [&](int s) __attribute__((always_inline)) {
#pragma unroll
    for (int i = 0; i < 4; ++i) R[s][i] = ld128<AUX_NT>(rk, vo[i], kvbase + s * 32768u);
    SB();
  };
  auto prefetch = [&](int from) __attribute__((always_inline)) {
#pragma unroll
    for (int s = 0; s < PRE; ++s) if (s >= from) fillk(s);
    const rsrc_t ro = mk(p.wo, H * DH * D * 2), rd = mk(p.wd, DF * D * 2);
    const u32 vw = c < 10 ? (128 * h + 16 * w + 4 * g) * (D * 2) + jc * 160 + c * 16 : OOB;
    const u32 vd = (ffn && c < 10) ? (64 * h + 8 * w + 2 * g) * (D * 2) + jc * 160 + c * 16 : OOB;
#pragma unroll
    for (int i = 0; i < 4; ++i) wor[i] = ld128<0>(ro, vw, i * (D * 2));
#pragma unroll
    for (int i = 0; i < 2; ++i) wdr[i] = ld128<0>(rd, vd, i * (D * 2));
    SB();
  };
  Sel sl;
  {
    const int mm = lane & 7;
    const u32 hm = (mm & 1) ? 0xffff0000u : 0x0000ffffu;
    sl.k0 = (mm >> 1) == 0 ? hm : 0u; sl.k1 = (mm >> 1) == 1 ? hm : 0u; sl.k2 = (mm >> 1) == 2 ? hm : 0u; sl.k3 = (mm >> 1) == 3 ? hm : 0u;
  }

  if (w == 0 || (w == 1 && ffn)) {
    u32x2 xs[5], xo[5];
    u32x4 wr[20];
    const rsrc_t rx = mk(p.x, 2 * D * 2);
#pragma unroll
    for (int i = 0; i < 5; ++i) xs[i] = ld64<0>(rx, (bb * D + 20 * (8 * g + (c & 7))) * 2 + i * 8, 0);
#pragma unroll
    for (int i = 0; i < 5; ++i) xo[i] = ld64<0>(rx, (bb * D + 80 * j + 20 * g) * 2 + i * 8, 0);
    const int ld = w == 0 ? H * DH : DF;
    const GAS u16* src = w == 0 ? (const GAS u16*)p.wq + h * DH + c * 8 : (const GAS u16*)(c < 8 ? p.wg : p.wu) + h * 64 + (c & 7) * 8;
    src += (80 * j + 20 * g) * ld;
#pragma unroll
    for (int i = 0; i < 20; ++i) wr[i] = __builtin_nontemporal_load((const GAS u32x4*)(src + i * ld));
    SB();
    __builtin_amdgcn_s_barrier();
    read_tag();
    float xv[20];
#pragma unroll
    for (int i = 0; i < 5; ++i) { xv[4 * i] = lo16(xs[i][0]); xv[4 * i + 1] = hi16(xs[i][0]); xv[4 * i + 2] = lo16(xs[i][1]); xv[4 * i + 3] = hi16(xs[i][1]); }
    float s = 0.f;
#pragma unroll
    for (int i = 0; i < 20; ++i) s += xv[i];
    s += dppf<XOR1>(s); s += dppf<XOR2>(s); s += dppf<HMIR>(s); s = xrow_sum(s);
    const float mean = s * (1.f / D);
    float q2 = 0.f;
#pragma unroll
    for (int i = 0; i < 20; ++i) { const float d = xv[i] - mean; q2 = fmaf(d, d, q2); }
    q2 += dppf<XOR1>(q2); q2 += dppf<XOR2>(q2); q2 += dppf<HMIR>(q2); q2 = xrow_sum(q2);
    const float mean_b = rbf(mean);
    const float rden = 1.f / rbf(__builtin_sqrtf(rbf(rbf(q2 * (1.f / D)) + 1e-5f)));
    u32 xn[20];
#pragma unroll
    for (int i = 0; i < 5; ++i) {
      const float t0 = rbf(lo16(xo[i][0]) - mean_b) * rden, t1 = rbf(hi16(xo[i][0]) - mean_b) * rden, t2 = rbf(lo16(xo[i][1]) - mean_b) * rden, t3 = rbf(hi16(xo[i][1]) - mean_b) * rden;
      xn[4 * i] = pk2(t0, t0); xn[4 * i + 1] = pk2(t1, t1); xn[4 * i + 2] = pk2(t2, t2); xn[4 * i + 3] = pk2(t3, t3);
    }
    f32x4 a = {0.f, 0.f, 0.f, 0.f}, a2 = a;
#pragma unroll
    for (int i = 0; i < 20; i += 2) { a = mfma(sel(xn[i], sl), wr[i], a); a2 = mfma(sel(xn[i + 1], sl), wr[i + 1], a2); }
    a += a2;
    const u32 o = (w == 0 ? WS_QP : WS_F1) + pid * 2048 + lane * 16;
    st_granule2(rws, o, a[0], a[1], tag);
    st_granule2(rws, o + 1024, a[2], a[3], tag);
    if (w == 0) {
      const u32 oq = WS_QP + h * 8 * 2048 + (lane >> 5) * 1024 + (32 * b + (lane & 31)) * 16;
      u32x4 qa[8];
      u32 need = 0xffu;
      __builtin_amdgcn_s_sleep(32);
      for (u32 spins = 0; spins < SPIN_CAP; ++spins) {
        FRESH();
#pragma unroll
        for (int pp = 0; pp < 8; ++pp) if (need >> pp & 1) qa[pp] = ld128<AUX_SC1>(rws, oq + pp * 2048, 0);
        u32 got = 0;
#pragma unroll
        for (int pp = 0; pp < 8; ++pp) got |= __all(tagged(qa[pp], tag)) ? 1u << pp : 0u;
        need &= ~got;
        if (!need) break;
        __builtin_amdgcn_s_sleep(1);
      }
      f32x2 q = {0.f, 0.f};
#pragma unroll
      for (int pp = 0; pp < 8; ++pp) { q[0] += __uint_as_float(qa[pp][0]); q[1] += __uint_as_float(qa[pp][2]); }
      ((u32*)lds)[L_Q + 4 * (lane & 15) + 2 * ((lane >> 4) & 1) + (lane >> 5)] = pk2(q[0], q[1]);
      __syncthreads();
      prefetch(0);
    } else {
      prefetch(0);
      __syncthreads();
    }
  } else {
    fillk(0);
    __builtin_amdgcn_s_barrier();
    SB();
    const u32 vtouch = __builtin_amdgcn_raw_buffer_load_b32(rv, (w == 7 && lane == 0) ? kvbase : OOB, 0, AUX_NT);
    prefetch(1);
    asm volatile("" :: "v"(vtouch));
    read_tag();
    __syncthreads();
  }
  const u32x4 qv = *(const u32x4*)&lds[L_Q + 4 * c];

  const bool c3 = (lane & 8) != 0, c2 = (lane & 4) != 0;
  float* sco = &lds[L_SC + w * (MAXST * 16) + g * 4 + (c >> 2)];
  float m = -INFINITY;
  auto score = [&](int s, int t, int NEXT, int nx) __attribute__((always_inline)) {
    float xd[4];
#pragma unroll
    for (int i = 0; i < 4; ++i) {
      float d = dot2(R[s][i][0], qv[0], 0.f);
      d = dot2(R[s][i][1], qv[1], d); d = dot2(R[s][i][2], qv[2], d); xd[i] = dot2(R[s][i][3], qv[3], d);
      if (NEXT) {
        asm volatile("" : "+v"(xd[i])); SB();
        R[s][i] = ld128<AUX_NT>(NEXT == 1 ? rk : rv, vo[i], kvbase + nx * 32768u);
        SB();
      }
    }
    const float y0 = (c3 ? xd[2] : xd[0]) + dppf<ROR8>(c3 ? xd[0] : xd[2]);
    const float y1 = (c3 ? xd[3] : xd[1]) + dppf<ROR8>(c3 ? xd[1] : xd[3]);
    float sc = (c2 ? y1 : y0) + dppf<HMIR>(c2 ? y0 : y1);
    sc += dppf<XOR1>(sc); sc += dppf<XOR2>(sc);
    sc = rbf(rbf(sc) * SCALE);
    m = fmaxf(m, sc);
    sco[t * 16] = sc;
    SB();
  };
  score(0, 0, 1, PRE);
#pragma unroll
  for (int s = 1; s < PRE; ++s) score(s, s, 0, 0);
#pragma unroll 1
  for (int t = PRE; t < nst - 1; ++t) {
    pace(t);
    score(0, t, 1, t + 1);
  }
  score(0, nst - 1, 2, 0);
  m = fmaxf(m, dppf<ROR4>(m)); m = fmaxf(m, dppf<ROR8>(m)); m = xrow_max(m);

  float l = 0.f;
  f32x4 acc = {0.f, 0.f, 0.f, 0.f};
  auto weigh = [&](int t, bool more) __attribute__((always_inline)) {
    const float pr = __builtin_amdgcn_exp2f((sco[t * 16] - m) * LOG2E);
    l += pr;
    const u32 pp = pk2(pr, pr);
    const u32x4 a0 = sel(dppu<NEWB + 0>(pp), sl), a1 = sel(dppu<NEWB + 4>(pp), sl), a2 = sel(dppu<NEWB + 8>(pp), sl), a3 = sel(dppu<NEWB + 12>(pp), sl);
    const u32 so = kvbase + (t + 1) * 32768u;
    acc = mfma(a0, R[0][0], acc); SB(); if (more) R[0][0] = ld128<AUX_NT>(rv, vo[0], so); SB();
    acc = mfma(a1, R[0][1], acc); SB(); if (more) R[0][1] = ld128<AUX_NT>(rv, vo[1], so); SB();
    acc = mfma(a2, R[0][2], acc); SB(); if (more) R[0][2] = ld128<AUX_NT>(rv, vo[2], so); SB();
    acc = mfma(a3, R[0][3], acc); SB(); if (more) R[0][3] = ld128<AUX_NT>(rv, vo[3], so); SB();
  };
#pragma unroll 1
  for (int t = 0; t < nst - 2; ++t) {
    pace(nst + t);
    weigh(t, true);
  }
  const u32 of1 = ffn ? WS_F1 + (h * 8 + w) * 2048 + lane * 16 : OOB;
  u32x4 f0 = ld128<AUX_SC1>(rws, of1, 0), f1 = ld128<AUX_SC1>(rws, of1, 1024);
  const int fk0 = 32 * (h < 5 ? h : 0) + 2 * c, fbo = fk0 >= 80, fcol = 80 * jc + fk0 - 80 * fbo;
  const u32 xraw = __builtin_amdgcn_raw_buffer_load_b32(mk(p.x, 2 * D * 2), (fbo * D + fcol) * 2, 0, 0);
  SB();
  weigh(nst - 2, true);
  weigh(nst - 1, false);
  if (ffn && !__all(tagged(f0, tag) & tagged(f1, tag))) {
    for (u32 spins = 0; spins < SPIN_CAP; ++spins) {
      __builtin_amdgcn_s_sleep(8);
      FRESH();
      f0 = ld128<AUX_SC1>(rws, of1, 0); f1 = ld128<AUX_SC1>(rws, of1, 1024);
      if (__all(tagged(f0, tag) & tagged(f1, tag))) break;
    }
  }

  {
    float ls = l; ls += dppf<XOR1>(ls); ls += dppf<XOR2>(ls); ls += dppf<ROR4>(ls); ls += dppf<ROR8>(ls); ls = xrow_sum(ls) * 0.25f;
    float* rec = &lds[L_REC + w * 132];
    if (lane < 32) *(f32x4*)&rec[4 + 8 * c + 4 * g] = acc;
    if (lane == 0) { rec[0] = m; rec[1] = ls; }
    *(u32x4*)&lds[L_F1 + w * 256 + (g >> 1) * 128 + 8 * c + 4 * (g & 1)] = u32x4{f0[0], f0[2], f1[0], f1[2]};
  }
  __syncthreads();
  auto ffn2 = [&]() __attribute__((always_inline)) {
    f32x4 af = {0.f, 0.f, 0.f, 0.f};
#pragma unroll
    for (int i = 0; i < 2; ++i) {
      float gs = 0.f, us = 0.f;
#pragma unroll
      for (int pp = 0; pp < 8; ++pp) { gs += lds[L_F1 + pp * 256 + bb * 128 + 8 * w + 2 * g + i]; us += lds[L_F1 + pp * 256 + bb * 128 + 64 + 8 * w + 2 * g + i]; }
      const float gg = rbf(gs), uu = rbf(us);
      const float z = 0.7978845608028654f * (gg + 0.044715f * gg * gg * gg);
      const float th = 1.f - 2.f / (__builtin_amdgcn_exp2f(z * (2.f * LOG2E)) + 1.f);
      const float hv = rbf(0.5f * gg * (1.f + th)) * uu;
      af = mfma(sel(pk2(hv, hv), sl), wdr[i], af);
    }
    *(f32x4*)&lds[L_FR + w * 256 + (g >> 1) * 128 + 8 * c + 4 * (g & 1)] = af;
    asm volatile("s_waitcnt lgkmcnt(0)" ::: "memory");
    __builtin_amdgcn_s_barrier();
    FRESH();
  };
  if (w == 0) {
    float mw[8], M = -INFINITY;
#pragma unroll
    for (int ww = 0; ww < 8; ++ww) { mw[ww] = lds[L_REC + ww * 132]; M = fmaxf(M, mw[ww]); }
    f32x2 o = {0.f, 0.f}; float L = 0.f;
#pragma unroll
    for (int ww = 0; ww < 8; ++ww) {
      const float e = __builtin_amdgcn_exp2f((mw[ww] - M) * LOG2E);
      o += *(const f32x2*)&lds[L_REC + ww * 132 + 4 + 2 * lane] * e; L = fmaf(e, lds[L_REC + ww * 132 + 1], L);
    }
    const u32 o1 = WS_H1 + pid * H1_STRIDE;
    st_granule2(rws, o1 + lane * 16, o[0], o[1], tag);
    st_granule2(rws, lane == 0 ? o1 + 1024 : OOB, M, L, tag);
    float* r1 = &lds[L_R1 + j * 132];
    *(f32x2*)&r1[4 + 2 * lane] = o;
    if (lane == 0) *(f32x2*)&r1[0] = f32x2{M, L};
    SB();
    ffn2();
  } else {
    const int mj = j ^ w;
    const u32 o1 = WS_H1 + (h * 8 + mj) * H1_STRIDE;
    const u32 oml = lane == 0 ? o1 + 1024 : OOB;
    u32x4 ra = ld128<AUX_SC1>(rws, o1 + lane * 16, 0), rml = ld128<AUX_SC1>(rws, oml, 0);
    SB();
    ffn2();
    const bool have = __all(tagged(ra, tag) & ((lane != 0) | tagged(rml, tag)));
    SB();
    if (w < 4 && tid - 64 < 160) {
      const int k = tid - 64, bo = k >= 80, col = k - 80 * bo;
      float v = 0.f;
#pragma unroll
      for (int ww = 0; ww < 8; ++ww) v += lds[L_FR + ww * 256 + bo * 128 + col];
      st_granule(rws, WS_H2 + pid * H2_STRIDE + (160 + k) * 8, v, tag);
    }
    if (!have) {
      for (u32 spins = 0; spins < SPIN_CAP; ++spins) {
        __builtin_amdgcn_s_sleep(1);
        FRESH();
        rml = ld128<AUX_SC1>(rws, oml, 0);
        ra = ld128<AUX_SC1>(rws, o1 + lane * 16, 0);
        if (__all(tagged(ra, tag) & ((lane != 0) | tagged(rml, tag)))) break;
      }
    }
    float* r1 = &lds[L_R1 + mj * 132];
    *(u32x2*)&r1[4 + 2 * lane] = u32x2{ra[0], ra[2]};
    if (lane == 0) *(u32x2*)&r1[0] = u32x2{rml[0], rml[2]};
  }
  __syncthreads();
  {
    const float* r1 = &lds[L_R1 + bb * 4 * 132];
    const float m0 = r1[0], m1 = r1[132], m2 = r1[264], m3 = r1[396];
    const float M = fmaxf(fmaxf(m0, m1), fmaxf(m2, m3));
    const float e0 = __builtin_amdgcn_exp2f((m0 - M) * LOG2E), e1 = __builtin_amdgcn_exp2f((m1 - M) * LOG2E), e2 = __builtin_amdgcn_exp2f((m2 - M) * LOG2E), e3 = __builtin_amdgcn_exp2f((m3 - M) * LOG2E);
    const float L = e0 * r1[1] + e1 * r1[133] + e2 * r1[265] + e3 * r1[397];
    const int d0 = 4 + 16 * w + 4 * g;
    f32x4 o = *(const f32x4*)&r1[d0] * e0 + *(const f32x4*)&r1[132 + d0] * e1 + *(const f32x4*)&r1[264 + d0] * e2 + *(const f32x4*)&r1[396 + d0] * e3;
    o *= 1.f / L;
    const f32x4 z4 = {0.f, 0.f, 0.f, 0.f};
    f32x4 as = mfma(sel(pk2(o[0], o[0]), sl), wor[0], z4), as2 = mfma(sel(pk2(o[1], o[1]), sl), wor[1], z4);
    as = mfma(sel(pk2(o[2], o[2]), sl), wor[2], as); as2 = mfma(sel(pk2(o[3], o[3]), sl), wor[3], as2);
    *(f32x4*)&lds[L_SR + w * 256 + (g >> 1) * 128 + 8 * c + 4 * (g & 1)] = as + as2;
  }
  __syncthreads();
  if (tid < 160) {
    const int bo = tid >= 80, col = tid - 80 * bo;
    float v = 0.f;
#pragma unroll
    for (int ww = 0; ww < 8; ++ww) v += lds[L_SR + ww * 256 + bo * 128 + col];
    st_granule(rws, WS_H2 + pid * H2_STRIDE + tid * 8, v, tag);
  }
  if (h < 5) {
    const u32 o2 = WS_H2 + ((4 * w + g) * 8 + j) * H2_STRIDE + fk0 * 8;
    u32x4 aS = {0u, 0u, 0u, 0u}, aF = aS;
    bool nS = true, nF = true;
    for (u32 spins = 0; spins < SPIN_CAP; ++spins) {
      FRESH();
      if (nS) aS = ld128<AUX_SC1>(rws, o2, 0);
      if (nF) aF = ld128<AUX_SC1>(rws, o2 + 1280, 0);
      nS = !tagged(aS, tag); nF = !tagged(aF, tag);
      if (!__any(nS || nF)) break;
      __builtin_amdgcn_s_sleep(1);
    }
    const f32x4 part = {xrow_sum(__uint_as_float(aS[0])), xrow_sum(__uint_as_float(aS[2])), xrow_sum(__uint_as_float(aF[0])), xrow_sum(__uint_as_float(aF[2]))};
    if (g == 0) *(f32x4*)&lds[L_FIN + w * 64 + 4 * c] = part;
    __syncthreads();
    if (w == 7) {
      f32x4 t = {0.f, 0.f, 0.f, 0.f};
#pragma unroll
      for (int ww = 0; ww < 8; ++ww) t += *(const f32x4*)&lds[L_FIN + ww * 64 + 4 * c];
      const float s0 = rbf(t[0]), s1 = rbf(t[1]), f0v = rbf(t[2]), f1v = rbf(t[3]);
      const float g0 = rbf(f0v / (1.f + __builtin_amdgcn_exp2f(-f0v * LOG2E))), g1 = rbf(f1v / (1.f + __builtin_amdgcn_exp2f(-f1v * LOG2E)));
      if (g == 0) *(u32*)(p.out + fbo * D + fcol) = pk2(lo16(xraw) + rbf(s0 * g0), hi16(xraw) + rbf(s1 * g1));
      if (bid == 0 && lane == 0) __builtin_amdgcn_raw_buffer_store_b32(tag, rws, WS_EPOCH, 0, AUX_SC1);
    }
  }
}
}

extern "C" void kernel_launch(void* const* d_in, const int* in_sizes, int n_in, void* d_out, int out_size, void* d_ws, size_t ws_size, hipStream_t stream) {
  Params p;
  p.x = (const u16*)d_in[0]; p.kc = (const u16*)d_in[1]; p.vc = (const u16*)d_in[2]; p.wq = (const u16*)d_in[3]; p.wo = (const u16*)d_in[4];
  p.wg = (const u16*)d_in[6]; p.wu = (const u16*)d_in[7]; p.wd = (const u16*)d_in[8];
  p.out = (u16*)d_out; p.ws = (unsigned char*)d_ws;
  fused_decode<<<NWG, NT, 0, stream>>>(p);
}
```
